```python
import torch

def _softmax(x, axis=-1):
    return torch.softmax(x, dim=axis)
def _gelu(x):
    return torch.nn.functional.gelu(x, approximate="tanh")
def _silu(x):
    return torch.nn.functional.silu(x)
def _sigmoid(x):
    return torch.sigmoid(x)
def _layernorm(x, axis=-1, eps=1e-5):
    m = x.mean(dim=axis, keepdim=True)
    v = x.var(dim=axis, keepdim=True, unbiased=False)
    return (x - m) / torch.sqrt(v + eps)
def _scatter_add(tgt, idx, src, axis=0):
    return tgt.clone().index_add_(axis, idx, src)
def _segment_sum(src, seg, n):
    out = torch.zeros((n,) + src.shape[1:], dtype=src.dtype)
    return out.index_add_(0, seg, src)
def _pad(x, pw):
    flat = tuple(v for pair in reversed(pw) for v in pair)
    return torch.nn.functional.pad(x, flat)

kernel_name = 'gated_mix__grouped_kv_mqa__sliding_local_dilated__prompt'

def setup_inputs():

    _w = (lambda a: (lambda t: t.to(torch.bfloat16) if t.is_floating_point() else t)(torch.as_tensor(a)))
    def _A_setup():
        B, S, D, H = 1, 2048, 448, 16
        Dh = D // H
        return {
            "x": _w((lambda *s: torch.randn(*s, dtype=torch.bfloat16) * 0.05)(B, S, D)),
            "wq": _w((lambda *s: torch.randn(*s, dtype=torch.bfloat16) * 1.6)(D, H * Dh)),
            "wk": _w((lambda *s: torch.randn(*s, dtype=torch.bfloat16) * 1.6)(D, Dh)),
            "wv": _w((lambda *s: torch.randn(*s, dtype=torch.bfloat16) * 0.2)(D, Dh)),
            "wo": _w((lambda *s: torch.randn(*s, dtype=torch.bfloat16) * 0.2)(H * Dh, D)),
        }
    _A_in = _A_setup()

    def _B_setup():
        B, S, D = 1, 2048, 448
        return {
            "x": _w((lambda *s: torch.randn(*s, dtype=torch.bfloat16) * 0.05)(B, S, D)),
            "wqkv": _w((lambda *s: torch.randn(*s, dtype=torch.bfloat16) * 0.8)(D, 3 * D)),
            "wo": _w((lambda *s: torch.randn(*s, dtype=torch.bfloat16) * 0.05)(D, D)),
        }
    _B_in = _B_setup()

    return {
        "A_x": _A_in["x"],
        "A_wq": _A_in["wq"],
        "A_wk": _A_in["wk"],
        "A_wv": _A_in["wv"],
        "A_wo": _A_in["wo"],
        "B_x": _B_in["x"],
        "B_wqkv": _B_in["wqkv"],
        "B_wo": _B_in["wo"],
        "gate_w": _w((lambda *s: torch.randn(*s, dtype=torch.bfloat16) * 0.05)(448) * 16.0),
    }

def reference(A_x, A_wq, A_wk, A_wv, A_wo, B_x, B_wqkv, B_wo, gate_w):
    def _A_ref(x, wq, wk, wv, wo):
        B, S, D = x.shape
        H = 16
        Dh = D // H
        q = (lambda x, *d: x.permute(*d))((x @ wq).reshape(B, S, H, Dh), 0, 2, 1, 3)
        k = x @ wk
        v = x @ wv
        scores = torch.einsum("bhqd,bkd->bhqk", q, k) * (Dh ** -0.5)
        p = _softmax(scores, axis=-1)
        o = torch.einsum("bhqk,bkd->bhqd", p, v)
        return (lambda x, *d: x.permute(*d))(o, 0, 2, 1, 3).reshape(B, S, H * Dh) @ wo

    def _B_ref(x, wqkv, wo):
        B, S, D = x.shape
        H = 16; Dh = D // H
        W, dil = 8, 2
        qkv = (lambda x, *d: x.permute(*d))((x @ wqkv).reshape(B, S, 3, H, Dh), 2, 0, 3, 1, 4)
        q, k, v = qkv[0], qkv[1], qkv[2]
        scale = Dh ** -0.5
        pos = torch.arange(S)
        off = torch.arange(W) * dil
        idx = pos[:, None] - off[None, :]
        mask = idx >= 0
        idx_c = torch.where(mask, idx, 0).reshape(-1)
        kg = k[:, :, idx_c, :].reshape(B, H, S, W, Dh)
        vg = v[:, :, idx_c, :].reshape(B, H, S, W, Dh)
        logits = torch.einsum("bhsd,bhswd->bhsw", q, kg) * scale
        logits = torch.where(mask[None, None, :, :], logits, -1e9)
        attn = _softmax(logits, axis=-1)
        o = torch.einsum("bhsw,bhswd->bhsd", attn, vg)
        return (lambda x, *d: x.permute(*d))(o, 0, 2, 1, 3).reshape(B, S, D) @ wo

    x = A_x
    g = _sigmoid((x * gate_w).sum(-1, keepdims=True))
    a = _A_ref(x, A_wq, A_wk, A_wv, A_wo)
    b = _B_ref(x, B_wqkv, B_wo)
    return g * a + (1 - g) * b
```

```cpp
#include <hip/hip_runtime.h>
#include <hip/hip_bf16.h>
#include <stdint.h>


typedef __attribute__((ext_vector_type(8))) short bf16x8;
typedef __attribute__((ext_vector_type(4))) short s16x4;
typedef __attribute__((ext_vector_type(16))) float f32x16;
typedef __attribute__((ext_vector_type(2))) __bf16 bf2;
typedef __attribute__((address_space(3))) s16x4* lds_s16x4_ptr;
typedef unsigned short u16;

#define DI __device__ __forceinline__
#define MFMA32(a, b, c) __builtin_amdgcn_mfma_f32_32x32x16_bf16((a), (b), (c), 0, 0, 0)

constexpr int S_LEN = 2048;
constexpr int DM = 448;
constexpr int NH = 16;
constexpr int DH = 28;
constexpr int NB3 = 1344;
constexpr int OC = 896;

DI unsigned pack_bf16(float a, float b) {
  bf2 v; v[0] = (__bf16)a; v[1] = (__bf16)b;
  return __builtin_bit_cast(unsigned, v);
}
DI u16 to_bf16(float a) { __bf16 v = (__bf16)a; return __builtin_bit_cast(u16, v); }
DI float bf_to_f(u16 a) { return __uint_as_float(((unsigned)a) << 16); }

constexpr int BK = 64;
constexpr int A_STRIDE = 72;

template <int BM, int BN>
struct GemmSmem {
  static constexpr int B_STRIDE = BN + 32;
  static constexpr int A_ELEMS = BM * A_STRIDE;
  static constexpr int B_ELEMS = BK * B_STRIDE;
  static constexpr int BUF_ELEMS = A_ELEMS + B_ELEMS;
  static constexpr int BYTES = 2 * BUF_ELEMS * 2;
};

template <int BM, int BN, class BLoad, class Epi>
DI void gemm_core(const u16* __restrict__ A, int lda, int m0, int n0, int K, u16* smem, BLoad bload, Epi epi) {
  using SM = GemmSmem<BM, BN>;
  constexpr int TM = BM / 64;
  constexpr int TN = BN / 64;
  constexpr int ACH = BM / 32;
  constexpr int BCH = BN / 32;
  const int tid = threadIdx.x, lane = tid & 63, wave = tid >> 6;
  const int wm = wave >> 1, wn = wave & 1;
  const int r = lane & 31, h = lane >> 5;

  f32x16 acc[TM][TN];
#pragma unroll
  for (int i = 0; i < TM; ++i)
#pragma unroll
    for (int j = 0; j < TN; ++j)
#pragma unroll
      for (int e = 0; e < 16; ++e) acc[i][j][e] = 0.f;

  uint4 ra[ACH], rb[BCH];
  auto load_regs = [&](int k0) {
#pragma unroll
    for (int i = 0; i < ACH; ++i) {
      int c = tid + i * 256;
      ra[i] = *(const uint4*)(A + (size_t)(m0 + (c >> 3)) * lda + k0 + (c & 7) * 8);
    }
#pragma unroll
    for (int i = 0; i < BCH; ++i) {
      int c = tid + i * 256;
      int kr = c / (BN / 8), nc = c % (BN / 8);
      rb[i] = bload(k0 + kr, n0 + nc * 8);
    }
  };
  auto store_lds = [&](int buf) {
    u16* as = smem + buf * SM::BUF_ELEMS;
    u16* bs = as + SM::A_ELEMS;
#pragma unroll
    for (int i = 0; i < ACH; ++i) {
      int c = tid + i * 256;
      *(uint4*)(as + (c >> 3) * A_STRIDE + (c & 7) * 8) = ra[i];
    }
#pragma unroll
    for (int i = 0; i < BCH; ++i) {
      int c = tid + i * 256;
      int kr = c / (BN / 8), nc = c % (BN / 8);
      *(uint4*)(bs + kr * SM::B_STRIDE + nc * 8) = rb[i];
    }
  };

  const int nsteps = K / BK;
  load_regs(0);
  store_lds(0);
  const int trq = (lane & 15) >> 2, trp = lane & 3, trblk = (lane >> 4) & 1;
  for (int st = 0; st < nsteps; ++st) {
    __syncthreads();
    if (st + 1 < nsteps) load_regs((st + 1) * BK);
    const u16* as = smem + (st & 1) * SM::BUF_ELEMS;
    const u16* bs = as + SM::A_ELEMS;
#pragma unroll
    for (int ks = 0; ks < BK / 16; ++ks) {
      bf16x8 af[TM], bfr[TN];
#pragma unroll
      for (int i = 0; i < TM; ++i)
        af[i] = *(const bf16x8*)(as + (wm * (BM / 2) + i * 32 + r) * A_STRIDE + ks * 16 + h * 8);
#pragma unroll
      for (int j = 0; j < TN; ++j) {
        const u16* p = bs + (ks * 16 + 8 * h + trq) * SM::B_STRIDE + wn * (BN / 2) + j * 32 + 16 * trblk + 4 * trp;
        s16x4 lo = __builtin_amdgcn_ds_read_tr16_b64_v4i16((lds_s16x4_ptr)(p));
        s16x4 hi = __builtin_amdgcn_ds_read_tr16_b64_v4i16((lds_s16x4_ptr)(p + 4 * SM::B_STRIDE));
        bfr[j] = __builtin_shufflevector(lo, hi, 0, 1, 2, 3, 4, 5, 6, 7);
      }
#pragma unroll
      for (int i = 0; i < TM; ++i)
#pragma unroll
        for (int j = 0; j < TN; ++j) acc[i][j] = MFMA32(af[i], bfr[j], acc[i][j]);
    }
    if (st + 1 < nsteps) store_lds((st + 1) & 1);
  }
#pragma unroll
  for (int i = 0; i < TM; ++i)
#pragma unroll
    for (int j = 0; j < TN; ++j)
      epi(m0 + wm * (BM / 2) + i * 32, n0 + wn * (BN / 2) + j * 32, acc[i][j], r, h);
}

constexpr int P_BM = 128, P_BN = 128;
constexpr int P_NT = 15;

__global__ __launch_bounds__(256) void k_proj(const u16* __restrict__ x, const u16* __restrict__ wq,
                                              const u16* __restrict__ wk, const u16* __restrict__ wv,
                                              const u16* __restrict__ wqkv, const u16* __restrict__ gw,
                                              u16* __restrict__ Qa, u16* __restrict__ KV,
                                              u16* __restrict__ QKVb, float* __restrict__ gate) {
  __shared__ __attribute__((aligned(16))) u16 smem[GemmSmem<P_BM, P_BN>::BYTES / 2];
  const int n0 = blockIdx.x * P_BN, m0 = blockIdx.y * P_BM;
  auto bload = [&](int k, int n) -> uint4 {
    uint4 v = make_uint4(0, 0, 0, 0);
    if (n < 448) {
      v = *(const uint4*)(wq + k * DM + n);
    } else if (n < 512) {
      const u16* w = (n < 480) ? wk : wv;
      int c = (n < 480) ? (n - 448) : (n - 480);
      u16 t[8];
#pragma unroll
      for (int i = 0; i < 8; ++i) t[i] = (c + i < DH) ? w[k * DH + c + i] : (u16)0;
      v.x = t[0] | ((unsigned)t[1] << 16); v.y = t[2] | ((unsigned)t[3] << 16);
      v.z = t[4] | ((unsigned)t[5] << 16); v.w = t[6] | ((unsigned)t[7] << 16);
    } else if (n < 1856) {
      v = *(const uint4*)(wqkv + k * NB3 + (n - 512));
    } else if (n == 1856) {
      v.x = gw[k];
    }
    return v;
  };
  const float qscale = 0.18898223650461362f * 1.4426950408889634f;
  auto epi = [&](int mb, int nb, const f32x16& a, int r, int h) {
    const int n = nb + r;
    if (nb < 448) {
#pragma unroll
      for (int e = 0; e < 16; ++e) {
        int m = mb + (e & 3) + 8 * (e >> 2) + 4 * h;
        Qa[m * DM + n] = to_bf16(a[e] * qscale);
      }
    } else if (nb < 480) {
      const int d = n - 448;
      const int s = d >> 4, hh = (d >> 3) & 1, j = d & 7;
#pragma unroll
      for (int e = 0; e < 16; ++e) {
        int m = mb + (e & 3) + 8 * (e >> 2) + 4 * h;
        int tile = m >> 5, rr = m & 31;
        KV[tile * 2048 + s * 512 + (hh * 32 + rr) * 8 + j] = (d < DH) ? to_bf16(a[e]) : (u16)0;
      }
    } else if (nb < 512) {
      const int d = n - 480;
#pragma unroll
      for (int e = 0; e < 16; ++e) {
        int m = mb + (e & 3) + 8 * (e >> 2) + 4 * h;
        int tile = m >> 5, kk = m & 31;
        int s = kk >> 4, k16 = kk & 15;
        int hh = (k16 >> 2) & 1, j = ((k16 >> 3) << 2) | (k16 & 3);
        u16 val = (d < DH) ? to_bf16(a[e]) : ((d == DH) ? (u16)0x3F80 : (u16)0);
        KV[tile * 2048 + 1024 + s * 512 + (hh * 32 + d) * 8 + j] = val;
      }
    } else if (nb < 1856) {
#pragma unroll
      for (int e = 0; e < 16; ++e) {
        int m = mb + (e & 3) + 8 * (e >> 2) + 4 * h;
        QKVb[m * NB3 + (n - 512)] = to_bf16(a[e]);
      }
    } else if (nb == 1856) {
      if (r == 0) {
#pragma unroll
        for (int e = 0; e < 16; ++e) {
          int m = mb + (e & 3) + 8 * (e >> 2) + 4 * h;
          gate[m] = 1.f / (1.f + __expf(-a[e]));
        }
      }
    }
  };
  gemm_core<P_BM, P_BN>(x, DM, m0, n0, DM, smem, bload, epi);
}

constexpr int O_BM = 64, O_BN = 64;

__global__ __launch_bounds__(256) void k_out(const u16* __restrict__ Ocat, const u16* __restrict__ woA,
                                             const u16* __restrict__ woB, u16* __restrict__ out) {
  __shared__ __attribute__((aligned(16))) u16 smem[GemmSmem<O_BM, O_BN>::BYTES / 2];
  const int n0 = blockIdx.x * O_BN, m0 = blockIdx.y * O_BM;
  auto bload = [&](int k, int n) -> uint4 {
    const u16* w = (k < DM) ? (woA + k * DM) : (woB + (k - DM) * DM);
    return *(const uint4*)(w + n);
  };
  auto epi = [&](int mb, int nb, const f32x16& a, int r, int h) {
    const int n = nb + r;
#pragma unroll
    for (int e = 0; e < 16; ++e) {
      int m = mb + (e & 3) + 8 * (e >> 2) + 4 * h;
      out[m * DM + n] = to_bf16(a[e]);
    }
  };
  gemm_core<O_BM, O_BN>(Ocat, OC, m0, n0, OC, smem, bload, epi);
}

constexpr int TPC = 4;
constexpr int NCH = 16 / TPC;

__global__ __launch_bounds__(1024) void k_attn(const u16* __restrict__ Qa, const u16* __restrict__ KV,
                                               const u16* __restrict__ QKVb, const float* __restrict__ gate,
                                               u16* __restrict__ Ocat) {
  __shared__ __attribute__((aligned(16))) uint4 lds[2 * 4 * TPC * 256];
  const int tid = threadIdx.x, lane = tid & 63, wave = tid >> 6;
  const int rb = wave & 3, kq = wave >> 2;
  const int r = lane & 31, h = lane >> 5;
  const int p0 = blockIdx.x * 8;
  const uint4* KVg = (const uint4*)KV;

  bf16x8 q0, q1;
  {
    const u16* qp = Qa + p0 * DM + (rb * 32 + r) * DH;
    uint2 a = *(const uint2*)(qp + 8 * h), b = *(const uint2*)(qp + 8 * h + 4);
    uint2 c = *(const uint2*)(qp + 16 + 8 * h);
    uint2 d = make_uint2(0, 0);
    if (h == 0) d = *(const uint2*)(qp + 20);
    uint4 t0 = make_uint4(a.x, a.y, b.x, b.y), t1 = make_uint4(c.x, c.y, d.x, d.y);
    q0 = __builtin_bit_cast(bf16x8, t0);
    q1 = __builtin_bit_cast(bf16x8, t1);
  }

  {
    const int pair = tid >> 3, j = tid & 7;
    const int pos = p0 + (pair >> 4), head = pair & 15;
    const int kp = pos - 2 * j;
    float lg = -1e30f;
    if (kp >= 0) {
      const u16* qp = QKVb + pos * NB3 + head * DH;
      const u16* kpp = QKVb + kp * NB3 + DM + head * DH;
      float acc = 0.f;
#pragma unroll
      for (int i = 0; i < 7; ++i) {
        uint2 a = *(const uint2*)(qp + 4 * i), b = *(const uint2*)(kpp + 4 * i);
        acc += __uint_as_float(a.x << 16) * __uint_as_float(b.x << 16);
        acc += __uint_as_float(a.x & 0xffff0000u) * __uint_as_float(b.x & 0xffff0000u);
        acc += __uint_as_float(a.y << 16) * __uint_as_float(b.y << 16);
        acc += __uint_as_float(a.y & 0xffff0000u) * __uint_as_float(b.y & 0xffff0000u);
      }
      lg = acc * 0.18898223650461362f;
    }
    float mx = lg;
    mx = fmaxf(mx, __shfl_xor(mx, 1)); mx = fmaxf(mx, __shfl_xor(mx, 2)); mx = fmaxf(mx, __shfl_xor(mx, 4));
    float e = (kp >= 0) ? __expf(lg - mx) : 0.f;
    float sm = e;
    sm += __shfl_xor(sm, 1); sm += __shfl_xor(sm, 2); sm += __shfl_xor(sm, 4);
    float a = e / sm;
    float o[4] = {0.f, 0.f, 0.f, 0.f};
#pragma unroll
    for (int w = 0; w < 8; ++w) {
      float aw = __shfl(a, (lane & 56) + w);
      int kw = pos - 2 * w;
      if (kw >= 0) {
        const u16* vp = QKVb + kw * NB3 + 2 * DM + head * DH;
#pragma unroll
        for (int i = 0; i < 4; ++i) {
          int d = j + 8 * i;
          if (d < DH) o[i] += aw * bf_to_f(vp[d]);
        }
      }
    }
    const float gb = 1.f - gate[pos];
#pragma unroll
    for (int i = 0; i < 4; ++i) {
      int d = j + 8 * i;
      if (d < DH) Ocat[pos * OC + DM + head * DH + d] = to_bf16(gb * o[i]);
    }
  }

  float mref;
  {
    bf16x8 k0 = __builtin_bit_cast(bf16x8, KVg[lane]);
    bf16x8 k1 = __builtin_bit_cast(bf16x8, KVg[64 + lane]);
    f32x16 z;
#pragma unroll
    for (int e = 0; e < 16; ++e) z[e] = 0.f;
    f32x16 s = MFMA32(k0, q0, z);
    s = MFMA32(k1, q1, s);
    float m = s[0];
#pragma unroll
    for (int e = 1; e < 16; ++e) m = fmaxf(m, s[e]);
    mref = fmaxf(m, __shfl_xor(m, 32));
  }

  f32x16 o;
  for (int attempt = 0; attempt < 2; ++attempt) {
    f32x16 negm;
#pragma unroll
    for (int e = 0; e < 16; ++e) { negm[e] = -mref; o[e] = 0.f; }

    uint4 stg[4];
    {
      const uint4* src = KVg + (kq * 16 + rb) * 256 + lane;
#pragma unroll
      for (int p = 0; p < 4; ++p) stg[p] = src[p * 64];
      uint4* dst = lds + ((0 * 4 + kq) * TPC + rb) * 256 + lane;
#pragma unroll
      for (int p = 0; p < 4; ++p) dst[p * 64] = stg[p];
    }
    for (int c = 0; c < NCH; ++c) {
      __syncthreads();
      if (c + 1 < NCH) {
        const uint4* src = KVg + (kq * 16 + (c + 1) * TPC + rb) * 256 + lane;
#pragma unroll
        for (int p = 0; p < 4; ++p) stg[p] = src[p * 64];
      }
      const uint4* base = lds + (((c & 1) * 4 + kq) * TPC) * 256 + lane;
#pragma unroll 2
      for (int t = 0; t < TPC; ++t) {
        bf16x8 kf0 = __builtin_bit_cast(bf16x8, base[t * 256]);
        bf16x8 kf1 = __builtin_bit_cast(bf16x8, base[t * 256 + 64]);
        bf16x8 vf0 = __builtin_bit_cast(bf16x8, base[t * 256 + 128]);
        bf16x8 vf1 = __builtin_bit_cast(bf16x8, base[t * 256 + 192]);
        f32x16 s = MFMA32(kf0, q0, negm);
        s = MFMA32(kf1, q1, s);
        uint4 pa, pb;
        pa.x = pack_bf16(__builtin_amdgcn_exp2f(s[0]), __builtin_amdgcn_exp2f(s[1]));
        pa.y = pack_bf16(__builtin_amdgcn_exp2f(s[2]), __builtin_amdgcn_exp2f(s[3]));
        pa.z = pack_bf16(__builtin_amdgcn_exp2f(s[4]), __builtin_amdgcn_exp2f(s[5]));
        pa.w = pack_bf16(__builtin_amdgcn_exp2f(s[6]), __builtin_amdgcn_exp2f(s[7]));
        pb.x = pack_bf16(__builtin_amdgcn_exp2f(s[8]), __builtin_amdgcn_exp2f(s[9]));
        pb.y = pack_bf16(__builtin_amdgcn_exp2f(s[10]), __builtin_amdgcn_exp2f(s[11]));
        pb.z = pack_bf16(__builtin_amdgcn_exp2f(s[12]), __builtin_amdgcn_exp2f(s[13]));
        pb.w = pack_bf16(__builtin_amdgcn_exp2f(s[14]), __builtin_amdgcn_exp2f(s[15]));
        o = MFMA32(vf0, __builtin_bit_cast(bf16x8, pa), o);
        o = MFMA32(vf1, __builtin_bit_cast(bf16x8, pb), o);
      }
      if (c + 1 < NCH) {
        uint4* dst = lds + ((((c + 1) & 1) * 4 + kq) * TPC + rb) * 256 + lane;
#pragma unroll
        for (int p = 0; p < 4; ++p) dst[p * 64] = stg[p];
      }
    }
    __syncthreads();
    float* red = (float*)lds;
#pragma unroll
    for (int e = 0; e < 16; ++e) red[(wave * 16 + e) * 64 + lane] = o[e];
    __syncthreads();
    float l = 0.f;
#pragma unroll
    for (int q = 0; q < 4; ++q) l += red[((q * 4 + rb) * 16 + 12) * 64 + 32 + r];
    float ov[4];
#pragma unroll
    for (int i = 0; i < 4; ++i) {
      float t = 0.f;
#pragma unroll
      for (int q = 0; q < 4; ++q) t += red[((q * 4 + rb) * 16 + 4 * kq + i) * 64 + lane];
      ov[i] = t;
    }
    const bool bad = !(l < 1.0e18f);
    const int anybad = __syncthreads_or(bad && attempt == 0);
    if (!anybad) {
      const int R = rb * 32 + r;
      const int pos = p0 + (R >> 4), head = R & 15;
      const int d = 8 * kq + 4 * h;
      if (d < DH) {
        const float sc = gate[pos] / l;
        uint2 w;
        w.x = pack_bf16(ov[0] * sc, ov[1] * sc);
        w.y = pack_bf16(ov[2] * sc, ov[3] * sc);
        *(uint2*)(Ocat + pos * OC + head * DH + d) = w;
      }
      break;
    }
    {
      f32x16 z;
#pragma unroll
      for (int e = 0; e < 16; ++e) z[e] = 0.f;
      float m = -3.0e38f;
      for (int t = 0; t < 16; ++t) {
        const uint4* src = KVg + (kq * 16 + t) * 256 + lane;
        bf16x8 k0 = __builtin_bit_cast(bf16x8, src[0]);
        bf16x8 k1 = __builtin_bit_cast(bf16x8, src[64]);
        f32x16 s = MFMA32(k0, q0, z);
        s = MFMA32(k1, q1, s);
#pragma unroll
        for (int e = 0; e < 16; ++e) m = fmaxf(m, s[e]);
      }
      m = fmaxf(m, __shfl_xor(m, 32));
      __syncthreads();
      red[wave * 64 + lane] = m;
      __syncthreads();
      float mm = red[(0 * 4 + rb) * 64 + lane];
#pragma unroll
      for (int q = 1; q < 4; ++q) mm = fmaxf(mm, red[(q * 4 + rb) * 64 + lane]);
      mref = mm;
      __syncthreads();
    }
  }
}

extern "C" void kernel_launch(void* const* d_in, const int* in_sizes, int n_in,
                              void* d_out, int out_size, void* d_ws, size_t ws_size,
                              hipStream_t stream) {
  const u16* x = (const u16*)d_in[0];
  const u16* wq = (const u16*)d_in[1];
  const u16* wk = (const u16*)d_in[2];
  const u16* wv = (const u16*)d_in[3];
  const u16* woA = (const u16*)d_in[4];
  const u16* wqkv = (const u16*)d_in[6];
  const u16* woB = (const u16*)d_in[7];
  const u16* gw = (const u16*)d_in[8];
  char* ws = (char*)d_ws;
  u16* Qa = (u16*)(ws);
  u16* KV = (u16*)(ws + (2u << 20));
  u16* QKVb = (u16*)(ws + (3u << 20));
  float* gate = (float*)(ws + (9u << 20));
  u16* Ocat = (u16*)(ws + (10u << 20));
  u16* out = (u16*)d_out;

  k_proj<<<dim3(P_NT, S_LEN / P_BM), 256, 0, stream>>>(x, wq, wk, wv, wqkv, gw, Qa, KV, QKVb, gate);
  k_attn<<<dim3(S_LEN / 8), 1024, 0, stream>>>(Qa, KV, QKVb, gate, Ocat);
  k_out<<<dim3(DM / O_BN, S_LEN / O_BM), 256, 0, stream>>>(Ocat, woA, woB, out);
}
```

```cpp
#include <hip/hip_runtime.h>
#include <hip/hip_bf16.h>
#include <stdint.h>


typedef __attribute__((ext_vector_type(8))) short bf16x8;
typedef __attribute__((ext_vector_type(4))) short s16x4;
typedef __attribute__((ext_vector_type(16))) float f32x16;
typedef __attribute__((ext_vector_type(2))) __bf16 bf2;
typedef __attribute__((address_space(3))) s16x4* lds_s16x4_ptr;
typedef unsigned short u16;

#define DI __device__ __forceinline__
#define MFMA32(a, b, c) __builtin_amdgcn_mfma_f32_32x32x16_bf16((a), (b), (c), 0, 0, 0)

constexpr int S_LEN = 2048;
constexpr int DM = 448;
constexpr int NH = 16;
constexpr int DH = 28;
constexpr int NB3 = 1344;
constexpr int OC = 896;

DI unsigned pack_bf16(float a, float b) {
  bf2 v; v[0] = (__bf16)a; v[1] = (__bf16)b;
  return __builtin_bit_cast(unsigned, v);
}
DI u16 to_bf16(float a) { __bf16 v = (__bf16)a; return __builtin_bit_cast(u16, v); }
DI float bf_to_f(u16 a) { return __uint_as_float(((unsigned)a) << 16); }

template <int V> struct IC { static constexpr int value = V; };
template <int I, int N, class F> DI void static_for(F&& f) {
  if constexpr (I < N) { f(IC<I>{}); static_for<I + 1, N>(f); }
}
template <int N> DI void wait_vm() { asm volatile("s_waitcnt vmcnt(%0)" ::"n"(N) : "memory"); }
DI void raw_barrier() { asm volatile("s_waitcnt lgkmcnt(0)" ::: "memory"); __builtin_amdgcn_s_barrier(); asm volatile("" ::: "memory"); }
DI void glds16(const void* g, void* l) {
  __builtin_amdgcn_global_load_lds((const __attribute__((address_space(1))) unsigned*)g,
                                   (__attribute__((address_space(3))) unsigned*)l, 16, 0, 0);
}

template <int BN> DI int bswz(int row) { return (BN == 128) ? ((row & 3) << 2) : (((row >> 1) & 1) << 2); }

template <int BM, int BN, int NS, int NBUF, class BPtr, class Epi>
DI void gemm_glds(const u16* __restrict__ A, int lda, int m0, char* smem, BPtr bptr, Epi epi) {
  constexpr int TM = BM / 64, TN = BN / 64;
  constexpr int GA = BM / 32, GB = BN / 32, G = GA + GB;
  constexpr int A_BYTES = BM * 128, B_BYTES = BN * 128, ST_BYTES = A_BYTES + B_BYTES;
  constexpr int CPR = BN / 8;
  const int tid = threadIdx.x, lane = tid & 63, wave = __builtin_amdgcn_readfirstlane(tid >> 6);
  const int wm = wave >> 1, wn = wave & 1;
  const int r = lane & 31, h = lane >> 5;

  const u16* asrc[GA];
#pragma unroll
  for (int i = 0; i < GA; ++i) {
    int idx = (wave * GA + i) * 64 + lane, row = idx >> 3, ch = (idx & 7) ^ ((row >> 1) & 7);
    asrc[i] = A + (size_t)(m0 + row) * lda + ch * 8;
  }
  int brow[GB], bch[GB];
#pragma unroll
  for (int i = 0; i < GB; ++i) {
    int idx = (wave * GB + i) * 64 + lane;
    brow[i] = idx / CPR;
    bch[i] = (idx % CPR) ^ bswz<BN>(brow[i]);
  }
  auto issue = [&](int st) {
    char* base = smem + (st % NBUF) * ST_BYTES;
#pragma unroll
    for (int i = 0; i < GA; ++i) glds16(asrc[i] + st * 64, base + (wave * GA + i) * 1024);
#pragma unroll
    for (int i = 0; i < GB; ++i) glds16(bptr(st * 64 + brow[i], bch[i] * 8), base + A_BYTES + (wave * GB + i) * 1024);
  };

  f32x16 acc[TM][TN];
#pragma unroll
  for (int i = 0; i < TM; ++i)
#pragma unroll
    for (int j = 0; j < TN; ++j)
#pragma unroll
      for (int e = 0; e < 16; ++e) acc[i][j][e] = 0.f;

  int aoff[TM], asw[TM];
#pragma unroll
  for (int i = 0; i < TM; ++i) {
    int row = wm * (BM / 2) + i * 32 + r;
    aoff[i] = row * 128; asw[i] = (row >> 1) & 7;
  }
  const int trq = (lane & 15) >> 2, trp = lane & 3, trblk = (lane >> 4) & 1;

#pragma unroll
  for (int st = 0; st < NBUF - 1; ++st) if (st < NS) issue(st);

  static_for<0, NS>([&](auto SI) {
    constexpr int st = decltype(SI)::value;
    constexpr int issued = (st + NBUF - 1 < NS) ? (st + NBUF - 1) : NS;
    wait_vm<G * (issued - st - 1)>();
    raw_barrier();
    if constexpr (st + NBUF - 1 < NS) issue(st + NBUF - 1);
    const char* as = smem + (st % NBUF) * ST_BYTES;
    const char* bs = as + A_BYTES;
#pragma unroll
    for (int ks = 0; ks < 4; ++ks) {
      bf16x8 af[TM], bfr[TN];
#pragma unroll
      for (int i = 0; i < TM; ++i) af[i] = *(const bf16x8*)(as + aoff[i] + (((2 * ks + h) ^ asw[i]) << 4));
      const int rk = ks * 16 + 8 * h + trq;
#pragma unroll
      for (int j = 0; j < TN; ++j) {
        const int col = wn * (BN / 2) + j * 32 + 16 * trblk + 4 * trp;
        const char* p = bs + rk * (BN * 2) + (((col >> 3) ^ bswz<BN>(rk)) << 4) + ((trp & 1) << 3);
        s16x4 lo = __builtin_amdgcn_ds_read_tr16_b64_v4i16((lds_s16x4_ptr)(p));
        s16x4 hi = __builtin_amdgcn_ds_read_tr16_b64_v4i16((lds_s16x4_ptr)(p + 4 * BN * 2 + (((((col >> 3) ^ bswz<BN>(rk + 4)) - ((col >> 3) ^ bswz<BN>(rk)))) << 4)));
        bfr[j] = __builtin_shufflevector(lo, hi, 0, 1, 2, 3, 4, 5, 6, 7);
      }
#pragma unroll
      for (int i = 0; i < TM; ++i)
#pragma unroll
        for (int j = 0; j < TN; ++j) acc[i][j] = MFMA32(af[i], bfr[j], acc[i][j]);
    }
  });
#pragma unroll
  for (int i = 0; i < TM; ++i)
#pragma unroll
    for (int j = 0; j < TN; ++j)
      epi(m0 + wm * (BM / 2) + i * 32, wn * (BN / 2) + j * 32, acc[i][j], r, h);
}

constexpr int P_NBUF = 4;
constexpr int SP_AST = 456, SP_BST = 96;
constexpr int P_SMEM = (64 * SP_AST + 448 * SP_BST) * 2;

__global__ __launch_bounds__(256) void k_proj(const u16* __restrict__ x, const u16* __restrict__ wq,
                                              const u16* __restrict__ wk, const u16* __restrict__ wv,
                                              const u16* __restrict__ wqkv, const u16* __restrict__ gw,
                                              u16* __restrict__ Qa, u16* __restrict__ KV,
                                              u16* __restrict__ QKVb, float* __restrict__ gate) {
  __shared__ __attribute__((aligned(16))) char smem[P_SMEM];
  const int bid = blockIdx.x;
  if (bid < 224) {
    const int n0 = (bid % 14) * 128, m0 = (bid / 14) * 128;
    auto bptr = [&](int k, int nc) -> const u16* {
      const int n = n0 + nc;
      return (n < DM) ? (wq + k * DM + n) : (wqkv + k * NB3 + (n - DM));
    };
    const float qscale = 0.18898223650461362f * 1.4426950408889634f;
    auto epi = [&](int mb, int nbl, const f32x16& a, int r, int h) {
      const int nb = n0 + nbl, n = nb + r;
      if (nb < DM) {
#pragma unroll
        for (int e = 0; e < 16; ++e) {
          int m = mb + (e & 3) + 8 * (e >> 2) + 4 * h;
          Qa[m * DM + n] = to_bf16(a[e] * qscale);
        }
      } else {
#pragma unroll
        for (int e = 0; e < 16; ++e) {
          int m = mb + (e & 3) + 8 * (e >> 2) + 4 * h;
          QKVb[m * NB3 + (n - DM)] = to_bf16(a[e]);
        }
      }
    };
    gemm_glds<128, 128, 7, P_NBUF>(x, DM, m0, smem, bptr, epi);
    return;
  }
  const int m0 = (bid - 224) * 64;
  const int tid = threadIdx.x, lane = tid & 63, wave = tid >> 6;
  u16* as = (u16*)smem;
  u16* bs = as + 64 * SP_AST;
  uint4 ra[14]; uint2 rb0[14], rb1[14]; u16 rg[14];
#pragma unroll
  for (int i = 0; i < 14; ++i) {
    int c = tid + i * 256, row = c / 56, ch = c % 56;
    ra[i] = *(const uint4*)(x + (m0 + row) * DM + ch * 8);
  }
  const int nc = tid & 7;
  const u16* wsrc = (nc < 4) ? wk : wv;
  const int cb = (nc & 3) * 8;
#pragma unroll
  for (int i = 0; i < 14; ++i) {
    int k = (tid >> 3) + i * 32;
    rb0[i] = *(const uint2*)(wsrc + k * DH + cb);
    rb1[i] = *(const uint2*)(wsrc + k * DH + ((cb < 24) ? cb + 4 : cb));
    rg[i] = gw[k];
  }
#pragma unroll
  for (int i = 0; i < 14; ++i) {
    int c = tid + i * 256, row = c / 56, ch = c % 56;
    *(uint4*)(as + row * SP_AST + ch * 8) = ra[i];
  }
#pragma unroll
  for (int i = 0; i < 14; ++i) {
    int k = (tid >> 3) + i * 32;
    uint4 v = make_uint4(rb0[i].x, rb0[i].y, rb1[i].x, rb1[i].y);
    if (cb == 24) { v.z = (nc == 7) ? (unsigned)rg[i] : 0u; v.w = 0u; }
    *(uint4*)(bs + k * SP_BST + nc * 8) = v;
  }
  __syncthreads();
  const int wm = wave >> 1, wn = wave & 1, r = lane & 31, h = lane >> 5;
  const int trq = (lane & 15) >> 2, trp = lane & 3, trblk = (lane >> 4) & 1;
  f32x16 a;
#pragma unroll
  for (int e = 0; e < 16; ++e) a[e] = 0.f;
#pragma unroll 4
  for (int ks = 0; ks < 28; ++ks) {
    bf16x8 af = *(const bf16x8*)(as + (wm * 32 + r) * SP_AST + ks * 16 + h * 8);
    const u16* p = bs + (ks * 16 + 8 * h + trq) * SP_BST + wn * 32 + 16 * trblk + 4 * trp;
    s16x4 lo = __builtin_amdgcn_ds_read_tr16_b64_v4i16((lds_s16x4_ptr)(p));
    s16x4 hi = __builtin_amdgcn_ds_read_tr16_b64_v4i16((lds_s16x4_ptr)(p + 4 * SP_BST));
    a = MFMA32(af, __builtin_shufflevector(lo, hi, 0, 1, 2, 3, 4, 5, 6, 7), a);
  }
  const int mb = m0 + wm * 32, d = r;
  if (wn == 0) {
    const int s = d >> 4, hh = (d >> 3) & 1, j = d & 7;
#pragma unroll
    for (int e = 0; e < 16; ++e) {
      int m = mb + (e & 3) + 8 * (e >> 2) + 4 * h;
      int tile = m >> 5, rr = m & 31;
      KV[tile * 2048 + s * 512 + (hh * 32 + rr) * 8 + j] = (d < DH) ? to_bf16(a[e]) : (u16)0;
    }
  } else {
#pragma unroll
    for (int e = 0; e < 16; ++e) {
      int m = mb + (e & 3) + 8 * (e >> 2) + 4 * h;
      int tile = m >> 5, kk = m & 31;
      int s = kk >> 4, k16 = kk & 15;
      int hh = (k16 >> 2) & 1, j = ((k16 >> 3) << 2) | (k16 & 3);
      u16 val = (d < DH) ? to_bf16(a[e]) : ((d == DH) ? (u16)0x3F80 : (u16)0);
      KV[tile * 2048 + 1024 + s * 512 + (hh * 32 + d) * 8 + j] = val;
      if (d == DH) gate[m] = 1.f / (1.f + __expf(-a[e]));
    }
  }
}

constexpr int O_NBUF = 8;

__global__ __launch_bounds__(256) void k_out(const u16* __restrict__ Ocat, const u16* __restrict__ woA,
                                             const u16* __restrict__ woB, u16* __restrict__ out) {
  __shared__ __attribute__((aligned(16))) char smem[O_NBUF * 16384];
  const int n0 = (blockIdx.x % 7) * 64, m0 = (blockIdx.x / 7) * 64;
  auto bptr = [&](int k, int nc) -> const u16* {
    return ((k < DM) ? (woA + k * DM) : (woB + (k - DM) * DM)) + n0 + nc;
  };
  auto epi = [&](int mb, int nbl, const f32x16& a, int r, int h) {
    const int n = n0 + nbl + r;
#pragma unroll
    for (int e = 0; e < 16; ++e) {
      int m = mb + (e & 3) + 8 * (e >> 2) + 4 * h;
      out[m * DM + n] = to_bf16(a[e]);
    }
  };
  gemm_glds<64, 64, 14, O_NBUF>(Ocat, OC, m0, smem, bptr, epi);
}

constexpr int TPC = 4;
constexpr int NCH = 16 / TPC;

__global__ __launch_bounds__(1024) void k_attn(const u16* __restrict__ Qa, const u16* __restrict__ KV,
                                               const u16* __restrict__ QKVb, const float* __restrict__ gate,
                                               u16* __restrict__ Ocat) {
  __shared__ __attribute__((aligned(16))) uint4 lds[2 * 4 * TPC * 256 + 16];
  const int tid = threadIdx.x, lane = tid & 63, wave = __builtin_amdgcn_readfirstlane(tid >> 6);
  const int rb = wave & 3, kq = wave >> 2;
  const int r = lane & 31, h = lane >> 5;
  const int p0 = blockIdx.x * 8;
  const uint4* KVg = (const uint4*)KV;
  volatile unsigned* flag = (volatile unsigned*)(lds + 2 * 4 * TPC * 256);

  auto issue = [&](int c) {
    const uint4* src = KVg + (kq * 16 + c * TPC + rb) * 256 + lane;
    uint4* dst = lds + (((c & 1) * 4 + kq) * TPC + rb) * 256;
#pragma unroll
    for (int p = 0; p < 4; ++p) glds16(src + p * 64, dst + p * 64);
  };

  const u16* qp = Qa + p0 * DM + (rb * 32 + r) * DH;
  uint2 qa = *(const uint2*)(qp + 8 * h), qb = *(const uint2*)(qp + 8 * h + 4);
  uint2 qc = *(const uint2*)(qp + 16 + 8 * h);
  uint2 qd = *(const uint2*)(qp + 20);
  uint4 k00 = KVg[lane], k01 = KVg[64 + lane];
  const int pair = tid >> 3, bc = tid & 7, bcc = (bc < 7) ? bc : 6;
  const int bpos = p0 + (pair >> 4), bhead = pair & 15;
  const u16* bbase = QKVb + bhead * DH + 4 * bcc;
  uint2 bq = *(const uint2*)(bbase + bpos * NB3);
  uint2 bk[8], bv[8];
#pragma unroll
  for (int w = 0; w < 8; ++w) {
    int kp = bpos - 2 * w; kp = kp < 0 ? 0 : kp;
    bk[w] = *(const uint2*)(bbase + kp * NB3 + DM);
    bv[w] = *(const uint2*)(bbase + kp * NB3 + 2 * DM);
  }
  const float gB = gate[bpos];
  const float gA = gate[p0 + ((rb * 32 + r) >> 4)];
  if (tid == 0) *flag = 0u;
  issue(0);

  if (h) qd = make_uint2(0, 0);
  const bf16x8 q0 = __builtin_bit_cast(bf16x8, make_uint4(qa.x, qa.y, qb.x, qb.y));
  const bf16x8 q1 = __builtin_bit_cast(bf16x8, make_uint4(qc.x, qc.y, qd.x, qd.y));

  {
    float q4[4] = {__uint_as_float(bq.x << 16), __uint_as_float(bq.x & 0xffff0000u),
                   __uint_as_float(bq.y << 16), __uint_as_float(bq.y & 0xffff0000u)};
    float lg[8];
#pragma unroll
    for (int w = 0; w < 8; ++w) {
      float t = q4[0] * __uint_as_float(bk[w].x << 16) + q4[1] * __uint_as_float(bk[w].x & 0xffff0000u) +
                q4[2] * __uint_as_float(bk[w].y << 16) + q4[3] * __uint_as_float(bk[w].y & 0xffff0000u);
      lg[w] = (bc < 7) ? t : 0.f;
    }
#pragma unroll
    for (int w = 0; w < 8; ++w) {
      lg[w] += __shfl_xor(lg[w], 1); lg[w] += __shfl_xor(lg[w], 2); lg[w] += __shfl_xor(lg[w], 4);
    }
    float mx = -3.0e38f;
#pragma unroll
    for (int w = 0; w < 8; ++w) {
      lg[w] = (bpos - 2 * w >= 0) ? lg[w] * 0.18898223650461362f : -3.0e38f;
      mx = fmaxf(mx, lg[w]);
    }
    float sm = 0.f;
#pragma unroll
    for (int w = 0; w < 8; ++w) { lg[w] = (bpos - 2 * w >= 0) ? __expf(lg[w] - mx) : 0.f; sm += lg[w]; }
    float o4[4] = {0.f, 0.f, 0.f, 0.f};
#pragma unroll
    for (int w = 0; w < 8; ++w) {
      o4[0] += lg[w] * __uint_as_float(bv[w].x << 16);
      o4[1] += lg[w] * __uint_as_float(bv[w].x & 0xffff0000u);
      o4[2] += lg[w] * __uint_as_float(bv[w].y << 16);
      o4[3] += lg[w] * __uint_as_float(bv[w].y & 0xffff0000u);
    }
    const float sc = (1.f - gB) / sm;
    if (bc < 7) {
      uint2 wv;
      wv.x = pack_bf16(o4[0] * sc, o4[1] * sc);
      wv.y = pack_bf16(o4[2] * sc, o4[3] * sc);
      *(uint2*)(Ocat + bpos * OC + DM + bhead * DH + 4 * bc) = wv;
    }
  }

  float mref;
  {
    f32x16 z;
#pragma unroll
    for (int e = 0; e < 16; ++e) z[e] = 0.f;
    f32x16 s = MFMA32(__builtin_bit_cast(bf16x8, k00), q0, z);
    s = MFMA32(__builtin_bit_cast(bf16x8, k01), q1, s);
    float m = s[0];
#pragma unroll
    for (int e = 1; e < 16; ++e) m = fmaxf(m, s[e]);
    mref = fmaxf(m, __shfl_xor(m, 32));
  }

  f32x16 o;
  for (int attempt = 0; attempt < 2; ++attempt) {
    f32x16 negm;
#pragma unroll
    for (int e = 0; e < 16; ++e) { negm[e] = -mref; o[e] = 0.f; }
    if (attempt) issue(0);
    for (int c = 0; c < NCH; ++c) {
      wait_vm<0>();
      raw_barrier();
      if (c + 1 < NCH) issue(c + 1);
      const uint4* base = lds + (((c & 1) * 4 + kq) * TPC) * 256 + lane;
#pragma unroll 2
      for (int t = 0; t < TPC; ++t) {
        bf16x8 kf0 = __builtin_bit_cast(bf16x8, base[t * 256]);
        bf16x8 kf1 = __builtin_bit_cast(bf16x8, base[t * 256 + 64]);
        bf16x8 vf0 = __builtin_bit_cast(bf16x8, base[t * 256 + 128]);
        bf16x8 vf1 = __builtin_bit_cast(bf16x8, base[t * 256 + 192]);
        f32x16 s = MFMA32(kf0, q0, negm);
        s = MFMA32(kf1, q1, s);
        uint4 pa, pb;
        pa.x = pack_bf16(__builtin_amdgcn_exp2f(s[0]), __builtin_amdgcn_exp2f(s[1]));
        pa.y = pack_bf16(__builtin_amdgcn_exp2f(s[2]), __builtin_amdgcn_exp2f(s[3]));
        pa.z = pack_bf16(__builtin_amdgcn_exp2f(s[4]), __builtin_amdgcn_exp2f(s[5]));
        pa.w = pack_bf16(__builtin_amdgcn_exp2f(s[6]), __builtin_amdgcn_exp2f(s[7]));
        pb.x = pack_bf16(__builtin_amdgcn_exp2f(s[8]), __builtin_amdgcn_exp2f(s[9]));
        pb.y = pack_bf16(__builtin_amdgcn_exp2f(s[10]), __builtin_amdgcn_exp2f(s[11]));
        pb.z = pack_bf16(__builtin_amdgcn_exp2f(s[12]), __builtin_amdgcn_exp2f(s[13]));
        pb.w = pack_bf16(__builtin_amdgcn_exp2f(s[14]), __builtin_amdgcn_exp2f(s[15]));
        o = MFMA32(vf0, __builtin_bit_cast(bf16x8, pa), o);
        o = MFMA32(vf1, __builtin_bit_cast(bf16x8, pb), o);
      }
    }
    raw_barrier();
    float* red = (float*)lds;
#pragma unroll
    for (int e = 0; e < 16; ++e) red[(wave * 16 + e) * 64 + lane] = o[e];
    raw_barrier();
    float l = 0.f;
#pragma unroll
    for (int q = 0; q < 4; ++q) l += red[((q * 4 + rb) * 16 + 12) * 64 + 32 + r];
    float ov[4];
#pragma unroll
    for (int i = 0; i < 4; ++i) {
      float t = 0.f;
#pragma unroll
      for (int q = 0; q < 4; ++q) t += red[((q * 4 + rb) * 16 + 4 * kq + i) * 64 + lane];
      ov[i] = t;
    }
    if (attempt == 0) {
      if (!(l < 1.0e18f)) *flag = 1u;
      raw_barrier();
    }
    if (attempt == 1 || *flag == 0u) {
      const int R = rb * 32 + r;
      const int pos = p0 + (R >> 4), head = R & 15;
      const int d = 8 * kq + 4 * h;
      if (d < DH) {
        const float sc = gA / l;
        uint2 w;
        w.x = pack_bf16(ov[0] * sc, ov[1] * sc);
        w.y = pack_bf16(ov[2] * sc, ov[3] * sc);
        *(uint2*)(Ocat + pos * OC + head * DH + d) = w;
      }
      break;
    }
    {
      f32x16 z;
#pragma unroll
      for (int e = 0; e < 16; ++e) z[e] = 0.f;
      float m = -3.0e38f;
      for (int t = 0; t < 16; ++t) {
        const uint4* src = KVg + (kq * 16 + t) * 256 + lane;
        bf16x8 k0 = __builtin_bit_cast(bf16x8, src[0]);
        bf16x8 k1 = __builtin_bit_cast(bf16x8, src[64]);
        f32x16 s = MFMA32(k0, q0, z);
        s = MFMA32(k1, q1, s);
#pragma unroll
        for (int e = 0; e < 16; ++e) m = fmaxf(m, s[e]);
      }
      m = fmaxf(m, __shfl_xor(m, 32));
      raw_barrier();
      red[wave * 64 + lane] = m;
      raw_barrier();
      float mm = red[(0 * 4 + rb) * 64 + lane];
#pragma unroll
      for (int q = 1; q < 4; ++q) mm = fmaxf(mm, red[(q * 4 + rb) * 64 + lane]);
      mref = mm;
      raw_barrier();
    }
  }
}

extern "C" void kernel_launch(void* const* d_in, const int* in_sizes, int n_in,
                              void* d_out, int out_size, void* d_ws, size_t ws_size,
                              hipStream_t stream) {
  const u16* x = (const u16*)d_in[0];
  const u16* wq = (const u16*)d_in[1];
  const u16* wk = (const u16*)d_in[2];
  const u16* wv = (const u16*)d_in[3];
  const u16* woA = (const u16*)d_in[4];
  const u16* wqkv = (const u16*)d_in[6];
  const u16* woB = (const u16*)d_in[7];
  const u16* gw = (const u16*)d_in[8];
  char* ws = (char*)d_ws;
  u16* Qa = (u16*)(ws);
  u16* KV = (u16*)(ws + (2u << 20));
  u16* QKVb = (u16*)(ws + (3u << 20));
  float* gate = (float*)(ws + (9u << 20));
  u16* Ocat = (u16*)(ws + (10u << 20));
  u16* out = (u16*)d_out;

  k_proj<<<dim3(256), 256, 0, stream>>>(x, wq, wk, wv, wqkv, gw, Qa, KV, QKVb, gate);
  k_attn<<<dim3(S_LEN / 8), 1024, 0, stream>>>(Qa, KV, QKVb, gate, Ocat);
  k_out<<<dim3(224), 256, 0, stream>>>(Ocat, woA, woB, out);
}
```

```cpp
#include <hip/hip_runtime.h>
#include <hip/hip_bf16.h>
#include <stdint.h>

typedef __attribute__((ext_vector_type(8))) short bf16x8;
typedef __attribute__((ext_vector_type(4))) short s16x4;
typedef __attribute__((ext_vector_type(16))) float f32x16;
typedef __attribute__((ext_vector_type(2))) __bf16 bf2;
typedef __attribute__((address_space(3))) s16x4* lds_s16x4_ptr;
typedef unsigned short u16;

#define DI __device__ __forceinline__
#define STAMP(i) do {} while (0)
#define EXP2(x) __builtin_amdgcn_exp2f(x)
#define MFMA32(a, b, c) __builtin_amdgcn_mfma_f32_32x32x16_bf16((a), (b), (c), 0, 0, 0)

constexpr int S_LEN = 2048;
constexpr int DM = 448;
constexpr int NH = 16;
constexpr int DH = 28;
constexpr int NB3 = 1344;
constexpr int OC = 896;

DI unsigned pack_bf16(float a, float b) {
  bf2 v; v[0] = (__bf16)a; v[1] = (__bf16)b;
  return __builtin_bit_cast(unsigned, v);
}
DI u16 to_bf16(float a) { __bf16 v = (__bf16)a; return __builtin_bit_cast(u16, v); }
DI float bf_to_f(u16 a) { return __uint_as_float(((unsigned)a) << 16); }

template <int V> struct IC { static constexpr int value = V; };
template <int I, int N, class F> DI void static_for(F&& f) {
  if constexpr (I < N) { f(IC<I>{}); static_for<I + 1, N>(f); }
}
template <int N> DI void wait_vm() { asm volatile("s_waitcnt vmcnt(%0)" ::"n"(N) : "memory"); }
DI void raw_barrier() { asm volatile("s_waitcnt lgkmcnt(0)" ::: "memory"); __builtin_amdgcn_s_barrier(); asm volatile("" ::: "memory"); }
DI void glds16(const void* g, void* l) {
  __builtin_amdgcn_global_load_lds((const __attribute__((address_space(1))) unsigned*)g,
                                   (__attribute__((address_space(3))) unsigned*)l, 16, 0, 0);
}

template <int BN> DI int bswz(int row) { return (BN == 128) ? ((row & 3) << 2) : (((row >> 1) & 1) << 2); }

template <int BM, int BN, int NS, int NBUF, class BPtr, class Epi>
DI void gemm_glds(const u16* __restrict__ A, int lda, int m0, char* smem, BPtr bptr, Epi epi) {
  constexpr int TM = BM / 64, TN = BN / 64;
  constexpr int GA = BM / 32, GB = BN / 32, G = GA + GB;
  constexpr int A_BYTES = BM * 128, B_BYTES = BN * 128, ST_BYTES = A_BYTES + B_BYTES;
  constexpr int CPR = BN / 8;
  const int tid = threadIdx.x, lane = tid & 63, wave = __builtin_amdgcn_readfirstlane(tid >> 6);
  const int wm = wave >> 1, wn = wave & 1;
  const int r = lane & 31, h = lane >> 5;

  const u16* asrc[GA];
#pragma unroll
  for (int i = 0; i < GA; ++i) {
    int idx = (wave * GA + i) * 64 + lane, row = idx >> 3, ch = (idx & 7) ^ ((row >> 1) & 7);
    asrc[i] = A + (size_t)(m0 + row) * lda + ch * 8;
  }
  int brow[GB], bch[GB];
#pragma unroll
  for (int i = 0; i < GB; ++i) {
    int idx = (wave * GB + i) * 64 + lane;
    brow[i] = idx / CPR;
    bch[i] = (idx % CPR) ^ bswz<BN>(brow[i]);
  }
  auto issue = [&](int st) {
    char* base = smem + (st % NBUF) * ST_BYTES;
#pragma unroll
    for (int i = 0; i < GA; ++i) glds16(asrc[i] + st * 64, base + (wave * GA + i) * 1024);
#pragma unroll
    for (int i = 0; i < GB; ++i) glds16(bptr(st * 64 + brow[i], bch[i] * 8), base + A_BYTES + (wave * GB + i) * 1024);
  };

  f32x16 acc[TM][TN];
#pragma unroll
  for (int i = 0; i < TM; ++i)
#pragma unroll
    for (int j = 0; j < TN; ++j)
#pragma unroll
      for (int e = 0; e < 16; ++e) acc[i][j][e] = 0.f;

  int aoff[TM], asw[TM];
#pragma unroll
  for (int i = 0; i < TM; ++i) {
    int row = wm * (BM / 2) + i * 32 + r;
    aoff[i] = row * 128; asw[i] = (row >> 1) & 7;
  }
  const int trq = (lane & 15) >> 2, trp = lane & 3, trblk = (lane >> 4) & 1;

#pragma unroll
  for (int st = 0; st < NBUF - 1; ++st) if (st < NS) issue(st);

  static_for<0, NS>([&](auto SI) {
    constexpr int st = decltype(SI)::value;
    constexpr int issued = (st + NBUF - 1 < NS) ? (st + NBUF - 1) : NS;
    wait_vm<G * (issued - st - 1)>();
    raw_barrier();
    if constexpr (st + NBUF - 1 < NS) issue(st + NBUF - 1);
    const char* as = smem + (st % NBUF) * ST_BYTES;
    const char* bs = as + A_BYTES;
#pragma unroll
    for (int ks = 0; ks < 4; ++ks) {
      bf16x8 af[TM], bfr[TN];
#pragma unroll
      for (int i = 0; i < TM; ++i) af[i] = *(const bf16x8*)(as + aoff[i] + (((2 * ks + h) ^ asw[i]) << 4));
      const int rk = ks * 16 + 8 * h + trq;
#pragma unroll
      for (int j = 0; j < TN; ++j) {
        const int col = wn * (BN / 2) + j * 32 + 16 * trblk + 4 * trp;
        const char* p = bs + rk * (BN * 2) + (((col >> 3) ^ bswz<BN>(rk)) << 4) + ((trp & 1) << 3);
        s16x4 lo = __builtin_amdgcn_ds_read_tr16_b64_v4i16((lds_s16x4_ptr)(p));
        s16x4 hi = __builtin_amdgcn_ds_read_tr16_b64_v4i16((lds_s16x4_ptr)(p + 4 * BN * 2 + (((((col >> 3) ^ bswz<BN>(rk + 4)) - ((col >> 3) ^ bswz<BN>(rk)))) << 4)));
        bfr[j] = __builtin_shufflevector(lo, hi, 0, 1, 2, 3, 4, 5, 6, 7);
      }
#pragma unroll
      for (int i = 0; i < TM; ++i)
#pragma unroll
        for (int j = 0; j < TN; ++j) acc[i][j] = MFMA32(af[i], bfr[j], acc[i][j]);
    }
  });
#pragma unroll
  for (int i = 0; i < TM; ++i)
#pragma unroll
    for (int j = 0; j < TN; ++j)
      epi(m0 + wm * (BM / 2) + i * 32, wn * (BN / 2) + j * 32, acc[i][j], r, h);
}

constexpr int P_NBUF = 4;
constexpr int SP_AST = 456, SP_BST = 96;
constexpr int P_SMEM = (64 * SP_AST + 448 * SP_BST) * 2;

__global__ __launch_bounds__(256) void k_proj(const u16* __restrict__ x, const u16* __restrict__ wq,
                                              const u16* __restrict__ wk, const u16* __restrict__ wv,
                                              const u16* __restrict__ wqkv, const u16* __restrict__ gw,
                                              u16* __restrict__ Qa, u16* __restrict__ KV,
                                              u16* __restrict__ QKVb, float* __restrict__ gate) {
  __shared__ __attribute__((aligned(16))) char smem[P_SMEM];
  const int bid = blockIdx.x;
  if (bid < 224) {
    const int n0 = (bid % 14) * 128, m0 = (bid / 14) * 128;
    auto bptr = [&](int k, int nc) -> const u16* {
      const int n = n0 + nc;
      return (n < DM) ? (wq + k * DM + n) : (wqkv + k * NB3 + (n - DM));
    };
    const float qscale = 0.18898223650461362f * 1.4426950408889634f;
    auto epi = [&](int mb, int nbl, const f32x16& a, int r, int h) {
      const int nb = n0 + nbl, n = nb + r;
      if (nb < DM) {
#pragma unroll
        for (int e = 0; e < 16; ++e) {
          int m = mb + (e & 3) + 8 * (e >> 2) + 4 * h;
          Qa[m * DM + n] = to_bf16(a[e] * qscale);
        }
      } else {
#pragma unroll
        for (int e = 0; e < 16; ++e) {
          int m = mb + (e & 3) + 8 * (e >> 2) + 4 * h;
          QKVb[m * NB3 + (n - DM)] = to_bf16(a[e]);
        }
      }
    };
    gemm_glds<128, 128, 7, P_NBUF>(x, DM, m0, smem, bptr, epi);
    return;
  }
  const int m0 = (bid - 224) * 64;
  const int tid = threadIdx.x, lane = tid & 63, wave = tid >> 6;
  u16* as = (u16*)smem;
  u16* bs = as + 64 * SP_AST;
  uint4 ra[14]; uint2 rb0[14], rb1[14]; u16 rg[14];
#pragma unroll
  for (int i = 0; i < 14; ++i) {
    int c = tid + i * 256, row = c / 56, ch = c % 56;
    ra[i] = *(const uint4*)(x + (m0 + row) * DM + ch * 8);
  }
  const int nc = tid & 7;
  const u16* wsrc = (nc < 4) ? wk : wv;
  const int cb = (nc & 3) * 8;
#pragma unroll
  for (int i = 0; i < 14; ++i) {
    int k = (tid >> 3) + i * 32;
    rb0[i] = *(const uint2*)(wsrc + k * DH + cb);
    rb1[i] = *(const uint2*)(wsrc + k * DH + ((cb < 24) ? cb + 4 : cb));
    rg[i] = gw[k];
  }
#pragma unroll
  for (int i = 0; i < 14; ++i) {
    int c = tid + i * 256, row = c / 56, ch = c % 56;
    *(uint4*)(as + row * SP_AST + ch * 8) = ra[i];
  }
#pragma unroll
  for (int i = 0; i < 14; ++i) {
    int k = (tid >> 3) + i * 32;
    uint4 v = make_uint4(rb0[i].x, rb0[i].y, rb1[i].x, rb1[i].y);
    if (cb == 24) { v.z = (nc == 7) ? (unsigned)rg[i] : 0u; v.w = 0u; }
    *(uint4*)(bs + k * SP_BST + nc * 8) = v;
  }
  __syncthreads();
  const int wm = wave >> 1, wn = wave & 1, r = lane & 31, h = lane >> 5;
  const int trq = (lane & 15) >> 2, trp = lane & 3, trblk = (lane >> 4) & 1;
  f32x16 a;
#pragma unroll
  for (int e = 0; e < 16; ++e) a[e] = 0.f;
#pragma unroll 4
  for (int ks = 0; ks < 28; ++ks) {
    bf16x8 af = *(const bf16x8*)(as + (wm * 32 + r) * SP_AST + ks * 16 + h * 8);
    const u16* p = bs + (ks * 16 + 8 * h + trq) * SP_BST + wn * 32 + 16 * trblk + 4 * trp;
    s16x4 lo = __builtin_amdgcn_ds_read_tr16_b64_v4i16((lds_s16x4_ptr)(p));
    s16x4 hi = __builtin_amdgcn_ds_read_tr16_b64_v4i16((lds_s16x4_ptr)(p + 4 * SP_BST));
    a = MFMA32(af, __builtin_shufflevector(lo, hi, 0, 1, 2, 3, 4, 5, 6, 7), a);
  }
  const int mb = m0 + wm * 32, d = r;
  if (wn == 0) {
    const int s = d >> 4, hh = (d >> 3) & 1, j = d & 7;
#pragma unroll
    for (int e = 0; e < 16; ++e) {
      int m = mb + (e & 3) + 8 * (e >> 2) + 4 * h;
      int tile = m >> 5, rr = m & 31;
      KV[tile * 2048 + s * 512 + (hh * 32 + rr) * 8 + j] = (d < DH) ? to_bf16(a[e]) : ((d < 31) ? (u16)0x3F80 : (u16)0);
    }
  } else {
#pragma unroll
    for (int e = 0; e < 16; ++e) {
      int m = mb + (e & 3) + 8 * (e >> 2) + 4 * h;
      int tile = m >> 5, kk = m & 31;
      int s = kk >> 4, k16 = kk & 15;
      int hh = (k16 >> 2) & 1, j = ((k16 >> 3) << 2) | (k16 & 3);
      u16 val = (d < DH) ? to_bf16(a[e]) : ((d == DH) ? (u16)0x3F80 : (u16)0);
      KV[tile * 2048 + 1024 + s * 512 + (hh * 32 + d) * 8 + j] = val;
      if (d == DH) gate[m] = 1.f / (1.f + __expf(-a[e]));
    }
  }
}

constexpr int O_NBUF = 8;

__global__ __launch_bounds__(256) void k_out(const u16* __restrict__ Ocat, const u16* __restrict__ woA,
                                             const u16* __restrict__ woB, u16* __restrict__ out) {
  __shared__ __attribute__((aligned(16))) char smem[O_NBUF * 16384];
  const int n0 = (blockIdx.x % 7) * 64, m0 = (blockIdx.x / 7) * 64;
  auto bptr = [&](int k, int nc) -> const u16* {
    return ((k < DM) ? (woA + k * DM) : (woB + (k - DM) * DM)) + n0 + nc;
  };
  auto epi = [&](int mb, int nbl, const f32x16& a, int r, int h) {
    const int n = n0 + nbl + r;
#pragma unroll
    for (int e = 0; e < 16; ++e) {
      int m = mb + (e & 3) + 8 * (e >> 2) + 4 * h;
      out[m * DM + n] = to_bf16(a[e]);
    }
  };
  gemm_glds<64, 64, 14, O_NBUF>(Ocat, OC, m0, smem, bptr, epi);
}

constexpr int TPC = 2;
constexpr int NCH = 16 / TPC;
constexpr int ANB = 3;
constexpr int CH_U4 = 4 * TPC * 256;
constexpr int BROWS = 22;
constexpr int BD_U4 = 3712;
constexpr int A_LDS_U4 = ANB * CH_U4 + BD_U4 + 16;

__global__ __launch_bounds__(1024) void k_attn(const u16* __restrict__ Qa, const u16* __restrict__ KV,
                                               const u16* __restrict__ QKVb, const float* __restrict__ gate,
                                               u16* __restrict__ Ocat) {
  __shared__ __attribute__((aligned(16))) uint4 lds[A_LDS_U4];
  const int tid = threadIdx.x, lane = tid & 63, wave = __builtin_amdgcn_readfirstlane(tid >> 6);
  const int rb = wave & 3, kq = wave >> 2;
  const int r = lane & 31, h = lane >> 5;
  const int p0 = blockIdx.x * 8;
  const int rot = blockIdx.x & (NCH - 1);
  const uint4* KVg = (const uint4*)KV;
  uint4* bdat = lds + ANB * CH_U4;
  volatile unsigned* flag = (volatile unsigned*)(bdat + BD_U4);

  auto issue2 = [&](int c, int slot) {
    const int cc = (c + rot) & (NCH - 1);
    const char* src = (const char*)(KVg + (kq * 16 + cc * TPC + (rb >> 1)) * 256 + (rb & 1) * 128);
    uint4* dst = lds + slot * CH_U4 + (kq * TPC + (rb >> 1)) * 256 + (rb & 1) * 128;
    glds16(src + (unsigned)(lane * 16), dst);
    glds16(src + 1024 + (unsigned)(lane * 16), dst + 64);
  };
  auto issue = [&](int c) { issue2(c, c); };

  STAMP(0);
  const int brow0 = (p0 >= 14) ? (p0 - 14) : 0;
  {
    const uint4* src = (const uint4*)(QKVb + brow0 * NB3);
    const int nu4 = (p0 + 8 - brow0) * (NB3 / 8);
#pragma unroll
    for (int i = 0; i < 4; ++i) {
      const int base = (wave + 16 * i) * 64;
      if (base < nu4) { int idx = base + lane; idx = idx < nu4 ? idx : nu4 - 1; glds16(src + idx, bdat + base); }
    }
  }
  issue(0);
  issue(1);
  const u16* qp = Qa + p0 * DM + (rb * 32 + r) * DH;
  uint2 qa = *(const uint2*)(qp + 8 * h), qb = *(const uint2*)(qp + 8 * h + 4);
  uint2 qc = *(const uint2*)(qp + 16 + 8 * h);
  uint2 qd = *(const uint2*)(qp + 20);
  uint4 k00 = KVg[lane], k01 = KVg[64 + lane];
  const float gA = gate[p0 + ((rb * 32 + r) >> 4)];
  if (tid == 0) *flag = 0u;

  if (h) qd = make_uint2(0, 0);
  const bf16x8 q0 = __builtin_bit_cast(bf16x8, make_uint4(qa.x, qa.y, qb.x, qb.y));
  const bf16x8 q1 = __builtin_bit_cast(bf16x8, make_uint4(qc.x, qc.y, qd.x, qd.y));
  STAMP(1);
  float mref;
  {
    f32x16 z;
#pragma unroll
    for (int e = 0; e < 16; ++e) z[e] = 0.f;
    f32x16 s = MFMA32(__builtin_bit_cast(bf16x8, k00), q0, z);
    s = MFMA32(__builtin_bit_cast(bf16x8, k01), q1, s);
    float m = s[0];
#pragma unroll
    for (int e = 1; e < 16; ++e) m = fmaxf(m, s[e]);
    mref = fmaxf(m, __shfl_xor(m, 32));
  }
  wait_vm<0>();

  STAMP(2);
  f32x16 o;
#pragma nounroll
  for (int attempt = 0; attempt < 2; ++attempt) {
    bf16x8 q1m = q1;
    {
      const float n0 = -mref;
      const u16 c0 = to_bf16(n0); const float n1 = n0 - bf_to_f(c0);
      const u16 c1 = to_bf16(n1); const float n2 = n1 - bf_to_f(c1);
      const u16 c2 = to_bf16(n2);
      if (h) { q1m[4] = (short)c0; q1m[5] = (short)c1; q1m[6] = (short)c2; }
    }
    f32x16 zero;
#pragma unroll
    for (int e = 0; e < 16; ++e) { zero[e] = 0.f; o[e] = 0.f; }
    if (attempt) { issue(0); issue(1); }
    auto softmax_pv = [&](const f32x16& s, const bf16x8& vf0, const bf16x8& vf1) {
      uint4 pa, pb;
      pa.x = pack_bf16(EXP2(s[0]), EXP2(s[1]));
      pa.y = pack_bf16(EXP2(s[2]), EXP2(s[3]));
      pa.z = pack_bf16(EXP2(s[4]), EXP2(s[5]));
      pa.w = pack_bf16(EXP2(s[6]), EXP2(s[7]));
      o = MFMA32(vf0, __builtin_bit_cast(bf16x8, pa), o);
      pb.x = pack_bf16(EXP2(s[8]), EXP2(s[9]));
      pb.y = pack_bf16(EXP2(s[10]), EXP2(s[11]));
      pb.z = pack_bf16(EXP2(s[12]), EXP2(s[13]));
      pb.w = pack_bf16(EXP2(s[14]), EXP2(s[15]));
      o = MFMA32(vf1, __builtin_bit_cast(bf16x8, pb), o);
    };
    f32x16 sA, sB;
    bf16x8 vA0, vA1, vB0, vB1;
    wait_vm<2>();
    raw_barrier();
    issue2(2, 2);
    {
      const uint4* base = lds + kq * TPC * 256 + lane;
      bf16x8 kf0 = __builtin_bit_cast(bf16x8, base[0]), kf1 = __builtin_bit_cast(bf16x8, base[64]);
      vA0 = __builtin_bit_cast(bf16x8, base[128]); vA1 = __builtin_bit_cast(bf16x8, base[192]);
      sA = MFMA32(kf0, q0, zero);
      sA = MFMA32(kf1, q1m, sA);
    }
    int slot = 0;
#pragma nounroll
    for (int c = 0; c < NCH; ++c) {
      {
        const uint4* base = lds + slot * CH_U4 + kq * TPC * 256 + 256 + lane;
        bf16x8 kf0 = __builtin_bit_cast(bf16x8, base[0]), kf1 = __builtin_bit_cast(bf16x8, base[64]);
        vB0 = __builtin_bit_cast(bf16x8, base[128]); vB1 = __builtin_bit_cast(bf16x8, base[192]);
        sB = MFMA32(kf0, q0, zero);
        sB = MFMA32(kf1, q1m, sB);
      }
      __builtin_amdgcn_sched_barrier(0);
      softmax_pv(sA, vA0, vA1);
      __builtin_amdgcn_sched_barrier(0);
      slot = (slot == 2) ? 0 : slot + 1;
      if (c + 1 < NCH) {
        if (c + 2 < NCH) wait_vm<2>(); else wait_vm<0>();
        raw_barrier();
        if (c + 3 < NCH) issue2(c + 3, slot == 0 ? 2 : slot - 1);
        const uint4* base = lds + slot * CH_U4 + kq * TPC * 256 + lane;
        bf16x8 kf0 = __builtin_bit_cast(bf16x8, base[0]), kf1 = __builtin_bit_cast(bf16x8, base[64]);
        vA0 = __builtin_bit_cast(bf16x8, base[128]); vA1 = __builtin_bit_cast(bf16x8, base[192]);
        sA = MFMA32(kf0, q0, zero);
        sA = MFMA32(kf1, q1m, sA);
      }
      __builtin_amdgcn_sched_barrier(0);
      softmax_pv(sB, vB0, vB1);
      __builtin_amdgcn_sched_barrier(0);
    }
    STAMP(3);
    raw_barrier();
    float* red = (float*)lds;
#pragma unroll
    for (int e = 0; e < 16; ++e) red[(wave * 16 + e) * 64 + lane] = o[e];

    if (attempt == 0) {
      const int pair = tid >> 3, bc = tid & 7, bcc = (bc < 7) ? bc : 6;
      const int bpos = p0 + (pair >> 4), bhead = pair & 15;
      const u16* bb = (const u16*)bdat + bhead * DH + 4 * bcc;
      const uint2 bq = *(const uint2*)(bb + (bpos - brow0) * NB3);
      const float gB = gate[bpos];
      float q4[4] = {__uint_as_float(bq.x << 16), __uint_as_float(bq.x & 0xffff0000u),
                     __uint_as_float(bq.y << 16), __uint_as_float(bq.y & 0xffff0000u)};
      float lg[8];
#pragma unroll
      for (int w = 0; w < 8; ++w) {
        int kp = bpos - 2 * w; kp = kp < 0 ? 0 : kp;
        const uint2 bk = *(const uint2*)(bb + (kp - brow0) * NB3 + DM);
        float t = q4[0] * __uint_as_float(bk.x << 16) + q4[1] * __uint_as_float(bk.x & 0xffff0000u) +
                  q4[2] * __uint_as_float(bk.y << 16) + q4[3] * __uint_as_float(bk.y & 0xffff0000u);
        lg[w] = (bc < 7) ? t : 0.f;
      }
#pragma unroll
      for (int w = 0; w < 8; ++w) {
        lg[w] += __shfl_xor(lg[w], 1); lg[w] += __shfl_xor(lg[w], 2); lg[w] += __shfl_xor(lg[w], 4);
      }
      float mx = -3.0e38f;
#pragma unroll
      for (int w = 0; w < 8; ++w) {
        lg[w] = (bpos - 2 * w >= 0) ? lg[w] * 0.18898223650461362f : -3.0e38f;
        mx = fmaxf(mx, lg[w]);
      }
      float sm = 0.f;
#pragma unroll
      for (int w = 0; w < 8; ++w) { lg[w] = (bpos - 2 * w >= 0) ? __expf(lg[w] - mx) : 0.f; sm += lg[w]; }
      float o4[4] = {0.f, 0.f, 0.f, 0.f};
#pragma unroll
      for (int w = 0; w < 8; ++w) {
        int kp = bpos - 2 * w; kp = kp < 0 ? 0 : kp;
        const uint2 bv = *(const uint2*)(bb + (kp - brow0) * NB3 + 2 * DM);
        o4[0] += lg[w] * __uint_as_float(bv.x << 16);
        o4[1] += lg[w] * __uint_as_float(bv.x & 0xffff0000u);
        o4[2] += lg[w] * __uint_as_float(bv.y << 16);
        o4[3] += lg[w] * __uint_as_float(bv.y & 0xffff0000u);
      }
      const float sc = (1.f - gB) / sm;
      if (bc < 7) {
        uint2 wv;
        wv.x = pack_bf16(o4[0] * sc, o4[1] * sc);
        wv.y = pack_bf16(o4[2] * sc, o4[3] * sc);
        *(uint2*)(Ocat + bpos * OC + DM + bhead * DH + 4 * bc) = wv;
      }
    }
    raw_barrier();
    float l = 0.f;
#pragma unroll
    for (int q = 0; q < 4; ++q) l += red[((q * 4 + rb) * 16 + 12) * 64 + 32 + r];
    float ov[4];
#pragma unroll
    for (int i = 0; i < 4; ++i) {
      float t = 0.f;
#pragma unroll
      for (int q = 0; q < 4; ++q) t += red[((q * 4 + rb) * 16 + 4 * kq + i) * 64 + lane];
      ov[i] = t;
    }
    if (attempt == 0) {
      if (!(l < 1.0e18f)) *flag = 1u;
      raw_barrier();
    }
    if (attempt == 1 || *flag == 0u) {
      const int R = rb * 32 + r;
      const int pos = p0 + (R >> 4), head = R & 15;
      const int d = 8 * kq + 4 * h;
      if (d < DH) {
        const float sc = gA / l;
        uint2 w;
        w.x = pack_bf16(ov[0] * sc, ov[1] * sc);
        w.y = pack_bf16(ov[2] * sc, ov[3] * sc);
        *(uint2*)(Ocat + pos * OC + head * DH + d) = w;
      }
      STAMP(4);
      break;
    }
    {
      f32x16 z;
#pragma unroll
      for (int e = 0; e < 16; ++e) z[e] = 0.f;
      float m = -3.0e38f;
      for (int t = 0; t < 16; ++t) {
        const uint4* src = KVg + (kq * 16 + t) * 256 + lane;
        bf16x8 k0 = __builtin_bit_cast(bf16x8, src[0]);
        bf16x8 k1 = __builtin_bit_cast(bf16x8, src[64]);
        f32x16 s = MFMA32(k0, q0, z);
        s = MFMA32(k1, q1, s);
#pragma unroll
        for (int e = 0; e < 16; ++e) m = fmaxf(m, s[e]);
      }
      m = fmaxf(m, __shfl_xor(m, 32));
      wait_vm<0>();
      raw_barrier();
      red[wave * 64 + lane] = m;
      raw_barrier();
      float mm = red[(0 * 4 + rb) * 64 + lane];
#pragma unroll
      for (int q = 1; q < 4; ++q) mm = fmaxf(mm, red[(q * 4 + rb) * 64 + lane]);
      mref = mm;
      raw_barrier();
    }
  }
}

extern "C" void kernel_launch(void* const* d_in, const int* in_sizes, int n_in,
                              void* d_out, int out_size, void* d_ws, size_t ws_size,
                              hipStream_t stream) {
  const u16* x = (const u16*)d_in[0];
  const u16* wq = (const u16*)d_in[1];
  const u16* wk = (const u16*)d_in[2];
  const u16* wv = (const u16*)d_in[3];
  const u16* woA = (const u16*)d_in[4];
  const u16* wqkv = (const u16*)d_in[6];
  const u16* woB = (const u16*)d_in[7];
  const u16* gw = (const u16*)d_in[8];
  char* ws = (char*)d_ws;
  u16* Qa = (u16*)(ws);
  u16* KV = (u16*)(ws + (2u << 20));
  u16* QKVb = (u16*)(ws + (3u << 20));
  float* gate = (float*)(ws + (9u << 20));
  u16* Ocat = (u16*)(ws + (10u << 20));
  u16* out = (u16*)d_out;

  k_proj<<<dim3(256), 256, 0, stream>>>(x, wq, wk, wv, wqkv, gw, Qa, KV, QKVb, gate);
  k_attn<<<dim3(S_LEN / 8), 1024, 0, stream>>>(Qa, KV, QKVb, gate, Ocat);
  k_out<<<dim3(224), 256, 0, stream>>>(Ocat, woA, woB, out);
}
```

```cpp
#include <hip/hip_runtime.h>
#include <hip/hip_bf16.h>
#include <stdint.h>

typedef __attribute__((ext_vector_type(8))) short bf16x8;
typedef __attribute__((ext_vector_type(4))) short s16x4;
typedef __attribute__((ext_vector_type(16))) float f32x16;
typedef __attribute__((ext_vector_type(2))) __bf16 bf2;
typedef __attribute__((address_space(3))) s16x4* lds_s16x4_ptr;
typedef unsigned short u16;

#define DI __device__ __forceinline__
#define STAMP(i) do {} while (0)
#define EXP2(x) __builtin_amdgcn_exp2f(x)
#define MFMA32(a, b, c) __builtin_amdgcn_mfma_f32_32x32x16_bf16((a), (b), (c), 0, 0, 0)

constexpr int S_LEN = 2048;
constexpr int DM = 448;
constexpr int NH = 16;
constexpr int DH = 28;
constexpr int NB3 = 1344;
constexpr int OC = 896;

DI unsigned pack_bf16(float a, float b) {
  bf2 v; v[0] = (__bf16)a; v[1] = (__bf16)b;
  return __builtin_bit_cast(unsigned, v);
}
DI u16 to_bf16(float a) { __bf16 v = (__bf16)a; return __builtin_bit_cast(u16, v); }
DI float bf_to_f(u16 a) { return __uint_as_float(((unsigned)a) << 16); }

template <int V> struct IC { static constexpr int value = V; };
template <int I, int N, class F> DI void static_for(F&& f) {
  if constexpr (I < N) { f(IC<I>{}); static_for<I + 1, N>(f); }
}
template <int N> DI void wait_vm() { asm volatile("s_waitcnt vmcnt(%0)" ::"n"(N) : "memory"); }
DI void raw_barrier() { asm volatile("s_waitcnt lgkmcnt(0)" ::: "memory"); __builtin_amdgcn_s_barrier(); asm volatile("" ::: "memory"); }
DI void glds16(const void* g, void* l) {
  __builtin_amdgcn_global_load_lds((const __attribute__((address_space(1))) unsigned*)g,
                                   (__attribute__((address_space(3))) unsigned*)l, 16, 0, 0);
}

template <int BN> DI int bswz(int row) { return (BN == 128) ? ((row & 3) << 2) : (((row >> 1) & 1) << 2); }

template <int BM, int BN, int NS, int NBUF, class BPtr, class Epi>
DI void gemm_glds(const u16* __restrict__ A, int lda, int m0, char* smem, BPtr bptr, Epi epi) {
  constexpr int TM = BM / 64, TN = BN / 64;
  constexpr int GA = BM / 32, GB = BN / 32, G = GA + GB;
  constexpr int A_BYTES = BM * 128, B_BYTES = BN * 128, ST_BYTES = A_BYTES + B_BYTES;
  constexpr int CPR = BN / 8;
  const int tid = threadIdx.x, lane = tid & 63, wave = __builtin_amdgcn_readfirstlane(tid >> 6);
  const int wm = wave >> 1, wn = wave & 1;
  const int r = lane & 31, h = lane >> 5;

  const u16* asrc[GA];
#pragma unroll
  for (int i = 0; i < GA; ++i) {
    int idx = (wave * GA + i) * 64 + lane, row = idx >> 3, ch = (idx & 7) ^ ((row >> 1) & 7);
    asrc[i] = A + (size_t)(m0 + row) * lda + ch * 8;
  }
  int brow[GB], bch[GB];
#pragma unroll
  for (int i = 0; i < GB; ++i) {
    int idx = (wave * GB + i) * 64 + lane;
    brow[i] = idx / CPR;
    bch[i] = (idx % CPR) ^ bswz<BN>(brow[i]);
  }
  auto issue = [&](int st) {
    char* base = smem + (st % NBUF) * ST_BYTES;
#pragma unroll
    for (int i = 0; i < GA; ++i) glds16(asrc[i] + st * 64, base + (wave * GA + i) * 1024);
#pragma unroll
    for (int i = 0; i < GB; ++i) glds16(bptr(st * 64 + brow[i], bch[i] * 8), base + A_BYTES + (wave * GB + i) * 1024);
  };

  f32x16 acc[TM][TN];
#pragma unroll
  for (int i = 0; i < TM; ++i)
#pragma unroll
    for (int j = 0; j < TN; ++j)
#pragma unroll
      for (int e = 0; e < 16; ++e) acc[i][j][e] = 0.f;

  int aoff[TM], asw[TM];
#pragma unroll
  for (int i = 0; i < TM; ++i) {
    int row = wm * (BM / 2) + i * 32 + r;
    aoff[i] = row * 128; asw[i] = (row >> 1) & 7;
  }
  const int trq = (lane & 15) >> 2, trp = lane & 3, trblk = (lane >> 4) & 1;

#pragma unroll
  for (int st = 0; st < NBUF - 1; ++st) if (st < NS) issue(st);

  static_for<0, NS>([&](auto SI) {
    constexpr int st = decltype(SI)::value;
    constexpr int issued = (st + NBUF - 1 < NS) ? (st + NBUF - 1) : NS;
    wait_vm<G * (issued - st - 1)>();
    raw_barrier();
    if constexpr (st + NBUF - 1 < NS) issue(st + NBUF - 1);
    const char* as = smem + (st % NBUF) * ST_BYTES;
    const char* bs = as + A_BYTES;
#pragma unroll
    for (int ks = 0; ks < 4; ++ks) {
      bf16x8 af[TM], bfr[TN];
#pragma unroll
      for (int i = 0; i < TM; ++i) af[i] = *(const bf16x8*)(as + aoff[i] + (((2 * ks + h) ^ asw[i]) << 4));
      const int rk = ks * 16 + 8 * h + trq;
#pragma unroll
      for (int j = 0; j < TN; ++j) {
        const int col = wn * (BN / 2) + j * 32 + 16 * trblk + 4 * trp;
        const char* p = bs + rk * (BN * 2) + (((col >> 3) ^ bswz<BN>(rk)) << 4) + ((trp & 1) << 3);
        s16x4 lo = __builtin_amdgcn_ds_read_tr16_b64_v4i16((lds_s16x4_ptr)(p));
        s16x4 hi = __builtin_amdgcn_ds_read_tr16_b64_v4i16((lds_s16x4_ptr)(p + 4 * BN * 2 + (((((col >> 3) ^ bswz<BN>(rk + 4)) - ((col >> 3) ^ bswz<BN>(rk)))) << 4)));
        bfr[j] = __builtin_shufflevector(lo, hi, 0, 1, 2, 3, 4, 5, 6, 7);
      }
#pragma unroll
      for (int i = 0; i < TM; ++i)
#pragma unroll
        for (int j = 0; j < TN; ++j) acc[i][j] = MFMA32(af[i], bfr[j], acc[i][j]);
    }
  });
#pragma unroll
  for (int i = 0; i < TM; ++i)
#pragma unroll
    for (int j = 0; j < TN; ++j)
      epi(m0 + wm * (BM / 2) + i * 32, wn * (BN / 2) + j * 32, acc[i][j], r, h);
}

constexpr int P_NBUF = 4;
constexpr int SP_AST = 456, SP_BST = 96;
constexpr int P_SMEM = (64 * SP_AST + 448 * SP_BST) * 2;

__global__ __launch_bounds__(256) void k_proj(const u16* __restrict__ x, const u16* __restrict__ wq,
                                              const u16* __restrict__ wk, const u16* __restrict__ wv,
                                              const u16* __restrict__ wqkv, const u16* __restrict__ gw,
                                              u16* __restrict__ Qa, u16* __restrict__ KV,
                                              u16* __restrict__ QKVb, float* __restrict__ gate) {
  __shared__ __attribute__((aligned(16))) char smem[P_SMEM];
  const int bid = blockIdx.x;
  if (bid < 224) {
    const int n0 = (bid % 14) * 128, m0 = (bid / 14) * 128;
    auto bptr = [&](int k, int nc) -> const u16* {
      const int n = n0 + nc;
      return (n < DM) ? (wq + k * DM + n) : (wqkv + k * NB3 + (n - DM));
    };
    const float qscale = 0.18898223650461362f * 1.4426950408889634f;
    auto epi = [&](int mb, int nbl, const f32x16& a, int r, int h) {
      const int nb = n0 + nbl, n = nb + r;
      if (nb < DM) {
#pragma unroll
        for (int e = 0; e < 16; ++e) {
          int m = mb + (e & 3) + 8 * (e >> 2) + 4 * h;
          Qa[m * DM + n] = to_bf16(a[e] * qscale);
        }
      } else {
#pragma unroll
        for (int e = 0; e < 16; ++e) {
          int m = mb + (e & 3) + 8 * (e >> 2) + 4 * h;
          QKVb[m * NB3 + (n - DM)] = to_bf16(a[e]);
        }
      }
    };
    gemm_glds<128, 128, 7, P_NBUF>(x, DM, m0, smem, bptr, epi);
    return;
  }
  const int m0 = (bid - 224) * 64;
  const int tid = threadIdx.x, lane = tid & 63, wave = tid >> 6;
  u16* as = (u16*)smem;
  u16* bs = as + 64 * SP_AST;
  uint4 ra[14]; uint2 rb0[14], rb1[14]; u16 rg[14];
#pragma unroll
  for (int i = 0; i < 14; ++i) {
    int c = tid + i * 256, row = c / 56, ch = c % 56;
    ra[i] = *(const uint4*)(x + (m0 + row) * DM + ch * 8);
  }
  const int nc = tid & 7;
  const u16* wsrc = (nc < 4) ? wk : wv;
  const int cb = (nc & 3) * 8;
#pragma unroll
  for (int i = 0; i < 14; ++i) {
    int k = (tid >> 3) + i * 32;
    rb0[i] = *(const uint2*)(wsrc + k * DH + cb);
    rb1[i] = *(const uint2*)(wsrc + k * DH + ((cb < 24) ? cb + 4 : cb));
    rg[i] = gw[k];
  }
#pragma unroll
  for (int i = 0; i < 14; ++i) {
    int c = tid + i * 256, row = c / 56, ch = c % 56;
    *(uint4*)(as + row * SP_AST + ch * 8) = ra[i];
  }
#pragma unroll
  for (int i = 0; i < 14; ++i) {
    int k = (tid >> 3) + i * 32;
    uint4 v = make_uint4(rb0[i].x, rb0[i].y, rb1[i].x, rb1[i].y);
    if (cb == 24) { v.z = (nc == 7) ? (unsigned)rg[i] : 0u; v.w = 0u; }
    *(uint4*)(bs + k * SP_BST + nc * 8) = v;
  }
  __syncthreads();
  const int wm = wave >> 1, wn = wave & 1, r = lane & 31, h = lane >> 5;
  const int trq = (lane & 15) >> 2, trp = lane & 3, trblk = (lane >> 4) & 1;
  f32x16 a;
#pragma unroll
  for (int e = 0; e < 16; ++e) a[e] = 0.f;
#pragma unroll 4
  for (int ks = 0; ks < 28; ++ks) {
    bf16x8 af = *(const bf16x8*)(as + (wm * 32 + r) * SP_AST + ks * 16 + h * 8);
    const u16* p = bs + (ks * 16 + 8 * h + trq) * SP_BST + wn * 32 + 16 * trblk + 4 * trp;
    s16x4 lo = __builtin_amdgcn_ds_read_tr16_b64_v4i16((lds_s16x4_ptr)(p));
    s16x4 hi = __builtin_amdgcn_ds_read_tr16_b64_v4i16((lds_s16x4_ptr)(p + 4 * SP_BST));
    a = MFMA32(af, __builtin_shufflevector(lo, hi, 0, 1, 2, 3, 4, 5, 6, 7), a);
  }
  const int mb = m0 + wm * 32, d = r;
  if (wn == 0) {
    const int s = d >> 4, hh = (d >> 3) & 1, j = d & 7;
#pragma unroll
    for (int e = 0; e < 16; ++e) {
      int m = mb + (e & 3) + 8 * (e >> 2) + 4 * h;
      int tile = m >> 5, rr = m & 31;
      KV[tile * 2048 + s * 512 + (hh * 32 + rr) * 8 + j] = (d < DH) ? to_bf16(a[e]) : ((d < 31) ? (u16)0x3F80 : (u16)0);
    }
  } else {
#pragma unroll
    for (int e = 0; e < 16; ++e) {
      int m = mb + (e & 3) + 8 * (e >> 2) + 4 * h;
      int tile = m >> 5, kk = m & 31;
      int s = kk >> 4, k16 = kk & 15;
      int hh = (k16 >> 2) & 1, j = ((k16 >> 3) << 2) | (k16 & 3);
      u16 val = (d < DH) ? to_bf16(a[e]) : ((d == DH) ? (u16)0x3F80 : (u16)0);
      KV[tile * 2048 + 1024 + s * 512 + (hh * 32 + d) * 8 + j] = val;
      if (d == DH) gate[m] = 1.f / (1.f + __expf(-a[e]));
    }
  }
}

constexpr int O_NBUF = 8;

__global__ __launch_bounds__(256) void k_out(const u16* __restrict__ Ocat, const u16* __restrict__ woA,
                                             const u16* __restrict__ woB, u16* __restrict__ out) {
  __shared__ __attribute__((aligned(16))) char smem[O_NBUF * 16384];
  const int n0 = (blockIdx.x % 7) * 64, m0 = (blockIdx.x / 7) * 64;
  auto bptr = [&](int k, int nc) -> const u16* {
    return ((k < DM) ? (woA + k * DM) : (woB + (k - DM) * DM)) + n0 + nc;
  };
  auto epi = [&](int mb, int nbl, const f32x16& a, int r, int h) {
    const int n = n0 + nbl + r;
#pragma unroll
    for (int e = 0; e < 16; ++e) {
      int m = mb + (e & 3) + 8 * (e >> 2) + 4 * h;
      out[m * DM + n] = to_bf16(a[e]);
    }
  };
  gemm_glds<64, 64, 14, O_NBUF>(Ocat, OC, m0, smem, bptr, epi);
}

constexpr int TPC = 2;
constexpr int NCH = 16 / TPC;
constexpr int CH_U4 = 4 * TPC * 256;
constexpr int QD_OFF = 3 * CH_U4;
constexpr int BQ_OFF = QD_OFF + 8 * 64;
constexpr int BK_OFF = BQ_OFF + 8 * 64;
constexpr int FL_OFF = BK_OFF + 16 * 128;
constexpr int A_LDS_U4 = FL_OFF + 16;

__global__ __launch_bounds__(512) void k_attn(const u16* __restrict__ Qa, const u16* __restrict__ KV,
                                              const u16* __restrict__ QKVb, const float* __restrict__ gate,
                                              u16* __restrict__ Ocat) {
  __shared__ __attribute__((aligned(16))) uint4 lds[A_LDS_U4];
  const int tid = threadIdx.x, lane = tid & 63, wave = __builtin_amdgcn_readfirstlane(tid >> 6);
  const int rg = wave & 1, kq = wave >> 1;
  const int r = lane & 31, h = lane >> 5;
  const int pbase = (blockIdx.x >> 1) * 16 + (blockIdx.x & 1);
  const int rot = (blockIdx.x >> 3) & (NCH - 1);
  const uint4* KVg = (const uint4*)KV;
  volatile unsigned* flag = (volatile unsigned*)(lds + FL_OFF);

  auto issue2 = [&](int c, int slot) {
    const int cc = (c + rot) & (NCH - 1);
    const char* src = (const char*)(KVg + (kq * 16 + cc * TPC + rg) * 256);
    uint4* dst = lds + slot * CH_U4 + (kq * TPC + rg) * 256;
#pragma unroll
    for (int p = 0; p < 4; ++p) glds16(src + p * 1024 + (unsigned)(lane * 16), dst + p * 64);
  };
  const int l56 = lane < 56 ? lane : 55;
  STAMP(0);
  glds16((const char*)(Qa + (pbase + 2 * wave) * DM) + (unsigned)(l56 * 16), lds + QD_OFF + wave * 64);
  issue2(0, 0);
  issue2(1, 1);
  glds16((const char*)(QKVb + (pbase + 2 * wave) * NB3) + (unsigned)(l56 * 16), lds + BQ_OFF + wave * 64);
#pragma unroll
  for (int i = 0; i < 2; ++i) {
    int row = pbase - 14 + 2 * (wave * 2 + i);
    row = row < 0 ? (pbase & 1) : row;
    row = row > S_LEN - 1 ? S_LEN - 1 : row;
    const char* src = (const char*)(QKVb + row * NB3 + DM);
    const int l48 = lane < 48 ? lane : 47;
    glds16(src + (unsigned)(lane * 16), lds + BK_OFF + (wave * 2 + i) * 128);
    glds16(src + 1024 + (unsigned)(l48 * 16), lds + BK_OFF + (wave * 2 + i) * 128 + 64);
  }
  if (tid == 0) *flag = 0u;
  float gsel0, gsel1;
  {
    const int i0 = rg * 4 + (r >> 4), i1 = i0 + 2;
    float g0 = 0.f, g1 = 0.f;
#pragma unroll
    for (int i = 0; i < 8; ++i) {
      const float gv = gate[pbase + 2 * i];
      g0 = (i == i0) ? gv : g0; g1 = (i == i1) ? gv : g1;
    }
    gsel0 = g0; gsel1 = g1;
  }
  wait_vm<9>();
  raw_barrier();
  issue2(2, 2);
  STAMP(1);

  bf16x8 q0[2], q1[2];
#pragma unroll
  for (int b = 0; b < 2; ++b) {
    const int R = rg * 64 + b * 32 + r;
    const u16* qp = (const u16*)(lds + QD_OFF + (R >> 4) * 64) + (R & 15) * DH;
    uint2 qa = *(const uint2*)(qp + 8 * h), qb = *(const uint2*)(qp + 8 * h + 4);
    uint2 qc = *(const uint2*)(qp + 16 + 8 * h);
    uint2 qd = *(const uint2*)(qp + 20);
    if (h) qd = make_uint2(0, 0);
    q0[b] = __builtin_bit_cast(bf16x8, make_uint4(qa.x, qa.y, qb.x, qb.y));
    q1[b] = __builtin_bit_cast(bf16x8, make_uint4(qc.x, qc.y, qd.x, qd.y));
  }
  f32x16 zero;
#pragma unroll
  for (int e = 0; e < 16; ++e) zero[e] = 0.f;
  float mref[2];
  {
    bf16x8 k0 = __builtin_bit_cast(bf16x8, lds[lane]), k1 = __builtin_bit_cast(bf16x8, lds[64 + lane]);
#pragma unroll
    for (int b = 0; b < 2; ++b) {
      f32x16 s = MFMA32(k0, q0[b], zero);
      s = MFMA32(k1, q1[b], s);
      float m = s[0];
#pragma unroll
      for (int e = 1; e < 16; ++e) m = fmaxf(m, s[e]);
      mref[b] = fmaxf(m, __shfl_xor(m, 32));
    }
  }
  STAMP(2);

  f32x16 o0, o1;
#pragma nounroll
  for (int attempt = 0; attempt < 2; ++attempt) {
    bf16x8 q1m[2];
#pragma unroll
    for (int b = 0; b < 2; ++b) {
      q1m[b] = q1[b];
      const float n0 = -mref[b];
      const u16 c0 = to_bf16(n0); const float n1 = n0 - bf_to_f(c0);
      const u16 c1 = to_bf16(n1); const float n2 = n1 - bf_to_f(c1);
      const u16 c2 = to_bf16(n2);
      if (h) { q1m[b][4] = (short)c0; q1m[b][5] = (short)c1; q1m[b][6] = (short)c2; }
    }
#pragma unroll
    for (int e = 0; e < 16; ++e) { o0[e] = 0.f; o1[e] = 0.f; }
    if (attempt) {
      issue2(0, 0); issue2(1, 1);
      wait_vm<4>(); raw_barrier();
      issue2(2, 2);
    }
    auto softmax_pv = [&](const f32x16& s, const bf16x8& vf0, const bf16x8& vf1, f32x16& o) {
      uint4 pa, pb;
      pa.x = pack_bf16(EXP2(s[0]), EXP2(s[1]));
      pa.y = pack_bf16(EXP2(s[2]), EXP2(s[3]));
      pa.z = pack_bf16(EXP2(s[4]), EXP2(s[5]));
      pa.w = pack_bf16(EXP2(s[6]), EXP2(s[7]));
      o = MFMA32(vf0, __builtin_bit_cast(bf16x8, pa), o);
      pb.x = pack_bf16(EXP2(s[8]), EXP2(s[9]));
      pb.y = pack_bf16(EXP2(s[10]), EXP2(s[11]));
      pb.z = pack_bf16(EXP2(s[12]), EXP2(s[13]));
      pb.w = pack_bf16(EXP2(s[14]), EXP2(s[15]));
      o = MFMA32(vf1, __builtin_bit_cast(bf16x8, pb), o);
    };
    f32x16 sA, sB;
    bf16x8 kf0, kf1, vf0, vf1;
    {
      const uint4* base = lds + kq * TPC * 256 + lane;
      kf0 = __builtin_bit_cast(bf16x8, base[0]); kf1 = __builtin_bit_cast(bf16x8, base[64]);
      vf0 = __builtin_bit_cast(bf16x8, base[128]); vf1 = __builtin_bit_cast(bf16x8, base[192]);
      sA = MFMA32(kf0, q0[0], zero);
      sA = MFMA32(kf1, q1m[0], sA);
    }
    int slot = 0;
#pragma nounroll
    for (int c = 0; c < NCH; ++c) {
#pragma unroll
      for (int t = 0; t < TPC; ++t) {
        sB = MFMA32(kf0, q0[1], zero);
        sB = MFMA32(kf1, q1m[1], sB);
        const bool more = (t + 1 < TPC) || (c + 1 < NCH);
        if (t + 1 == TPC) {
          slot = (slot == 2) ? 0 : slot + 1;
          if (c + 1 < NCH) {
            if (c == 0 && attempt == 0) wait_vm<9>(); else if (c + 2 < NCH) wait_vm<4>(); else wait_vm<0>();
            raw_barrier();
            if (c + 3 < NCH) issue2(c + 3, slot == 0 ? 2 : slot - 1);
          }
        }
        bf16x8 nk0 = kf0, nk1 = kf1, nv0 = vf0, nv1 = vf1;
        if (more) {
          const uint4* base = lds + slot * CH_U4 + kq * TPC * 256 + ((t + 1) & (TPC - 1)) * 256 + lane;
          nk0 = __builtin_bit_cast(bf16x8, base[0]); nk1 = __builtin_bit_cast(bf16x8, base[64]);
          nv0 = __builtin_bit_cast(bf16x8, base[128]); nv1 = __builtin_bit_cast(bf16x8, base[192]);
        }
        __builtin_amdgcn_sched_barrier(0);
        softmax_pv(sA, vf0, vf1, o0);
        __builtin_amdgcn_sched_barrier(0);
        if (more) {
          sA = MFMA32(nk0, q0[0], zero);
          sA = MFMA32(nk1, q1m[0], sA);
        }
        __builtin_amdgcn_sched_barrier(0);
        softmax_pv(sB, vf0, vf1, o1);
        __builtin_amdgcn_sched_barrier(0);
        kf0 = nk0; kf1 = nk1; vf0 = nv0; vf1 = nv1;
      }
      if (attempt == 0 && c == 2 + (wave >> 1)) {
#pragma unroll
        for (int half = 0; half < 2; ++half) {
          const int pair = half * 64 + (tid >> 3), bc = tid & 7, bcc = (bc < 7) ? bc : 6;
          const int pi = pair >> 4, bhead = pair & 15, bpos = pbase + 2 * pi;
          const uint2 bq = *(const uint2*)((const u16*)(lds + BQ_OFF + pi * 64) + bhead * DH + 4 * bcc);
          const u16* kb = (const u16*)(lds + BK_OFF) + bhead * DH + 4 * bcc;
          float q4[4] = {__uint_as_float(bq.x << 16), __uint_as_float(bq.x & 0xffff0000u),
                         __uint_as_float(bq.y << 16), __uint_as_float(bq.y & 0xffff0000u)};
          float lg[8];
#pragma unroll
          for (int w = 0; w < 8; ++w) {
            const uint2 bk = *(const uint2*)(kb + (pi + 7 - w) * 1024);
            float t = q4[0] * __uint_as_float(bk.x << 16) + q4[1] * __uint_as_float(bk.x & 0xffff0000u) +
                      q4[2] * __uint_as_float(bk.y << 16) + q4[3] * __uint_as_float(bk.y & 0xffff0000u);
            lg[w] = (bc < 7) ? t : 0.f;
          }
#pragma unroll
          for (int w = 0; w < 8; ++w) {
            lg[w] += __shfl_xor(lg[w], 1); lg[w] += __shfl_xor(lg[w], 2); lg[w] += __shfl_xor(lg[w], 4);
          }
          float mx = -3.0e38f;
#pragma unroll
          for (int w = 0; w < 8; ++w) {
            lg[w] = (bpos - 2 * w >= 0) ? lg[w] * 0.18898223650461362f : -3.0e38f;
            mx = fmaxf(mx, lg[w]);
          }
          float sm = 0.f;
#pragma unroll
          for (int w = 0; w < 8; ++w) { lg[w] = (bpos - 2 * w >= 0) ? __expf(lg[w] - mx) : 0.f; sm += lg[w]; }
          float o4[4] = {0.f, 0.f, 0.f, 0.f};
#pragma unroll
          for (int w = 0; w < 8; ++w) {
            const uint2 bv = *(const uint2*)(kb + (pi + 7 - w) * 1024 + DM);
            o4[0] += lg[w] * __uint_as_float(bv.x << 16);
            o4[1] += lg[w] * __uint_as_float(bv.x & 0xffff0000u);
            o4[2] += lg[w] * __uint_as_float(bv.y << 16);
            o4[3] += lg[w] * __uint_as_float(bv.y & 0xffff0000u);
          }
          float gB = 0.f;
#pragma unroll
          for (int i = 0; i < 8; ++i) { const float gv = gate[pbase + 2 * i]; gB = (i == pi) ? gv : gB; }
          const float sc = (1.f - gB) / sm;
          if (bc < 7) {
            uint2 wv;
            wv.x = pack_bf16(o4[0] * sc, o4[1] * sc);
            wv.y = pack_bf16(o4[2] * sc, o4[3] * sc);
            *(uint2*)(Ocat + bpos * OC + DM + bhead * DH + 4 * bc) = wv;
          }
        }
      }
    }
    STAMP(3);
    float* red = (float*)lds;
    {
      float* dst = red + (kq >> 1) * (2 * CH_U4 * 4) + ((kq & 1) * 4 + rg * 2) * 1024 + lane;
#pragma unroll
      for (int e = 0; e < 16; ++e) { dst[e * 64] = o0[e]; dst[1024 + e * 64] = o1[e]; }
    }
    if (attempt == 0 && h && !(o0[12] < 1.0e18f && o1[12] < 1.0e18f)) *flag = 1u;
    raw_barrier();
    if (attempt == 1 || *flag == 0u) {
      const int rb = wave & 3, dq = (wave >> 2) * 2;
      float l = 0.f, ov[8];
#pragma unroll
      for (int i = 0; i < 8; ++i) ov[i] = 0.f;
#pragma unroll
      for (int q = 0; q < 4; ++q) {
        const float* src = red + (q >> 1) * (2 * CH_U4 * 4) + ((q & 1) * 4 + rb) * 1024;
        l += src[12 * 64 + 32 + r];
#pragma unroll
        for (int i = 0; i < 8; ++i) ov[i] += src[(4 * dq + i) * 64 + lane];
      }
      const int R = rb * 32 + r;
      const int pos = pbase + 2 * (R >> 4), head = R & 15;
      float gA = 0.f;
#pragma unroll
      for (int i = 0; i < 8; ++i) { const float gv = gate[pbase + 2 * i]; gA = (i == (R >> 4)) ? gv : gA; }
      const float sc = gA / l;
#pragma unroll
      for (int g = 0; g < 2; ++g) {
        const int d = 8 * (dq + g) + 4 * h;
        if (d < DH) {
          uint2 w;
          w.x = pack_bf16(ov[4 * g] * sc, ov[4 * g + 1] * sc);
          w.y = pack_bf16(ov[4 * g + 2] * sc, ov[4 * g + 3] * sc);
          *(uint2*)(Ocat + pos * OC + head * DH + d) = w;
        }
      }
      STAMP(4);
      break;
    }
    {
      float m[2] = {-3.0e38f, -3.0e38f};
      for (int t = 0; t < 16; ++t) {
        const uint4* src = KVg + (kq * 16 + t) * 256 + lane;
        bf16x8 k0 = __builtin_bit_cast(bf16x8, src[0]);
        bf16x8 k1 = __builtin_bit_cast(bf16x8, src[64]);
#pragma unroll
        for (int b = 0; b < 2; ++b) {
          f32x16 s = MFMA32(k0, q0[b], zero);
          s = MFMA32(k1, q1[b], s);
#pragma unroll
          for (int e = 0; e < 16; ++e) m[b] = fmaxf(m[b], s[e]);
        }
      }
      m[0] = fmaxf(m[0], __shfl_xor(m[0], 32));
      m[1] = fmaxf(m[1], __shfl_xor(m[1], 32));
      wait_vm<0>();
      raw_barrier();
      red[(wave * 2) * 64 + lane] = m[0];
      red[(wave * 2 + 1) * 64 + lane] = m[1];
      raw_barrier();
#pragma unroll
      for (int b = 0; b < 2; ++b) {
        float mm = red[(rg * 2 + b) * 64 + lane];
#pragma unroll
        for (int q = 1; q < 4; ++q) mm = fmaxf(mm, red[((q * 2 + rg) * 2 + b) * 64 + lane]);
        mref[b] = mm;
      }
      raw_barrier();
    }
  }
}

extern "C" void kernel_launch(void* const* d_in, const int* in_sizes, int n_in,
                              void* d_out, int out_size, void* d_ws, size_t ws_size,
                              hipStream_t stream) {
  const u16* x = (const u16*)d_in[0];
  const u16* wq = (const u16*)d_in[1];
  const u16* wk = (const u16*)d_in[2];
  const u16* wv = (const u16*)d_in[3];
  const u16* woA = (const u16*)d_in[4];
  const u16* wqkv = (const u16*)d_in[6];
  const u16* woB = (const u16*)d_in[7];
  const u16* gw = (const u16*)d_in[8];
  char* ws = (char*)d_ws;
  u16* Qa = (u16*)(ws);
  u16* KV = (u16*)(ws + (2u << 20));
  u16* QKVb = (u16*)(ws + (3u << 20));
  float* gate = (float*)(ws + (9u << 20));
  u16* Ocat = (u16*)(ws + (10u << 20));
  u16* out = (u16*)d_out;

  k_proj<<<dim3(256), 256, 0, stream>>>(x, wq, wk, wv, wqkv, gw, Qa, KV, QKVb, gate);
  k_attn<<<dim3(S_LEN / 8), 512, 0, stream>>>(Qa, KV, QKVb, gate, Ocat);
  k_out<<<dim3(224), 256, 0, stream>>>(Ocat, woA, woB, out);
}
```

```cpp
#include <hip/hip_runtime.h>
#include <hip/hip_bf16.h>
#include <stdint.h>

typedef __attribute__((ext_vector_type(8))) short bf16x8;
typedef __attribute__((ext_vector_type(4))) short s16x4;
typedef __attribute__((ext_vector_type(16))) float f32x16;
typedef __attribute__((ext_vector_type(2))) __bf16 bf2;
typedef __attribute__((address_space(3))) s16x4* lds_s16x4_ptr;
typedef unsigned short u16;

#define DI __device__ __forceinline__
#define STAMP(i) do {} while (0)
#define EXP2(x) __builtin_amdgcn_exp2f(x)
#define MFMA32(a, b, c) __builtin_amdgcn_mfma_f32_32x32x16_bf16((a), (b), (c), 0, 0, 0)

constexpr int S_LEN = 2048;
constexpr int DM = 448;
constexpr int NH = 16;
constexpr int DH = 28;
constexpr int NB3 = 1344;
constexpr int OC = 896;

DI unsigned pack_bf16(float a, float b) {
  bf2 v; v[0] = (__bf16)a; v[1] = (__bf16)b;
  return __builtin_bit_cast(unsigned, v);
}
DI u16 to_bf16(float a) { __bf16 v = (__bf16)a; return __builtin_bit_cast(u16, v); }
DI float bf_to_f(u16 a) { return __uint_as_float(((unsigned)a) << 16); }

template <int V> struct IC { static constexpr int value = V; };
template <int I, int N, class F> DI void static_for(F&& f) {
  if constexpr (I < N) { f(IC<I>{}); static_for<I + 1, N>(f); }
}
template <int N> DI void wait_vm() { asm volatile("s_waitcnt vmcnt(%0)" ::"n"(N) : "memory"); }
DI void raw_barrier() { asm volatile("s_waitcnt lgkmcnt(0)" ::: "memory"); __builtin_amdgcn_s_barrier(); asm volatile("" ::: "memory"); }
DI void glds16(const void* g, void* l) {
  __builtin_amdgcn_global_load_lds((const __attribute__((address_space(1))) unsigned*)g,
                                   (__attribute__((address_space(3))) unsigned*)l, 16, 0, 0);
}

DI void glds16a(const void* g, const void* l) {
  const unsigned la = __builtin_amdgcn_readfirstlane((unsigned)(size_t)l);
  unsigned keep;
  asm volatile("s_mov_b32 %0, m0\n\ts_mov_b32 m0, %2\n\ts_nop 0\n\tglobal_load_lds_dwordx4 %1, off\n\ts_mov_b32 m0, %0"
               : "=&s"(keep) : "v"(g), "s"(la) : "memory");
}

template <int BN> DI int bswz(int row) { return (BN == 128) ? ((row & 3) << 2) : (((row >> 1) & 1) << 2); }

template <int BM, int BN, int NS, int NBUF, class BPtr, class Epi>
DI void gemm_glds(const u16* __restrict__ A, int lda, int m0, char* smem, BPtr bptr, Epi epi) {
  constexpr int TM = BM / 64, TN = BN / 64;
  constexpr int GA = BM / 32, GB = BN / 32, G = GA + GB;
  constexpr int A_BYTES = BM * 128, B_BYTES = BN * 128, ST_BYTES = A_BYTES + B_BYTES;
  constexpr int CPR = BN / 8;
  const int tid = threadIdx.x, lane = tid & 63, wave = __builtin_amdgcn_readfirstlane(tid >> 6);
  const int wm = wave >> 1, wn = wave & 1;
  const int r = lane & 31, h = lane >> 5;

  const u16* asrc[GA];
#pragma unroll
  for (int i = 0; i < GA; ++i) {
    int idx = (wave * GA + i) * 64 + lane, row = idx >> 3, ch = (idx & 7) ^ ((row >> 1) & 7);
    asrc[i] = A + (size_t)(m0 + row) * lda + ch * 8;
  }
  int brow[GB], bch[GB];
#pragma unroll
  for (int i = 0; i < GB; ++i) {
    int idx = (wave * GB + i) * 64 + lane;
    brow[i] = idx / CPR;
    bch[i] = (idx % CPR) ^ bswz<BN>(brow[i]);
  }
  auto issue = [&](int st) {
    char* base = smem + (st % NBUF) * ST_BYTES;
#pragma unroll
    for (int i = 0; i < GA; ++i) glds16(asrc[i] + st * 64, base + (wave * GA + i) * 1024);
#pragma unroll
    for (int i = 0; i < GB; ++i) glds16(bptr(st * 64 + brow[i], bch[i] * 8), base + A_BYTES + (wave * GB + i) * 1024);
  };

  f32x16 acc[TM][TN];
#pragma unroll
  for (int i = 0; i < TM; ++i)
#pragma unroll
    for (int j = 0; j < TN; ++j)
#pragma unroll
      for (int e = 0; e < 16; ++e) acc[i][j][e] = 0.f;

  int aoff[TM], asw[TM];
#pragma unroll
  for (int i = 0; i < TM; ++i) {
    int row = wm * (BM / 2) + i * 32 + r;
    aoff[i] = row * 128; asw[i] = (row >> 1) & 7;
  }
  const int trq = (lane & 15) >> 2, trp = lane & 3, trblk = (lane >> 4) & 1;

#pragma unroll
  for (int st = 0; st < NBUF - 1; ++st) if (st < NS) issue(st);

  static_for<0, NS>([&](auto SI) {
    constexpr int st = decltype(SI)::value;
    constexpr int issued = (st + NBUF - 1 < NS) ? (st + NBUF - 1) : NS;
    wait_vm<G * (issued - st - 1)>();
    raw_barrier();
    if constexpr (st + NBUF - 1 < NS) issue(st + NBUF - 1);
    const char* as = smem + (st % NBUF) * ST_BYTES;
    const char* bs = as + A_BYTES;
#pragma unroll
    for (int ks = 0; ks < 4; ++ks) {
      bf16x8 af[TM], bfr[TN];
#pragma unroll
      for (int i = 0; i < TM; ++i) af[i] = *(const bf16x8*)(as + aoff[i] + (((2 * ks + h) ^ asw[i]) << 4));
      const int rk = ks * 16 + 8 * h + trq;
#pragma unroll
      for (int j = 0; j < TN; ++j) {
        const int col = wn * (BN / 2) + j * 32 + 16 * trblk + 4 * trp;
        const char* p = bs + rk * (BN * 2) + (((col >> 3) ^ bswz<BN>(rk)) << 4) + ((trp & 1) << 3);
        s16x4 lo = __builtin_amdgcn_ds_read_tr16_b64_v4i16((lds_s16x4_ptr)(p));
        s16x4 hi = __builtin_amdgcn_ds_read_tr16_b64_v4i16((lds_s16x4_ptr)(p + 4 * BN * 2 + (((((col >> 3) ^ bswz<BN>(rk + 4)) - ((col >> 3) ^ bswz<BN>(rk)))) << 4)));
        bfr[j] = __builtin_shufflevector(lo, hi, 0, 1, 2, 3, 4, 5, 6, 7);
      }
#pragma unroll
      for (int i = 0; i < TM; ++i)
#pragma unroll
        for (int j = 0; j < TN; ++j) acc[i][j] = MFMA32(af[i], bfr[j], acc[i][j]);
    }
  });
#pragma unroll
  for (int i = 0; i < TM; ++i)
#pragma unroll
    for (int j = 0; j < TN; ++j)
      epi(m0 + wm * (BM / 2) + i * 32, wn * (BN / 2) + j * 32, acc[i][j], r, h);
}

constexpr int P_NBUF = 4;
constexpr int SP_AST = 456, SP_BST = 96;
constexpr int P_SMEM = (64 * SP_AST + 448 * SP_BST) * 2;

__global__ __launch_bounds__(256) void k_proj(const u16* __restrict__ x, const u16* __restrict__ wq,
                                              const u16* __restrict__ wk, const u16* __restrict__ wv,
                                              const u16* __restrict__ wqkv, const u16* __restrict__ gw,
                                              u16* __restrict__ Qa, u16* __restrict__ KV,
                                              u16* __restrict__ QKVb, float* __restrict__ gate) {
  __shared__ __attribute__((aligned(16))) char smem[P_SMEM];
  const int bid = blockIdx.x;
  if (bid < 224) {
    const int n0 = (bid % 14) * 128, m0 = (bid / 14) * 128;
    auto bptr = [&](int k, int nc) -> const u16* {
      const int n = n0 + nc;
      return (n < DM) ? (wq + k * DM + n) : (wqkv + k * NB3 + (n - DM));
    };
    const float qscale = 0.18898223650461362f * 1.4426950408889634f;
    auto epi = [&](int mb, int nbl, const f32x16& a, int r, int h) {
      const int nb = n0 + nbl, n = nb + r;
      if (nb < DM) {
#pragma unroll
        for (int e = 0; e < 16; ++e) {
          int m = mb + (e & 3) + 8 * (e >> 2) + 4 * h;
          Qa[m * DM + n] = to_bf16(a[e] * qscale);
        }
      } else {
#pragma unroll
        for (int e = 0; e < 16; ++e) {
          int m = mb + (e & 3) + 8 * (e >> 2) + 4 * h;
          QKVb[m * NB3 + (n - DM)] = to_bf16(a[e]);
        }
      }
    };
    gemm_glds<128, 128, 7, P_NBUF>(x, DM, m0, smem, bptr, epi);
    return;
  }
  const int m0 = (bid - 224) * 64;
  const int tid = threadIdx.x, lane = tid & 63, wave = tid >> 6;
  u16* as = (u16*)smem;
  u16* bs = as + 64 * SP_AST;
  uint4 ra[14]; uint2 rb0[14], rb1[14]; u16 rg[14];
#pragma unroll
  for (int i = 0; i < 14; ++i) {
    int c = tid + i * 256, row = c / 56, ch = c % 56;
    ra[i] = *(const uint4*)(x + (m0 + row) * DM + ch * 8);
  }
  const int nc = tid & 7;
  const u16* wsrc = (nc < 4) ? wk : wv;
  const int cb = (nc & 3) * 8;
#pragma unroll
  for (int i = 0; i < 14; ++i) {
    int k = (tid >> 3) + i * 32;
    rb0[i] = *(const uint2*)(wsrc + k * DH + cb);
    rb1[i] = *(const uint2*)(wsrc + k * DH + ((cb < 24) ? cb + 4 : cb));
    rg[i] = gw[k];
  }
#pragma unroll
  for (int i = 0; i < 14; ++i) {
    int c = tid + i * 256, row = c / 56, ch = c % 56;
    *(uint4*)(as + row * SP_AST + ch * 8) = ra[i];
  }
#pragma unroll
  for (int i = 0; i < 14; ++i) {
    int k = (tid >> 3) + i * 32;
    uint4 v = make_uint4(rb0[i].x, rb0[i].y, rb1[i].x, rb1[i].y);
    if (cb == 24) { v.z = (nc == 7) ? (unsigned)rg[i] : 0u; v.w = 0u; }
    *(uint4*)(bs + k * SP_BST + nc * 8) = v;
  }
  __syncthreads();
  const int wm = wave >> 1, wn = wave & 1, r = lane & 31, h = lane >> 5;
  const int trq = (lane & 15) >> 2, trp = lane & 3, trblk = (lane >> 4) & 1;
  f32x16 a;
#pragma unroll
  for (int e = 0; e < 16; ++e) a[e] = 0.f;
#pragma unroll 4
  for (int ks = 0; ks < 28; ++ks) {
    bf16x8 af = *(const bf16x8*)(as + (wm * 32 + r) * SP_AST + ks * 16 + h * 8);
    const u16* p = bs + (ks * 16 + 8 * h + trq) * SP_BST + wn * 32 + 16 * trblk + 4 * trp;
    s16x4 lo = __builtin_amdgcn_ds_read_tr16_b64_v4i16((lds_s16x4_ptr)(p));
    s16x4 hi = __builtin_amdgcn_ds_read_tr16_b64_v4i16((lds_s16x4_ptr)(p + 4 * SP_BST));
    a = MFMA32(af, __builtin_shufflevector(lo, hi, 0, 1, 2, 3, 4, 5, 6, 7), a);
  }
  const int mb = m0 + wm * 32, d = r;
  if (wn == 0) {
    const int s = d >> 4, hh = (d >> 3) & 1, j = d & 7;
#pragma unroll
    for (int e = 0; e < 16; ++e) {
      int m = mb + (e & 3) + 8 * (e >> 2) + 4 * h;
      int tile = m >> 5, rr = m & 31;
      KV[tile * 2048 + s * 512 + (hh * 32 + rr) * 8 + j] = (d < DH) ? to_bf16(a[e]) : ((d < 31) ? (u16)0x3F80 : (u16)0);
    }
  } else {
#pragma unroll
    for (int e = 0; e < 16; ++e) {
      int m = mb + (e & 3) + 8 * (e >> 2) + 4 * h;
      int tile = m >> 5, kk = m & 31;
      int s = kk >> 4, k16 = kk & 15;
      int hh = (k16 >> 2) & 1, j = ((k16 >> 3) << 2) | (k16 & 3);
      u16 val = (d < DH) ? to_bf16(a[e]) : ((d == DH) ? (u16)0x3F80 : (u16)0);
      KV[tile * 2048 + 1024 + s * 512 + (hh * 32 + d) * 8 + j] = val;
      if (d == DH) gate[m] = 1.f / (1.f + __expf(-a[e]));
    }
  }
}

constexpr int O_NBUF = 8;

__global__ __launch_bounds__(256) void k_out(const u16* __restrict__ Ocat, const u16* __restrict__ woA,
                                             const u16* __restrict__ woB, u16* __restrict__ out) {
  __shared__ __attribute__((aligned(16))) char smem[O_NBUF * 16384];
  const int n0 = (blockIdx.x % 7) * 64, m0 = (blockIdx.x / 7) * 64;
  auto bptr = [&](int k, int nc) -> const u16* {
    return ((k < DM) ? (woA + k * DM) : (woB + (k - DM) * DM)) + n0 + nc;
  };
  auto epi = [&](int mb, int nbl, const f32x16& a, int r, int h) {
    const int n = n0 + nbl + r;
#pragma unroll
    for (int e = 0; e < 16; ++e) {
      int m = mb + (e & 3) + 8 * (e >> 2) + 4 * h;
      out[m * DM + n] = to_bf16(a[e]);
    }
  };
  gemm_glds<64, 64, 14, O_NBUF>(Ocat, OC, m0, smem, bptr, epi);
}

constexpr int TPC = 2;
constexpr int NCH = 16 / TPC;
constexpr int CH_U4 = 4 * TPC * 256;
constexpr int QD_OFF = 3 * CH_U4;
constexpr int BQ_OFF = QD_OFF + 8 * 64;
constexpr int BK_OFF = BQ_OFF + 8 * 64;
constexpr int FL_OFF = BK_OFF + 16 * 128;
constexpr int A_LDS_U4 = FL_OFF + 16;

__global__ __launch_bounds__(512) void k_attn(const u16* __restrict__ Qa, const u16* __restrict__ KV,
                                              const u16* __restrict__ QKVb, const float* __restrict__ gate,
                                              u16* __restrict__ Ocat) {
  __shared__ __attribute__((aligned(16))) uint4 lds[A_LDS_U4];
  const int tid = threadIdx.x, lane = tid & 63, wave = __builtin_amdgcn_readfirstlane(tid >> 6);
  const int rg = wave & 1, kq = wave >> 1;
  const int r = lane & 31, h = lane >> 5;
  const int pbase = (blockIdx.x >> 1) * 16 + (blockIdx.x & 1);
  const int rot = (blockIdx.x >> 3) & (NCH - 1);
  const uint4* KVg = (const uint4*)KV;
  __attribute__((address_space(3))) volatile unsigned* flag = (__attribute__((address_space(3))) volatile unsigned*)(lds + FL_OFF);

  auto issue2 = [&](int c, int slot) {
    const int cc = (c + rot) & (NCH - 1);
    const char* src = (const char*)(KVg + (kq * 16 + cc * TPC + rg) * 256);
    uint4* dst = lds + slot * CH_U4 + (kq * TPC + rg) * 256;
#pragma unroll
    for (int p = 0; p < 4; ++p) glds16a(src + p * 1024 + (unsigned)(lane * 16), dst + p * 64);
  };
  const int l56 = lane < 56 ? lane : 55;
  STAMP(0);
  glds16a((const char*)(Qa + (pbase + 2 * wave) * DM) + (unsigned)(l56 * 16), lds + QD_OFF + wave * 64);
  issue2(0, 0);
  issue2(1, 1);
  glds16a((const char*)(QKVb + (pbase + 2 * wave) * NB3) + (unsigned)(l56 * 16), lds + BQ_OFF + wave * 64);
#pragma unroll
  for (int i = 0; i < 2; ++i) {
    int row = pbase - 14 + 2 * (wave * 2 + i);
    row = row < 0 ? (pbase & 1) : row;
    row = row > S_LEN - 1 ? S_LEN - 1 : row;
    const char* src = (const char*)(QKVb + row * NB3 + DM);
    const int l48 = lane < 48 ? lane : 47;
    glds16a(src + (unsigned)(lane * 16), lds + BK_OFF + (wave * 2 + i) * 128);
    glds16a(src + 1024 + (unsigned)(l48 * 16), lds + BK_OFF + (wave * 2 + i) * 128 + 64);
  }
  if (tid == 0) *flag = 0u;
  wait_vm<9>();
  raw_barrier();
  issue2(2, 2);
  STAMP(1);

  bf16x8 q0[2], q1[2];
#pragma unroll
  for (int b = 0; b < 2; ++b) {
    const int R = rg * 64 + b * 32 + r;
    const u16* qp = (const u16*)(lds + QD_OFF + (R >> 4) * 64) + (R & 15) * DH;
    uint2 qa = *(const uint2*)(qp + 8 * h), qb = *(const uint2*)(qp + 8 * h + 4);
    uint2 qc = *(const uint2*)(qp + 16 + 8 * h);
    uint2 qd = *(const uint2*)(qp + 20);
    if (h) qd = make_uint2(0, 0);
    q0[b] = __builtin_bit_cast(bf16x8, make_uint4(qa.x, qa.y, qb.x, qb.y));
    q1[b] = __builtin_bit_cast(bf16x8, make_uint4(qc.x, qc.y, qd.x, qd.y));
  }
  f32x16 zero;
#pragma unroll
  for (int e = 0; e < 16; ++e) zero[e] = 0.f;
  float mref[2];
  {
    bf16x8 k0 = __builtin_bit_cast(bf16x8, lds[lane]), k1 = __builtin_bit_cast(bf16x8, lds[64 + lane]);
#pragma unroll
    for (int b = 0; b < 2; ++b) {
      f32x16 s = MFMA32(k0, q0[b], zero);
      s = MFMA32(k1, q1[b], s);
      float m = s[0];
#pragma unroll
      for (int e = 1; e < 16; ++e) m = fmaxf(m, s[e]);
      mref[b] = fmaxf(m, __shfl_xor(m, 32));
    }
  }
  STAMP(2);

  auto branch_b = [&]() {
#pragma unroll
    for (int half = 0; half < 2; ++half) {
      const int pair = half * 64 + (tid >> 3), bc = tid & 7, bcc = (bc < 7) ? bc : 6;
      const int pi = pair >> 4, bhead = pair & 15, bpos = pbase + 2 * pi;
      const uint2 bq = *(const uint2*)((const u16*)(lds + BQ_OFF + pi * 64) + bhead * DH + 4 * bcc);
      const u16* kb = (const u16*)(lds + BK_OFF) + bhead * DH + 4 * bcc + (pi + 7) * 1024;
      uint2 bk[8], bv[8];
#pragma unroll
      for (int w = 0; w < 8; ++w) { bk[w] = *(const uint2*)(kb - w * 1024); bv[w] = *(const uint2*)(kb - w * 1024 + DM); }
      __builtin_amdgcn_sched_barrier(0);
      float q4[4] = {__uint_as_float(bq.x << 16), __uint_as_float(bq.x & 0xffff0000u),
                     __uint_as_float(bq.y << 16), __uint_as_float(bq.y & 0xffff0000u)};
      float lg[8];
#pragma unroll
      for (int w = 0; w < 8; ++w) {
        float t = q4[0] * __uint_as_float(bk[w].x << 16) + q4[1] * __uint_as_float(bk[w].x & 0xffff0000u) +
                  q4[2] * __uint_as_float(bk[w].y << 16) + q4[3] * __uint_as_float(bk[w].y & 0xffff0000u);
        lg[w] = (bc < 7) ? t : 0.f;
      }
#pragma unroll
      for (int w = 0; w < 8; ++w) lg[w] += __shfl_xor(lg[w], 1);
#pragma unroll
      for (int w = 0; w < 8; ++w) lg[w] += __shfl_xor(lg[w], 2);
#pragma unroll
      for (int w = 0; w < 8; ++w) lg[w] += __shfl_xor(lg[w], 4);
      float mx = -3.0e38f;
#pragma unroll
      for (int w = 0; w < 8; ++w) {
        lg[w] = (bpos - 2 * w >= 0) ? lg[w] * 0.18898223650461362f : -3.0e38f;
        mx = fmaxf(mx, lg[w]);
      }
      float sm = 0.f;
#pragma unroll
      for (int w = 0; w < 8; ++w) { lg[w] = (bpos - 2 * w >= 0) ? __expf(lg[w] - mx) : 0.f; sm += lg[w]; }
      float o4[4] = {0.f, 0.f, 0.f, 0.f};
#pragma unroll
      for (int w = 0; w < 8; ++w) {
        o4[0] += lg[w] * __uint_as_float(bv[w].x << 16);
        o4[1] += lg[w] * __uint_as_float(bv[w].x & 0xffff0000u);
        o4[2] += lg[w] * __uint_as_float(bv[w].y << 16);
        o4[3] += lg[w] * __uint_as_float(bv[w].y & 0xffff0000u);
      }
      float gB = 0.f;
#pragma unroll
      for (int i = 0; i < 8; ++i) { const float gv = gate[pbase + 2 * i]; gB = (i == pi) ? gv : gB; }
      const float sc = (1.f - gB) / sm;
      if (bc < 7) {
        uint2 wv;
        wv.x = pack_bf16(o4[0] * sc, o4[1] * sc);
        wv.y = pack_bf16(o4[2] * sc, o4[3] * sc);
        *(uint2*)(Ocat + bpos * OC + DM + bhead * DH + 4 * bc) = wv;
      }
    }
  };

  f32x16 o0, o1;
#pragma nounroll
  for (int attempt = 0; attempt < 2; ++attempt) {
    bf16x8 q1m[2];
#pragma unroll
    for (int b = 0; b < 2; ++b) {
      q1m[b] = q1[b];
      const float n0 = -mref[b];
      const u16 c0 = to_bf16(n0); const float n1 = n0 - bf_to_f(c0);
      const u16 c1 = to_bf16(n1); const float n2 = n1 - bf_to_f(c1);
      const u16 c2 = to_bf16(n2);
      if (h) { q1m[b][4] = (short)c0; q1m[b][5] = (short)c1; q1m[b][6] = (short)c2; }
    }
#pragma unroll
    for (int e = 0; e < 16; ++e) { o0[e] = 0.f; o1[e] = 0.f; }
    if (attempt) {
      issue2(0, 0); issue2(1, 1);
      wait_vm<4>(); raw_barrier();
      issue2(2, 2);
    }
    auto softmax_pv = [&](const f32x16& s, const bf16x8& vf0, const bf16x8& vf1, f32x16& o) {
      uint4 pa, pb;
      pa.x = pack_bf16(EXP2(s[0]), EXP2(s[1]));
      pa.y = pack_bf16(EXP2(s[2]), EXP2(s[3]));
      pa.z = pack_bf16(EXP2(s[4]), EXP2(s[5]));
      pa.w = pack_bf16(EXP2(s[6]), EXP2(s[7]));
      o = MFMA32(vf0, __builtin_bit_cast(bf16x8, pa), o);
      pb.x = pack_bf16(EXP2(s[8]), EXP2(s[9]));
      pb.y = pack_bf16(EXP2(s[10]), EXP2(s[11]));
      pb.z = pack_bf16(EXP2(s[12]), EXP2(s[13]));
      pb.w = pack_bf16(EXP2(s[14]), EXP2(s[15]));
      o = MFMA32(vf1, __builtin_bit_cast(bf16x8, pb), o);
    };
    f32x16 sA, sB;
    bf16x8 kf0, kf1, vf0, vf1;
    {
      const uint4* base = lds + kq * TPC * 256 + lane;
      kf0 = __builtin_bit_cast(bf16x8, base[0]); kf1 = __builtin_bit_cast(bf16x8, base[64]);
      vf0 = __builtin_bit_cast(bf16x8, base[128]); vf1 = __builtin_bit_cast(bf16x8, base[192]);
      sA = MFMA32(kf0, q0[0], zero);
      sA = MFMA32(kf1, q1m[0], sA);
    }
    int slot = 0;
#pragma nounroll
    for (int c = 0; c < NCH; ++c) {
      if (attempt == 0 && c == 3 && wave < 4) branch_b();
#pragma unroll
      for (int t = 0; t < TPC; ++t) {
        sB = MFMA32(kf0, q0[1], zero);
        sB = MFMA32(kf1, q1m[1], sB);
        const bool more = (t + 1 < TPC) || (c + 1 < NCH);
        if (t + 1 == TPC) {
          slot = (slot == 2) ? 0 : slot + 1;
          if (c + 1 < NCH) {
            if (c == 0 && attempt == 0) wait_vm<9>(); else if (c + 2 < NCH) wait_vm<4>(); else wait_vm<0>();
            raw_barrier();
            if (c + 3 < NCH) issue2(c + 3, slot == 0 ? 2 : slot - 1);
          }
        }
        bf16x8 nk0 = kf0, nk1 = kf1, nv0 = vf0, nv1 = vf1;
        if (more) {
          const uint4* base = lds + slot * CH_U4 + kq * TPC * 256 + ((t + 1) & (TPC - 1)) * 256 + lane;
          nk0 = __builtin_bit_cast(bf16x8, base[0]); nk1 = __builtin_bit_cast(bf16x8, base[64]);
          nv0 = __builtin_bit_cast(bf16x8, base[128]); nv1 = __builtin_bit_cast(bf16x8, base[192]);
        }
        __builtin_amdgcn_sched_barrier(0);
        softmax_pv(sA, vf0, vf1, o0);
        __builtin_amdgcn_sched_barrier(0);
        if (more) {
          sA = MFMA32(nk0, q0[0], zero);
          sA = MFMA32(nk1, q1m[0], sA);
        }
        __builtin_amdgcn_sched_barrier(0);
        softmax_pv(sB, vf0, vf1, o1);
        __builtin_amdgcn_sched_barrier(0);
        kf0 = nk0; kf1 = nk1; vf0 = nv0; vf1 = nv1;
      }
      if (attempt == 0 && c == 3 && wave >= 4) branch_b();
    }
    STAMP(3);
    float* red = (float*)lds;
    {
      float* dst = red + (kq >> 1) * (2 * CH_U4 * 4) + ((kq & 1) * 4 + rg * 2) * 1024 + lane;
#pragma unroll
      for (int e = 0; e < 16; ++e) { dst[e * 64] = o0[e]; dst[1024 + e * 64] = o1[e]; }
    }
    if (attempt == 0 && h && !(o0[12] < 1.0e18f && o1[12] < 1.0e18f)) *flag = 1u;
    raw_barrier();
    if (attempt == 1 || *flag == 0u) {
      const int rb = wave & 3, dq = (wave >> 2) * 2;
      float l = 0.f, ov[8];
#pragma unroll
      for (int i = 0; i < 8; ++i) ov[i] = 0.f;
#pragma unroll
      for (int q = 0; q < 4; ++q) {
        const float* src = red + (q >> 1) * (2 * CH_U4 * 4) + ((q & 1) * 4 + rb) * 1024;
        l += src[12 * 64 + 32 + r];
#pragma unroll
        for (int i = 0; i < 8; ++i) ov[i] += src[(4 * dq + i) * 64 + lane];
      }
      const int R = rb * 32 + r;
      const int pos = pbase + 2 * (R >> 4), head = R & 15;
      float gA = 0.f;
#pragma unroll
      for (int i = 0; i < 8; ++i) { const float gv = gate[pbase + 2 * i]; gA = (i == (R >> 4)) ? gv : gA; }
      const float sc = gA / l;
#pragma unroll
      for (int g = 0; g < 2; ++g) {
        const int d = 8 * (dq + g) + 4 * h;
        if (d < DH) {
          uint2 w;
          w.x = pack_bf16(ov[4 * g] * sc, ov[4 * g + 1] * sc);
          w.y = pack_bf16(ov[4 * g + 2] * sc, ov[4 * g + 3] * sc);
          *(uint2*)(Ocat + pos * OC + head * DH + d) = w;
        }
      }
      STAMP(4);
      break;
    }
    {
      float m[2] = {-3.0e38f, -3.0e38f};
      for (int t = 0; t < 16; ++t) {
        const uint4* src = KVg + (kq * 16 + t) * 256 + lane;
        bf16x8 k0 = __builtin_bit_cast(bf16x8, src[0]);
        bf16x8 k1 = __builtin_bit_cast(bf16x8, src[64]);
#pragma unroll
        for (int b = 0; b < 2; ++b) {
          f32x16 s = MFMA32(k0, q0[b], zero);
          s = MFMA32(k1, q1[b], s);
#pragma unroll
          for (int e = 0; e < 16; ++e) m[b] = fmaxf(m[b], s[e]);
        }
      }
      m[0] = fmaxf(m[0], __shfl_xor(m[0], 32));
      m[1] = fmaxf(m[1], __shfl_xor(m[1], 32));
      wait_vm<0>();
      raw_barrier();
      red[(wave * 2) * 64 + lane] = m[0];
      red[(wave * 2 + 1) * 64 + lane] = m[1];
      raw_barrier();
#pragma unroll
      for (int b = 0; b < 2; ++b) {
        float mm = red[(rg * 2 + b) * 64 + lane];
#pragma unroll
        for (int q = 1; q < 4; ++q) mm = fmaxf(mm, red[((q * 2 + rg) * 2 + b) * 64 + lane]);
        mref[b] = mm;
      }
      raw_barrier();
    }
  }
}

extern "C" void kernel_launch(void* const* d_in, const int* in_sizes, int n_in,
                              void* d_out, int out_size, void* d_ws, size_t ws_size,
                              hipStream_t stream) {
  const u16* x = (const u16*)d_in[0];
  const u16* wq = (const u16*)d_in[1];
  const u16* wk = (const u16*)d_in[2];
  const u16* wv = (const u16*)d_in[3];
  const u16* woA = (const u16*)d_in[4];
  const u16* wqkv = (const u16*)d_in[6];
  const u16* woB = (const u16*)d_in[7];
  const u16* gw = (const u16*)d_in[8];
  char* ws = (char*)d_ws;
  u16* Qa = (u16*)(ws);
  u16* KV = (u16*)(ws + (2u << 20));
  u16* QKVb = (u16*)(ws + (3u << 20));
  float* gate = (float*)(ws + (9u << 20));
  u16* Ocat = (u16*)(ws + (10u << 20));
  u16* out = (u16*)d_out;

  k_proj<<<dim3(256), 256, 0, stream>>>(x, wq, wk, wv, wqkv, gw, Qa, KV, QKVb, gate);
  k_attn<<<dim3(S_LEN / 8), 512, 0, stream>>>(Qa, KV, QKVb, gate, Ocat);
  k_out<<<dim3(224), 256, 0, stream>>>(Ocat, woA, woB, out);
}
```

```cpp
#include <hip/hip_runtime.h>
#include <hip/hip_bf16.h>
#include <stdint.h>

typedef __attribute__((ext_vector_type(8))) short bf16x8;
typedef __attribute__((ext_vector_type(4))) short s16x4;
typedef __attribute__((ext_vector_type(16))) float f32x16;
typedef __attribute__((ext_vector_type(2))) __bf16 bf2;
typedef __attribute__((address_space(3))) s16x4* lds_s16x4_ptr;
typedef unsigned short u16;

#define DI __device__ __forceinline__
#define STAMP(i) do {} while (0)
#define EXP2(x) __builtin_amdgcn_exp2f(x)
#define MFMA32(a, b, c) __builtin_amdgcn_mfma_f32_32x32x16_bf16((a), (b), (c), 0, 0, 0)

constexpr int S_LEN = 2048;
constexpr int DM = 448;
constexpr int NH = 16;
constexpr int DH = 28;
constexpr int NB3 = 1344;
constexpr int OC = 896;

DI unsigned pack_bf16(float a, float b) {
  bf2 v; v[0] = (__bf16)a; v[1] = (__bf16)b;
  return __builtin_bit_cast(unsigned, v);
}
DI u16 to_bf16(float a) { __bf16 v = (__bf16)a; return __builtin_bit_cast(u16, v); }
DI float bf_to_f(u16 a) { return __uint_as_float(((unsigned)a) << 16); }

template <int V> struct IC { static constexpr int value = V; };
template <int I, int N, class F> DI void static_for(F&& f) {
  if constexpr (I < N) { f(IC<I>{}); static_for<I + 1, N>(f); }
}
template <int N> DI void wait_vm() { asm volatile("s_waitcnt vmcnt(%0)" ::"n"(N) : "memory"); }
DI void raw_barrier() { asm volatile("s_waitcnt lgkmcnt(0)" ::: "memory"); __builtin_amdgcn_s_barrier(); asm volatile("" ::: "memory"); }
DI void glds16(const void* g, void* l) {
  __builtin_amdgcn_global_load_lds((const __attribute__((address_space(1))) unsigned*)g,
                                   (__attribute__((address_space(3))) unsigned*)l, 16, 0, 0);
}

DI void glds16a(const void* g, const void* l) {
  const unsigned la = __builtin_amdgcn_readfirstlane((unsigned)(size_t)l);
  unsigned keep;
  asm volatile("s_mov_b32 %0, m0\n\ts_mov_b32 m0, %2\n\ts_nop 0\n\tglobal_load_lds_dwordx4 %1, off\n\ts_mov_b32 m0, %0"
               : "=&s"(keep) : "v"(g), "s"(la) : "memory");
}

template <int BN> DI int bswz(int row) { return (BN == 128) ? ((row & 3) << 2) : (((row >> 1) & 1) << 2); }

template <int BM, int BN, int NS, int NBUF, class BPtr, class Epi>
DI void gemm_glds(const u16* __restrict__ A, int lda, int m0, char* smem, BPtr bptr, Epi epi) {
  constexpr int TM = BM / 64, TN = BN / 64;
  constexpr int GA = BM / 32, GB = BN / 32, G = GA + GB;
  constexpr int A_BYTES = BM * 128, B_BYTES = BN * 128, ST_BYTES = A_BYTES + B_BYTES;
  constexpr int CPR = BN / 8;
  const int tid = threadIdx.x, lane = tid & 63, wave = __builtin_amdgcn_readfirstlane(tid >> 6);
  const int wm = wave >> 1, wn = wave & 1;
  const int r = lane & 31, h = lane >> 5;

  const u16* asrc[GA];
#pragma unroll
  for (int i = 0; i < GA; ++i) {
    int idx = (wave * GA + i) * 64 + lane, row = idx >> 3, ch = (idx & 7) ^ ((row >> 1) & 7);
    asrc[i] = A + (size_t)(m0 + row) * lda + ch * 8;
  }
  int brow[GB], bch[GB];
#pragma unroll
  for (int i = 0; i < GB; ++i) {
    int idx = (wave * GB + i) * 64 + lane;
    brow[i] = idx / CPR;
    bch[i] = (idx % CPR) ^ bswz<BN>(brow[i]);
  }
  auto issue = [&](int st) {
    char* base = smem + (st % NBUF) * ST_BYTES;
#pragma unroll
    for (int i = 0; i < GA; ++i) glds16(asrc[i] + st * 64, base + (wave * GA + i) * 1024);
#pragma unroll
    for (int i = 0; i < GB; ++i) glds16(bptr(st * 64 + brow[i], bch[i] * 8), base + A_BYTES + (wave * GB + i) * 1024);
  };

  f32x16 acc[TM][TN];
#pragma unroll
  for (int i = 0; i < TM; ++i)
#pragma unroll
    for (int j = 0; j < TN; ++j)
#pragma unroll
      for (int e = 0; e < 16; ++e) acc[i][j][e] = 0.f;

  int aoff[TM], asw[TM];
#pragma unroll
  for (int i = 0; i < TM; ++i) {
    int row = wm * (BM / 2) + i * 32 + r;
    aoff[i] = row * 128; asw[i] = (row >> 1) & 7;
  }
  const int trq = (lane & 15) >> 2, trp = lane & 3, trblk = (lane >> 4) & 1;

#pragma unroll
  for (int st = 0; st < NBUF - 1; ++st) if (st < NS) issue(st);

  static_for<0, NS>([&](auto SI) {
    constexpr int st = decltype(SI)::value;
    constexpr int issued = (st + NBUF - 1 < NS) ? (st + NBUF - 1) : NS;
    wait_vm<G * (issued - st - 1)>();
    raw_barrier();
    if constexpr (st + NBUF - 1 < NS) issue(st + NBUF - 1);
    const char* as = smem + (st % NBUF) * ST_BYTES;
    const char* bs = as + A_BYTES;
#pragma unroll
    for (int ks = 0; ks < 4; ++ks) {
      bf16x8 af[TM], bfr[TN];
#pragma unroll
      for (int i = 0; i < TM; ++i) af[i] = *(const bf16x8*)(as + aoff[i] + (((2 * ks + h) ^ asw[i]) << 4));
      const int rk = ks * 16 + 8 * h + trq;
#pragma unroll
      for (int j = 0; j < TN; ++j) {
        const int col = wn * (BN / 2) + j * 32 + 16 * trblk + 4 * trp;
        const char* p = bs + rk * (BN * 2) + (((col >> 3) ^ bswz<BN>(rk)) << 4) + ((trp & 1) << 3);
        s16x4 lo = __builtin_amdgcn_ds_read_tr16_b64_v4i16((lds_s16x4_ptr)(p));
        s16x4 hi = __builtin_amdgcn_ds_read_tr16_b64_v4i16((lds_s16x4_ptr)(p + 4 * BN * 2 + (((((col >> 3) ^ bswz<BN>(rk + 4)) - ((col >> 3) ^ bswz<BN>(rk)))) << 4)));
        bfr[j] = __builtin_shufflevector(lo, hi, 0, 1, 2, 3, 4, 5, 6, 7);
      }
#pragma unroll
      for (int i = 0; i < TM; ++i)
#pragma unroll
        for (int j = 0; j < TN; ++j) acc[i][j] = MFMA32(af[i], bfr[j], acc[i][j]);
    }
  });
#pragma unroll
  for (int i = 0; i < TM; ++i)
#pragma unroll
    for (int j = 0; j < TN; ++j)
      epi(m0 + wm * (BM / 2) + i * 32, wn * (BN / 2) + j * 32, acc[i][j], r, h);
}

constexpr int P_NBUF = 4;
constexpr int SP_AST = 456, SP_BST = 96;
constexpr int P_SMEM = (64 * SP_AST + 448 * SP_BST) * 2;

__global__ __launch_bounds__(256) void k_proj(const u16* __restrict__ x, const u16* __restrict__ wq,
                                              const u16* __restrict__ wk, const u16* __restrict__ wv,
                                              const u16* __restrict__ wqkv, const u16* __restrict__ gw,
                                              u16* __restrict__ Qa, u16* __restrict__ KV,
                                              u16* __restrict__ QKVb, float* __restrict__ gate) {
  __shared__ __attribute__((aligned(16))) char smem[P_SMEM];
  const int bid = blockIdx.x;
  if (bid < 224) {
    const int n0 = (bid % 14) * 128, m0 = (bid / 14) * 128;
    auto bptr = [&](int k, int nc) -> const u16* {
      const int n = n0 + nc;
      return (n < DM) ? (wq + k * DM + n) : (wqkv + k * NB3 + (n - DM));
    };
    const float qscale = 0.18898223650461362f * 1.4426950408889634f;
    auto epi = [&](int mb, int nbl, const f32x16& a, int r, int h) {
      const int nb = n0 + nbl, n = nb + r;
      if (nb < DM) {
#pragma unroll
        for (int e = 0; e < 16; ++e) {
          int m = mb + (e & 3) + 8 * (e >> 2) + 4 * h;
          Qa[m * DM + n] = to_bf16(a[e] * qscale);
        }
      } else {
#pragma unroll
        for (int e = 0; e < 16; ++e) {
          int m = mb + (e & 3) + 8 * (e >> 2) + 4 * h;
          QKVb[m * NB3 + (n - DM)] = to_bf16(a[e]);
        }
      }
    };
    gemm_glds<128, 128, 7, P_NBUF>(x, DM, m0, smem, bptr, epi);
    return;
  }
  const int m0 = (bid - 224) * 64;
  const int tid = threadIdx.x, lane = tid & 63, wave = tid >> 6;
  u16* as = (u16*)smem;
  u16* bs = as + 64 * SP_AST;
  uint4 ra[14]; uint2 rb0[14], rb1[14]; u16 rg[14];
#pragma unroll
  for (int i = 0; i < 14; ++i) {
    int c = tid + i * 256, row = c / 56, ch = c % 56;
    ra[i] = *(const uint4*)(x + (m0 + row) * DM + ch * 8);
  }
  const int nc = tid & 7;
  const u16* wsrc = (nc < 4) ? wk : wv;
  const int cb = (nc & 3) * 8;
#pragma unroll
  for (int i = 0; i < 14; ++i) {
    int k = (tid >> 3) + i * 32;
    rb0[i] = *(const uint2*)(wsrc + k * DH + cb);
    rb1[i] = *(const uint2*)(wsrc + k * DH + ((cb < 24) ? cb + 4 : cb));
    rg[i] = gw[k];
  }
#pragma unroll
  for (int i = 0; i < 14; ++i) {
    int c = tid + i * 256, row = c / 56, ch = c % 56;
    *(uint4*)(as + row * SP_AST + ch * 8) = ra[i];
  }
#pragma unroll
  for (int i = 0; i < 14; ++i) {
    int k = (tid >> 3) + i * 32;
    uint4 v = make_uint4(rb0[i].x, rb0[i].y, rb1[i].x, rb1[i].y);
    if (cb == 24) { v.z = (nc == 7) ? (unsigned)rg[i] : 0u; v.w = 0u; }
    *(uint4*)(bs + k * SP_BST + nc * 8) = v;
  }
  __syncthreads();
  const int wm = wave >> 1, wn = wave & 1, r = lane & 31, h = lane >> 5;
  const int trq = (lane & 15) >> 2, trp = lane & 3, trblk = (lane >> 4) & 1;
  f32x16 a;
#pragma unroll
  for (int e = 0; e < 16; ++e) a[e] = 0.f;
#pragma unroll 4
  for (int ks = 0; ks < 28; ++ks) {
    bf16x8 af = *(const bf16x8*)(as + (wm * 32 + r) * SP_AST + ks * 16 + h * 8);
    const u16* p = bs + (ks * 16 + 8 * h + trq) * SP_BST + wn * 32 + 16 * trblk + 4 * trp;
    s16x4 lo = __builtin_amdgcn_ds_read_tr16_b64_v4i16((lds_s16x4_ptr)(p));
    s16x4 hi = __builtin_amdgcn_ds_read_tr16_b64_v4i16((lds_s16x4_ptr)(p + 4 * SP_BST));
    a = MFMA32(af, __builtin_shufflevector(lo, hi, 0, 1, 2, 3, 4, 5, 6, 7), a);
  }
  const int mb = m0 + wm * 32, d = r;
  if (wn == 0) {
    const int s = d >> 4, hh = (d >> 3) & 1, j = d & 7;
#pragma unroll
    for (int e = 0; e < 16; ++e) {
      int m = mb + (e & 3) + 8 * (e >> 2) + 4 * h;
      int tile = m >> 5, rr = m & 31;
      KV[tile * 2048 + s * 512 + (hh * 32 + rr) * 8 + j] = (d < DH) ? to_bf16(a[e]) : ((d < 31) ? (u16)0x3F80 : (u16)0);
    }
  } else {
#pragma unroll
    for (int e = 0; e < 16; ++e) {
      int m = mb + (e & 3) + 8 * (e >> 2) + 4 * h;
      int tile = m >> 5, kk = m & 31;
      int s = kk >> 4, k16 = kk & 15;
      int hh = (k16 >> 2) & 1, j = ((k16 >> 3) << 2) | (k16 & 3);
      u16 val = (d < DH) ? to_bf16(a[e]) : ((d == DH) ? (u16)0x3F80 : (u16)0);
      KV[tile * 2048 + 1024 + s * 512 + (hh * 32 + d) * 8 + j] = val;
      if (d == DH) gate[m] = 1.f / (1.f + __expf(-a[e]));
    }
  }
}

constexpr int O_NBUF = 8;

__global__ __launch_bounds__(256) void k_out(const u16* __restrict__ Ocat, const u16* __restrict__ woA,
                                             const u16* __restrict__ woB, u16* __restrict__ out) {
  __shared__ __attribute__((aligned(16))) char smem[O_NBUF * 16384];
  const int n0 = (blockIdx.x % 7) * 64, m0 = (blockIdx.x / 7) * 64;
  auto bptr = [&](int k, int nc) -> const u16* {
    return ((k < DM) ? (woA + k * DM) : (woB + (k - DM) * DM)) + n0 + nc;
  };
  auto epi = [&](int mb, int nbl, const f32x16& a, int r, int h) {
    const int n = n0 + nbl + r;
#pragma unroll
    for (int e = 0; e < 16; ++e) {
      int m = mb + (e & 3) + 8 * (e >> 2) + 4 * h;
      out[m * DM + n] = to_bf16(a[e]);
    }
  };
  gemm_glds<64, 64, 14, O_NBUF>(Ocat, OC, m0, smem, bptr, epi);
}

constexpr int TPC = 2;
constexpr int NCH = 16 / TPC;
constexpr int CH_U4 = 4 * TPC * 256;
constexpr int QD_OFF = 3 * CH_U4;
constexpr int BQ_OFF = QD_OFF + 8 * 64;
constexpr int BK_OFF = BQ_OFF + 8 * 64;
constexpr int FL_OFF = BK_OFF + 16 * 128;
constexpr int A_LDS_U4 = FL_OFF + 16;

__global__ __launch_bounds__(512) void k_attn(const u16* __restrict__ Qa, const u16* __restrict__ KV,
                                              const u16* __restrict__ QKVb, const float* __restrict__ gate,
                                              u16* __restrict__ Ocat) {
  __shared__ __attribute__((aligned(16))) uint4 lds[A_LDS_U4];
  const int tid = threadIdx.x, lane = tid & 63, wave = __builtin_amdgcn_readfirstlane(tid >> 6);
  const int rg = wave & 1, kq = wave >> 1;
  const int r = lane & 31, h = lane >> 5;
  const int pbase = (blockIdx.x >> 1) * 16 + (blockIdx.x & 1);
  const int rot = (blockIdx.x >> 3) & (NCH - 1);
  const uint4* KVg = (const uint4*)KV;
  __attribute__((address_space(3))) volatile unsigned* flag = (__attribute__((address_space(3))) volatile unsigned*)(lds + FL_OFF);

  auto issue2 = [&](int c, int slot) {
    const int cc = (c + rot) & (NCH - 1);
    const char* src = (const char*)(KVg + (kq * 16 + cc * TPC + rg) * 256);
    uint4* dst = lds + slot * CH_U4 + (kq * TPC + rg) * 256;
#pragma unroll
    for (int p = 0; p < 4; ++p) glds16a(src + p * 1024 + (unsigned)(lane * 16), dst + p * 64);
  };
  const int l56 = lane < 56 ? lane : 55;
  auto issue_b = [&]() {
  glds16a((const char*)(QKVb + (pbase + 2 * wave) * NB3) + (unsigned)(l56 * 16), lds + BQ_OFF + wave * 64);
#pragma unroll
  for (int i = 0; i < 2; ++i) {
    int row = pbase - 14 + 2 * (wave * 2 + i);
    row = row < 0 ? (pbase & 1) : row;
    row = row > S_LEN - 1 ? S_LEN - 1 : row;
    const char* src = (const char*)(QKVb + row * NB3 + DM);
    const int l48 = lane < 48 ? lane : 47;
    glds16a(src + (unsigned)(lane * 16), lds + BK_OFF + (wave * 2 + i) * 128);
    glds16a(src + 1024 + (unsigned)(l48 * 16), lds + BK_OFF + (wave * 2 + i) * 128 + 64);
  }
  };
  STAMP(0);
  glds16a((const char*)(Qa + (pbase + 2 * wave) * DM) + (unsigned)(l56 * 16), lds + QD_OFF + wave * 64);
  issue2(0, 0);
  issue2(1, 1);
  if (tid == 0) *flag = 0u;
  float g8[8];
#pragma unroll
  for (int i = 0; i < 8; ++i) g8[i] = gate[pbase + 2 * i];
#pragma unroll
  for (int i = 0; i < 8; ++i) asm volatile("" : "+s"(g8[i]));
  wait_vm<4>();
  raw_barrier();
  issue2(2, 2);
  STAMP(1);

  bf16x8 q0[2], q1[2];
#pragma unroll
  for (int b = 0; b < 2; ++b) {
    const int R = rg * 64 + b * 32 + r;
    const u16* qp = (const u16*)(lds + QD_OFF + (R >> 4) * 64) + (R & 15) * DH;
    uint2 qa = *(const uint2*)(qp + 8 * h), qb = *(const uint2*)(qp + 8 * h + 4);
    uint2 qc = *(const uint2*)(qp + 16 + 8 * h);
    uint2 qd = *(const uint2*)(qp + 20);
    if (h) qd = make_uint2(0, 0);
    q0[b] = __builtin_bit_cast(bf16x8, make_uint4(qa.x, qa.y, qb.x, qb.y));
    q1[b] = __builtin_bit_cast(bf16x8, make_uint4(qc.x, qc.y, qd.x, qd.y));
  }
  f32x16 zero;
#pragma unroll
  for (int e = 0; e < 16; ++e) zero[e] = 0.f;
  float mref[2];
  {
    bf16x8 k0 = __builtin_bit_cast(bf16x8, lds[lane]), k1 = __builtin_bit_cast(bf16x8, lds[64 + lane]);
#pragma unroll
    for (int b = 0; b < 2; ++b) {
      f32x16 s = MFMA32(k0, q0[b], zero);
      s = MFMA32(k1, q1[b], s);
      float m = s[0];
#pragma unroll
      for (int e = 1; e < 16; ++e) m = fmaxf(m, s[e]);
      mref[b] = fmaxf(m, __shfl_xor(m, 32));
    }
  }
  STAMP(2);

  auto branch_b = [&]() {
#pragma unroll
    for (int half = 0; half < 2; ++half) {
      const int pair = half * 64 + (tid >> 3), j = tid & 7, jc = (j < 7) ? j : 6;
      const int pi = pair >> 4, bhead = pair & 15, bpos = pbase + 2 * pi;
      const uint2* qrow = (const uint2*)((const u16*)(lds + BQ_OFF + pi * 64) + bhead * DH);
      const u16* kb = (const u16*)(lds + BK_OFF) + bhead * DH + (pi + 7) * 1024;
      const uint2* krow = (const uint2*)(kb - j * 1024);
      uint2 qv[7], kv[7], vv[8];
#pragma unroll
      for (int i = 0; i < 7; ++i) { qv[i] = qrow[i]; kv[i] = krow[i]; }
#pragma unroll
      for (int w = 0; w < 8; ++w) vv[w] = *(const uint2*)(kb - w * 1024 + DM + 4 * jc);
      __builtin_amdgcn_sched_barrier(0);
      float lg = 0.f;
#pragma unroll
      for (int i = 0; i < 7; ++i) {
        lg = __builtin_amdgcn_fdot2_f32_bf16(__builtin_bit_cast(bf2, qv[i].x), __builtin_bit_cast(bf2, kv[i].x), lg, false);
        lg = __builtin_amdgcn_fdot2_f32_bf16(__builtin_bit_cast(bf2, qv[i].y), __builtin_bit_cast(bf2, kv[i].y), lg, false);
      }
      const bool valid = (bpos - 2 * j >= 0);
      lg = valid ? lg * (0.18898223650461362f * 1.4426950408889634f) : -3.0e38f;
      float mx = lg;
      mx = fmaxf(mx, __shfl_xor(mx, 1)); mx = fmaxf(mx, __shfl_xor(mx, 2)); mx = fmaxf(mx, __shfl_xor(mx, 4));
      const float e = valid ? __builtin_amdgcn_exp2f(lg - mx) : 0.f;
      float sm = e;
      sm += __shfl_xor(sm, 1); sm += __shfl_xor(sm, 2); sm += __shfl_xor(sm, 4);
      float o4[4] = {0.f, 0.f, 0.f, 0.f};
#pragma unroll
      for (int w = 0; w < 8; ++w) {
        const float aw = __shfl(e, (lane & 56) + w);
        o4[0] += aw * __uint_as_float(vv[w].x << 16);
        o4[1] += aw * __uint_as_float(vv[w].x & 0xffff0000u);
        o4[2] += aw * __uint_as_float(vv[w].y << 16);
        o4[3] += aw * __uint_as_float(vv[w].y & 0xffff0000u);
      }
      float gB = 0.f;
#pragma unroll
      for (int i = 0; i < 8; ++i) gB = (i == pi) ? g8[i] : gB;
      const float sc = (1.f - gB) / sm;
      if (j < 7) {
        uint2 wv;
        wv.x = pack_bf16(o4[0] * sc, o4[1] * sc);
        wv.y = pack_bf16(o4[2] * sc, o4[3] * sc);
        *(uint2*)(Ocat + bpos * OC + DM + bhead * DH + 4 * j) = wv;
      }
    }
  };

  f32x16 o0, o1;
#pragma nounroll
  for (int attempt = 0; attempt < 2; ++attempt) {
    bf16x8 q1m[2];
#pragma unroll
    for (int b = 0; b < 2; ++b) {
      q1m[b] = q1[b];
      const float n0 = -mref[b];
      const u16 c0 = to_bf16(n0); const float n1 = n0 - bf_to_f(c0);
      const u16 c1 = to_bf16(n1); const float n2 = n1 - bf_to_f(c1);
      const u16 c2 = to_bf16(n2);
      if (h) { q1m[b][4] = (short)c0; q1m[b][5] = (short)c1; q1m[b][6] = (short)c2; }
    }
#pragma unroll
    for (int e = 0; e < 16; ++e) { o0[e] = 0.f; o1[e] = 0.f; }
    if (attempt) {
      issue2(0, 0); issue2(1, 1);
      wait_vm<4>(); raw_barrier();
      issue2(2, 2);
    }
    auto softmax_pv = [&](const f32x16& s, const bf16x8& vf0, const bf16x8& vf1, f32x16& o) {
      uint4 pa, pb;
      pa.x = pack_bf16(EXP2(s[0]), EXP2(s[1]));
      pa.y = pack_bf16(EXP2(s[2]), EXP2(s[3]));
      pa.z = pack_bf16(EXP2(s[4]), EXP2(s[5]));
      pa.w = pack_bf16(EXP2(s[6]), EXP2(s[7]));
      o = MFMA32(vf0, __builtin_bit_cast(bf16x8, pa), o);
      pb.x = pack_bf16(EXP2(s[8]), EXP2(s[9]));
      pb.y = pack_bf16(EXP2(s[10]), EXP2(s[11]));
      pb.z = pack_bf16(EXP2(s[12]), EXP2(s[13]));
      pb.w = pack_bf16(EXP2(s[14]), EXP2(s[15]));
      o = MFMA32(vf1, __builtin_bit_cast(bf16x8, pb), o);
    };
    f32x16 sA, sB;
    bf16x8 kf0, kf1, vf0, vf1;
    {
      const uint4* base = lds + kq * TPC * 256 + lane;
      kf0 = __builtin_bit_cast(bf16x8, base[0]); kf1 = __builtin_bit_cast(bf16x8, base[64]);
      vf0 = __builtin_bit_cast(bf16x8, base[128]); vf1 = __builtin_bit_cast(bf16x8, base[192]);
      sA = MFMA32(kf0, q0[0], zero);
      sA = MFMA32(kf1, q1m[0], sA);
    }
    int slot = 0;
#pragma nounroll
    for (int c = 0; c < NCH; ++c) {
      if (attempt == 0 && c == 5 && wave < 4) { STAMP(5); branch_b(); STAMP(6); }
#pragma unroll
      for (int t = 0; t < TPC; ++t) {
        sB = MFMA32(kf0, q0[1], zero);
        sB = MFMA32(kf1, q1m[1], sB);
        const bool more = (t + 1 < TPC) || (c + 1 < NCH);
        if (t + 1 == TPC) {
          slot = (slot == 2) ? 0 : slot + 1;
          if (c + 1 < NCH) {
            if ((c == 1 || c == 2) && attempt == 0) wait_vm<9>(); else if (c + 2 < NCH) wait_vm<4>(); else wait_vm<0>();
            raw_barrier();
            if (c + 3 < NCH) issue2(c + 3, slot == 0 ? 2 : slot - 1);
            if (c == 0 && attempt == 0) issue_b();
          }
        }
        bf16x8 nk0 = kf0, nk1 = kf1, nv0 = vf0, nv1 = vf1;
        if (more) {
          const uint4* base = lds + slot * CH_U4 + kq * TPC * 256 + ((t + 1) & (TPC - 1)) * 256 + lane;
          nk0 = __builtin_bit_cast(bf16x8, base[0]); nk1 = __builtin_bit_cast(bf16x8, base[64]);
          nv0 = __builtin_bit_cast(bf16x8, base[128]); nv1 = __builtin_bit_cast(bf16x8, base[192]);
        }
        __builtin_amdgcn_sched_barrier(0);
        softmax_pv(sA, vf0, vf1, o0);
        __builtin_amdgcn_sched_barrier(0);
        if (more) {
          sA = MFMA32(nk0, q0[0], zero);
          sA = MFMA32(nk1, q1m[0], sA);
        }
        __builtin_amdgcn_sched_barrier(0);
        softmax_pv(sB, vf0, vf1, o1);
        __builtin_amdgcn_sched_barrier(0);
        kf0 = nk0; kf1 = nk1; vf0 = nv0; vf1 = nv1;
      }
      if (attempt == 0 && c == 5 && wave >= 4) { STAMP(5); branch_b(); STAMP(6); }
    }
    STAMP(3);
    float* red = (float*)lds;
    {
      float* dst = red + (kq >> 1) * (2 * CH_U4 * 4) + ((kq & 1) * 4 + rg * 2) * 1024 + lane;
#pragma unroll
      for (int e = 0; e < 16; ++e) { dst[e * 64] = o0[e]; dst[1024 + e * 64] = o1[e]; }
    }
    if (attempt == 0 && h && !(o0[12] < 1.0e18f && o1[12] < 1.0e18f)) *flag = 1u;
    raw_barrier();
    if (attempt == 1 || *flag == 0u) {
      const int rb = wave & 3, dq = (wave >> 2) * 2;
      float l = 0.f, ov[8];
#pragma unroll
      for (int i = 0; i < 8; ++i) ov[i] = 0.f;
#pragma unroll
      for (int q = 0; q < 4; ++q) {
        const float* src = red + (q >> 1) * (2 * CH_U4 * 4) + ((q & 1) * 4 + rb) * 1024;
        l += src[12 * 64 + 32 + r];
#pragma unroll
        for (int i = 0; i < 8; ++i) ov[i] += src[(4 * dq + i) * 64 + lane];
      }
      const int R = rb * 32 + r;
      const int pos = pbase + 2 * (R >> 4), head = R & 15;
      float gA = 0.f;
#pragma unroll
      for (int i = 0; i < 8; ++i) gA = (i == (R >> 4)) ? g8[i] : gA;
      const float sc = gA / l;
#pragma unroll
      for (int g = 0; g < 2; ++g) {
        const int d = 8 * (dq + g) + 4 * h;
        if (d < DH) {
          uint2 w;
          w.x = pack_bf16(ov[4 * g] * sc, ov[4 * g + 1] * sc);
          w.y = pack_bf16(ov[4 * g + 2] * sc, ov[4 * g + 3] * sc);
          *(uint2*)(Ocat + pos * OC + head * DH + d) = w;
        }
      }
      STAMP(4);
      break;
    }
    {
      float m[2] = {-3.0e38f, -3.0e38f};
      for (int t = 0; t < 16; ++t) {
        const uint4* src = KVg + (kq * 16 + t) * 256 + lane;
        bf16x8 k0 = __builtin_bit_cast(bf16x8, src[0]);
        bf16x8 k1 = __builtin_bit_cast(bf16x8, src[64]);
#pragma unroll
        for (int b = 0; b < 2; ++b) {
          f32x16 s = MFMA32(k0, q0[b], zero);
          s = MFMA32(k1, q1[b], s);
#pragma unroll
          for (int e = 0; e < 16; ++e) m[b] = fmaxf(m[b], s[e]);
        }
      }
      m[0] = fmaxf(m[0], __shfl_xor(m[0], 32));
      m[1] = fmaxf(m[1], __shfl_xor(m[1], 32));
      wait_vm<0>();
      raw_barrier();
      red[(wave * 2) * 64 + lane] = m[0];
      red[(wave * 2 + 1) * 64 + lane] = m[1];
      raw_barrier();
#pragma unroll
      for (int b = 0; b < 2; ++b) {
        float mm = red[(rg * 2 + b) * 64 + lane];
#pragma unroll
        for (int q = 1; q < 4; ++q) mm = fmaxf(mm, red[((q * 2 + rg) * 2 + b) * 64 + lane]);
        mref[b] = mm;
      }
      raw_barrier();
    }
  }
}

extern "C" void kernel_launch(void* const* d_in, const int* in_sizes, int n_in,
                              void* d_out, int out_size, void* d_ws, size_t ws_size,
                              hipStream_t stream) {
  const u16* x = (const u16*)d_in[0];
  const u16* wq = (const u16*)d_in[1];
  const u16* wk = (const u16*)d_in[2];
  const u16* wv = (const u16*)d_in[3];
  const u16* woA = (const u16*)d_in[4];
  const u16* wqkv = (const u16*)d_in[6];
  const u16* woB = (const u16*)d_in[7];
  const u16* gw = (const u16*)d_in[8];
  char* ws = (char*)d_ws;
  u16* Qa = (u16*)(ws);
  u16* KV = (u16*)(ws + (2u << 20));
  u16* QKVb = (u16*)(ws + (3u << 20));
  float* gate = (float*)(ws + (9u << 20));
  u16* Ocat = (u16*)(ws + (10u << 20));
  u16* out = (u16*)d_out;

  k_proj<<<dim3(256), 256, 0, stream>>>(x, wq, wk, wv, wqkv, gw, Qa, KV, QKVb, gate);
  k_attn<<<dim3(S_LEN / 8), 512, 0, stream>>>(Qa, KV, QKVb, gate, Ocat);
  k_out<<<dim3(224), 256, 0, stream>>>(Ocat, woA, woB, out);
}
```

```cpp
#include <hip/hip_runtime.h>
#include <hip/hip_bf16.h>
#include <stdint.h>

typedef __attribute__((ext_vector_type(8))) short bf16x8;
typedef __attribute__((ext_vector_type(4))) short s16x4;
typedef __attribute__((ext_vector_type(16))) float f32x16;
typedef __attribute__((ext_vector_type(2))) __bf16 bf2;
typedef __attribute__((address_space(3))) s16x4* lds_s16x4_ptr;
typedef unsigned short u16;

#define DI __device__ __forceinline__
#define STAMP(i) do {} while (0)
#define EXP2(x) __builtin_amdgcn_exp2f(x)
#define MFMA32(a, b, c) __builtin_amdgcn_mfma_f32_32x32x16_bf16((a), (b), (c), 0, 0, 0)

constexpr int S_LEN = 2048;
constexpr int DM = 448;
constexpr int NH = 16;
constexpr int DH = 28;
constexpr int NB3 = 1344;
constexpr int OC = 896;

DI unsigned pack_bf16(float a, float b) {
  bf2 v; v[0] = (__bf16)a; v[1] = (__bf16)b;
  return __builtin_bit_cast(unsigned, v);
}
DI u16 to_bf16(float a) { __bf16 v = (__bf16)a; return __builtin_bit_cast(u16, v); }
DI float bf_to_f(u16 a) { return __uint_as_float(((unsigned)a) << 16); }

template <int V> struct IC { static constexpr int value = V; };
template <int I, int N, class F> DI void static_for(F&& f) {
  if constexpr (I < N) { f(IC<I>{}); static_for<I + 1, N>(f); }
}
template <int N> DI void wait_vm() { asm volatile("s_waitcnt vmcnt(%0)" ::"n"(N) : "memory"); }
DI void raw_barrier() { asm volatile("s_waitcnt lgkmcnt(0)" ::: "memory"); __builtin_amdgcn_s_barrier(); asm volatile("" ::: "memory"); }
DI void glds16(const void* g, void* l) {
  __builtin_amdgcn_global_load_lds((const __attribute__((address_space(1))) unsigned*)g,
                                   (__attribute__((address_space(3))) unsigned*)l, 16, 0, 0);
}

template <int CTRL> DI float dpp_f(float v) {
  return __int_as_float(__builtin_amdgcn_update_dpp(0, __float_as_int(v), CTRL, 0xf, 0xf, true));
}
DI void glds16a(const void* g, const void* l) {
  const unsigned la = __builtin_amdgcn_readfirstlane((unsigned)(size_t)l);
  unsigned keep;
  asm volatile("s_mov_b32 %0, m0\n\ts_mov_b32 m0, %2\n\ts_nop 0\n\tglobal_load_lds_dwordx4 %1, off\n\ts_mov_b32 m0, %0"
               : "=&s"(keep) : "v"(g), "s"(la) : "memory");
}

template <int BN> DI int bswz(int row) { return (BN == 128) ? ((row & 3) << 2) : (((row >> 1) & 1) << 2); }

template <int BM, int BN, int NS, int NBUF, class BPtr, class Epi>
DI void gemm_glds(const u16* __restrict__ A, int lda, int m0, char* smem, BPtr bptr, Epi epi) {
  constexpr int TM = BM / 64, TN = BN / 64;
  constexpr int GA = BM / 32, GB = BN / 32, G = GA + GB;
  constexpr int A_BYTES = BM * 128, B_BYTES = BN * 128, ST_BYTES = A_BYTES + B_BYTES;
  constexpr int CPR = BN / 8;
  const int tid = threadIdx.x, lane = tid & 63, wave = __builtin_amdgcn_readfirstlane(tid >> 6);
  const int wm = wave >> 1, wn = wave & 1;
  const int r = lane & 31, h = lane >> 5;

  const u16* asrc[GA];
#pragma unroll
  for (int i = 0; i < GA; ++i) {
    int idx = (wave * GA + i) * 64 + lane, row = idx >> 3, ch = (idx & 7) ^ ((row >> 1) & 7);
    asrc[i] = A + (size_t)(m0 + row) * lda + ch * 8;
  }
  int brow[GB], bch[GB];
#pragma unroll
  for (int i = 0; i < GB; ++i) {
    int idx = (wave * GB + i) * 64 + lane;
    brow[i] = idx / CPR;
    bch[i] = (idx % CPR) ^ bswz<BN>(brow[i]);
  }
  auto issue = [&](int st, int sl) {
    char* base = smem + sl * ST_BYTES;
#pragma unroll
    for (int i = 0; i < GA; ++i) glds16(asrc[i] + st * 64, base + (wave * GA + i) * 1024);
#pragma unroll
    for (int i = 0; i < GB; ++i) glds16(bptr(st * 64 + brow[i], bch[i] * 8), base + A_BYTES + (wave * GB + i) * 1024);
  };

  f32x16 acc[TM][TN];
#pragma unroll
  for (int i = 0; i < TM; ++i)
#pragma unroll
    for (int j = 0; j < TN; ++j)
#pragma unroll
      for (int e = 0; e < 16; ++e) acc[i][j][e] = 0.f;

  int aoff[TM], asw[TM];
#pragma unroll
  for (int i = 0; i < TM; ++i) {
    int row = wm * (BM / 2) + i * 32 + r;
    aoff[i] = row * 128; asw[i] = (row >> 1) & 7;
  }
  const int trq = (lane & 15) >> 2, trp = lane & 3, trblk = (lane >> 4) & 1;

#pragma nounroll
  for (int st = 0; st < NBUF - 1; ++st) issue(st, st);

  int slot = 0;
#pragma nounroll
  for (int st = 0; st < NS; ++st) {
    if (st + NBUF - 2 < NS) wait_vm<G * (NBUF - 2)>(); else wait_vm<0>();
    raw_barrier();
    if (st + NBUF - 1 < NS) issue(st + NBUF - 1, slot == 0 ? NBUF - 1 : slot - 1);
    const char* as = smem + slot * ST_BYTES;
    const char* bs = as + A_BYTES;
    slot = (slot == NBUF - 1) ? 0 : slot + 1;
#pragma unroll
    for (int ks = 0; ks < 4; ++ks) {
      bf16x8 af[TM], bfr[TN];
#pragma unroll
      for (int i = 0; i < TM; ++i) af[i] = *(const bf16x8*)(as + aoff[i] + (((2 * ks + h) ^ asw[i]) << 4));
      const int rk = ks * 16 + 8 * h + trq;
#pragma unroll
      for (int j = 0; j < TN; ++j) {
        const int col = wn * (BN / 2) + j * 32 + 16 * trblk + 4 * trp;
        const char* p = bs + rk * (BN * 2) + (((col >> 3) ^ bswz<BN>(rk)) << 4) + ((trp & 1) << 3);
        s16x4 lo = __builtin_amdgcn_ds_read_tr16_b64_v4i16((lds_s16x4_ptr)(p));
        s16x4 hi = __builtin_amdgcn_ds_read_tr16_b64_v4i16((lds_s16x4_ptr)(p + 4 * BN * 2 + (((((col >> 3) ^ bswz<BN>(rk + 4)) - ((col >> 3) ^ bswz<BN>(rk)))) << 4)));
        bfr[j] = __builtin_shufflevector(lo, hi, 0, 1, 2, 3, 4, 5, 6, 7);
      }
#pragma unroll
      for (int i = 0; i < TM; ++i)
#pragma unroll
        for (int j = 0; j < TN; ++j) acc[i][j] = MFMA32(af[i], bfr[j], acc[i][j]);
    }
  }
#pragma unroll
  for (int i = 0; i < TM; ++i)
#pragma unroll
    for (int j = 0; j < TN; ++j)
      epi(m0 + wm * (BM / 2) + i * 32, wn * (BN / 2) + j * 32, acc[i][j], r, h);
}

constexpr int P_NBUF = 4;
constexpr int SP_AST = 456, SP_BST = 96;
constexpr int P_SMEM = (64 * SP_AST + 448 * SP_BST) * 2;

__global__ __launch_bounds__(256) void k_proj(const u16* __restrict__ x, const u16* __restrict__ wq,
                                              const u16* __restrict__ wk, const u16* __restrict__ wv,
                                              const u16* __restrict__ wqkv, const u16* __restrict__ gw,
                                              u16* __restrict__ Qa, u16* __restrict__ KV,
                                              u16* __restrict__ QKVb, float* __restrict__ gate) {
  __shared__ __attribute__((aligned(16))) char smem[P_SMEM];
  const int bid = blockIdx.x;
  if (bid < 224) {
    const int n0 = (bid % 14) * 128, m0 = (bid / 14) * 128;
    auto bptr = [&](int k, int nc) -> const u16* {
      const int n = n0 + nc;
      return (n < DM) ? (wq + k * DM + n) : (wqkv + k * NB3 + (n - DM));
    };
    const float qscale = 0.18898223650461362f * 1.4426950408889634f;
    auto epi = [&](int mb, int nbl, const f32x16& a, int r, int h) {
      const int nb = n0 + nbl, n = nb + r;
      if (nb < DM) {
#pragma unroll
        for (int e = 0; e < 16; ++e) {
          int m = mb + (e & 3) + 8 * (e >> 2) + 4 * h;
          Qa[m * DM + n] = to_bf16(a[e] * qscale);
        }
      } else {
#pragma unroll
        for (int e = 0; e < 16; ++e) {
          int m = mb + (e & 3) + 8 * (e >> 2) + 4 * h;
          QKVb[m * NB3 + (n - DM)] = to_bf16(a[e]);
        }
      }
    };
    gemm_glds<128, 128, 7, P_NBUF>(x, DM, m0, smem, bptr, epi);
    return;
  }
  const int m0 = (bid - 224) * 64;
  const int tid = threadIdx.x, lane = tid & 63, wave = tid >> 6;
  u16* as = (u16*)smem;
  u16* bs = as + 64 * SP_AST;
  uint4 ra[14]; uint2 rb0[14], rb1[14]; u16 rg[14];
#pragma unroll
  for (int i = 0; i < 14; ++i) {
    int c = tid + i * 256, row = c / 56, ch = c % 56;
    ra[i] = *(const uint4*)(x + (m0 + row) * DM + ch * 8);
  }
  const int nc = tid & 7;
  const u16* wsrc = (nc < 4) ? wk : wv;
  const int cb = (nc & 3) * 8;
#pragma unroll
  for (int i = 0; i < 14; ++i) {
    int k = (tid >> 3) + i * 32;
    rb0[i] = *(const uint2*)(wsrc + k * DH + cb);
    rb1[i] = *(const uint2*)(wsrc + k * DH + ((cb < 24) ? cb + 4 : cb));
    rg[i] = gw[k];
  }
#pragma unroll
  for (int i = 0; i < 14; ++i) {
    int c = tid + i * 256, row = c / 56, ch = c % 56;
    *(uint4*)(as + row * SP_AST + ch * 8) = ra[i];
  }
#pragma unroll
  for (int i = 0; i < 14; ++i) {
    int k = (tid >> 3) + i * 32;
    uint4 v = make_uint4(rb0[i].x, rb0[i].y, rb1[i].x, rb1[i].y);
    if (cb == 24) { v.z = (nc == 7) ? (unsigned)rg[i] : 0u; v.w = 0u; }
    *(uint4*)(bs + k * SP_BST + nc * 8) = v;
  }
  __syncthreads();
  const int wm = wave >> 1, wn = wave & 1, r = lane & 31, h = lane >> 5;
  const int trq = (lane & 15) >> 2, trp = lane & 3, trblk = (lane >> 4) & 1;
  f32x16 a;
#pragma unroll
  for (int e = 0; e < 16; ++e) a[e] = 0.f;
#pragma unroll 4
  for (int ks = 0; ks < 28; ++ks) {
    bf16x8 af = *(const bf16x8*)(as + (wm * 32 + r) * SP_AST + ks * 16 + h * 8);
    const u16* p = bs + (ks * 16 + 8 * h + trq) * SP_BST + wn * 32 + 16 * trblk + 4 * trp;
    s16x4 lo = __builtin_amdgcn_ds_read_tr16_b64_v4i16((lds_s16x4_ptr)(p));
    s16x4 hi = __builtin_amdgcn_ds_read_tr16_b64_v4i16((lds_s16x4_ptr)(p + 4 * SP_BST));
    a = MFMA32(af, __builtin_shufflevector(lo, hi, 0, 1, 2, 3, 4, 5, 6, 7), a);
  }
  const int mb = m0 + wm * 32, d = r;
  if (wn == 0) {
    const int s = d >> 4, hh = (d >> 3) & 1, j = d & 7;
#pragma unroll
    for (int e = 0; e < 16; ++e) {
      int m = mb + (e & 3) + 8 * (e >> 2) + 4 * h;
      int tile = m >> 5, rr = m & 31;
      KV[tile * 2048 + s * 512 + (hh * 32 + rr) * 8 + j] = (d < DH) ? to_bf16(a[e]) : ((d < 31) ? (u16)0x3F80 : (u16)0);
    }
  } else {
#pragma unroll
    for (int e = 0; e < 16; ++e) {
      int m = mb + (e & 3) + 8 * (e >> 2) + 4 * h;
      int tile = m >> 5, kk = m & 31;
      int s = kk >> 4, k16 = kk & 15;
      int hh = (k16 >> 2) & 1, j = ((k16 >> 3) << 2) | (k16 & 3);
      u16 val = (d < DH) ? to_bf16(a[e]) : ((d == DH) ? (u16)0x3F80 : (u16)0);
      KV[tile * 2048 + 1024 + s * 512 + (hh * 32 + d) * 8 + j] = val;
      if (d == DH) gate[m] = 1.f / (1.f + __expf(-a[e]));
    }
  }
}

constexpr int O_NBUF = 8;

__global__ __launch_bounds__(256) void k_out(const u16* __restrict__ Ocat, const u16* __restrict__ woA,
                                             const u16* __restrict__ woB, u16* __restrict__ out) {
  __shared__ __attribute__((aligned(16))) char smem[O_NBUF * 16384];
  const int n0 = (blockIdx.x % 7) * 64, m0 = (blockIdx.x / 7) * 64;
  auto bptr = [&](int k, int nc) -> const u16* {
    return ((k < DM) ? (woA + k * DM) : (woB + (k - DM) * DM)) + n0 + nc;
  };
  auto epi = [&](int mb, int nbl, const f32x16& a, int r, int h) {
    const int n = n0 + nbl + r;
#pragma unroll
    for (int e = 0; e < 16; ++e) {
      int m = mb + (e & 3) + 8 * (e >> 2) + 4 * h;
      out[m * DM + n] = to_bf16(a[e]);
    }
  };
  gemm_glds<64, 64, 14, O_NBUF>(Ocat, OC, m0, smem, bptr, epi);
}

constexpr int TPC = 2;
constexpr int NCH = 16 / TPC;
constexpr int CH_U4 = 4 * TPC * 256;
constexpr int QD_OFF = 3 * CH_U4;
constexpr int BQ_OFF = QD_OFF + 8 * 64;
constexpr int BK_OFF = BQ_OFF + 8 * 64;
constexpr int FL_OFF = BK_OFF + 16 * 128;
constexpr int A_LDS_U4 = FL_OFF + 16;

__global__ __launch_bounds__(512) void k_attn(const u16* __restrict__ Qa, const u16* __restrict__ KV,
                                              const u16* __restrict__ QKVb, const float* __restrict__ gate,
                                              u16* __restrict__ Ocat) {
  __shared__ __attribute__((aligned(16))) uint4 lds[A_LDS_U4];
  const int tid = threadIdx.x, lane = tid & 63, wave = __builtin_amdgcn_readfirstlane(tid >> 6);
  const int rg = wave & 1, kq = wave >> 1;
  const int r = lane & 31, h = lane >> 5;
  const int pbase = (blockIdx.x >> 1) * 16 + (blockIdx.x & 1);
  const int rot = (blockIdx.x >> 3) & (NCH - 1);
  const uint4* KVg = (const uint4*)KV;
  __attribute__((address_space(3))) volatile unsigned* flag = (__attribute__((address_space(3))) volatile unsigned*)(lds + FL_OFF);

  auto issue2 = [&](int c, int slot) {
    const int cc = (c + rot) & (NCH - 1);
    const char* src = (const char*)(KVg + (kq * 16 + cc * TPC + rg) * 256);
    uint4* dst = lds + slot * CH_U4 + (kq * TPC + rg) * 256;
#pragma unroll
    for (int p = 0; p < 4; ++p) glds16a(src + p * 1024 + (unsigned)(lane * 16), dst + p * 64);
  };
  const int l56 = lane < 56 ? lane : 55;
  auto issue_b = [&]() {
  glds16a((const char*)(QKVb + (pbase + 2 * wave) * NB3) + (unsigned)(l56 * 16), lds + BQ_OFF + wave * 64);
#pragma unroll
  for (int i = 0; i < 2; ++i) {
    int row = pbase - 14 + 2 * (wave * 2 + i);
    row = row < 0 ? (pbase & 1) : row;
    row = row > S_LEN - 1 ? S_LEN - 1 : row;
    const char* src = (const char*)(QKVb + row * NB3 + DM);
    const int l48 = lane < 48 ? lane : 47;
    glds16a(src + (unsigned)(lane * 16), lds + BK_OFF + (wave * 2 + i) * 128);
    glds16a(src + 1024 + (unsigned)(l48 * 16), lds + BK_OFF + (wave * 2 + i) * 128 + 64);
  }
  };
  STAMP(0);
  issue_b();
  glds16a((const char*)(Qa + (pbase + 2 * wave) * DM) + (unsigned)(l56 * 16), lds + QD_OFF + wave * 64);
  issue2(0, 0);
  issue2(1, 1);
  if (tid == 0) *flag = 0u;
  float g8[8];
#pragma unroll
  for (int i = 0; i < 8; ++i) g8[i] = gate[pbase + 2 * i];
#pragma unroll
  for (int i = 0; i < 8; ++i) asm volatile("" : "+s"(g8[i]));
  wait_vm<9>();
  raw_barrier();
  STAMP(5);
  uint2 bres[2];
  {
    const int j = tid & 7, jc = (j < 7) ? j : 6;
    uint2 qv[2][7], kv[2][7], vv[2][8];
#pragma unroll
    for (int half = 0; half < 2; ++half) {
      const int pair = half * 64 + (tid >> 3), pi = pair >> 4, bhead = pair & 15;
      const uint2* qrow = (const uint2*)((const u16*)(lds + BQ_OFF + pi * 64) + bhead * DH);
      const u16* kb = (const u16*)(lds + BK_OFF) + bhead * DH + (pi + 7) * 1024;
      const uint2* krow = (const uint2*)(kb - j * 1024);
#pragma unroll
      for (int i = 0; i < 7; ++i) { qv[half][i] = qrow[i]; kv[half][i] = krow[i]; }
#pragma unroll
      for (int w = 0; w < 8; ++w) vv[half][w] = *(const uint2*)(kb - w * 1024 + DM + 4 * jc);
    }
    __builtin_amdgcn_sched_barrier(0);
    float e[2], sm[2];
#pragma unroll
    for (int half = 0; half < 2; ++half) {
      const int pi = (half * 64 + (tid >> 3)) >> 4;
      float lg = 0.f;
#pragma unroll
      for (int i = 0; i < 7; ++i) {
        lg = __builtin_amdgcn_fdot2_f32_bf16(__builtin_bit_cast(bf2, qv[half][i].x), __builtin_bit_cast(bf2, kv[half][i].x), lg, false);
        lg = __builtin_amdgcn_fdot2_f32_bf16(__builtin_bit_cast(bf2, qv[half][i].y), __builtin_bit_cast(bf2, kv[half][i].y), lg, false);
      }
      const bool valid = (pbase + 2 * pi - 2 * j >= 0);
      lg = valid ? lg * (0.18898223650461362f * 1.4426950408889634f) : -3.0e38f;
      float mx = lg;
      mx = fmaxf(mx, dpp_f<0xB1>(mx)); mx = fmaxf(mx, dpp_f<0x4E>(mx)); mx = fmaxf(mx, dpp_f<0x141>(mx));
      e[half] = valid ? __builtin_amdgcn_exp2f(lg - mx) : 0.f;
      float t = e[half];
      t += dpp_f<0xB1>(t); t += dpp_f<0x4E>(t); t += dpp_f<0x141>(t);
      sm[half] = t;
    }
    float aw[2][8];
#pragma unroll
    for (int half = 0; half < 2; ++half)
#pragma unroll
      for (int w = 0; w < 8; ++w) aw[half][w] = __shfl(e[half], (lane & 56) + w);
    __builtin_amdgcn_sched_barrier(0);
#pragma unroll
    for (int half = 0; half < 2; ++half) {
      const int pair = half * 64 + (tid >> 3), pi = pair >> 4, bhead = pair & 15, bpos = pbase + 2 * pi;
      float o4[4] = {0.f, 0.f, 0.f, 0.f};
#pragma unroll
      for (int w = 0; w < 8; ++w) {
        o4[0] += aw[half][w] * __uint_as_float(vv[half][w].x << 16);
        o4[1] += aw[half][w] * __uint_as_float(vv[half][w].x & 0xffff0000u);
        o4[2] += aw[half][w] * __uint_as_float(vv[half][w].y << 16);
        o4[3] += aw[half][w] * __uint_as_float(vv[half][w].y & 0xffff0000u);
      }
      float gB = 0.f;
#pragma unroll
      for (int i = 0; i < 8; ++i) gB = (i == pi) ? g8[i] : gB;
      const float sc = (1.f - gB) / sm[half];
      bres[half].x = pack_bf16(o4[0] * sc, o4[1] * sc);
      bres[half].y = pack_bf16(o4[2] * sc, o4[3] * sc);
    }
  }

  STAMP(6);
  wait_vm<4>();
  raw_barrier();
  issue2(2, 2);
  STAMP(1);

  bf16x8 q0[2], q1[2];
#pragma unroll
  for (int b = 0; b < 2; ++b) {
    const int R = rg * 64 + b * 32 + r;
    const u16* qp = (const u16*)(lds + QD_OFF + (R >> 4) * 64) + (R & 15) * DH;
    uint2 qa = *(const uint2*)(qp + 8 * h), qb = *(const uint2*)(qp + 8 * h + 4);
    uint2 qc = *(const uint2*)(qp + 16 + 8 * h);
    uint2 qd = *(const uint2*)(qp + 20);
    if (h) qd = make_uint2(0, 0);
    q0[b] = __builtin_bit_cast(bf16x8, make_uint4(qa.x, qa.y, qb.x, qb.y));
    q1[b] = __builtin_bit_cast(bf16x8, make_uint4(qc.x, qc.y, qd.x, qd.y));
  }
  f32x16 zero;
#pragma unroll
  for (int e = 0; e < 16; ++e) zero[e] = 0.f;
  float mref[2];
  {
    bf16x8 k0 = __builtin_bit_cast(bf16x8, lds[lane]), k1 = __builtin_bit_cast(bf16x8, lds[64 + lane]);
#pragma unroll
    for (int b = 0; b < 2; ++b) {
      f32x16 s = MFMA32(k0, q0[b], zero);
      s = MFMA32(k1, q1[b], s);
      float m = s[0];
#pragma unroll
      for (int e = 1; e < 16; ++e) m = fmaxf(m, s[e]);
      mref[b] = fmaxf(m, __shfl_xor(m, 32));
    }
  }
  STAMP(2);

  f32x16 o0, o1;
#pragma nounroll
  for (int attempt = 0; attempt < 2; ++attempt) {
    bf16x8 q1m[2];
#pragma unroll
    for (int b = 0; b < 2; ++b) {
      q1m[b] = q1[b];
      const float n0 = -mref[b];
      const u16 c0 = to_bf16(n0); const float n1 = n0 - bf_to_f(c0);
      const u16 c1 = to_bf16(n1); const float n2 = n1 - bf_to_f(c1);
      const u16 c2 = to_bf16(n2);
      if (h) { q1m[b][4] = (short)c0; q1m[b][5] = (short)c1; q1m[b][6] = (short)c2; }
    }
#pragma unroll
    for (int e = 0; e < 16; ++e) { o0[e] = 0.f; o1[e] = 0.f; }
    if (attempt) {
      issue2(0, 0); issue2(1, 1);
      wait_vm<4>(); raw_barrier();
      issue2(2, 2);
    }
    auto softmax_pv = [&](const f32x16& s, const bf16x8& vf0, const bf16x8& vf1, f32x16& o) {
      uint4 pa, pb;
      pa.x = pack_bf16(EXP2(s[0]), EXP2(s[1]));
      pa.y = pack_bf16(EXP2(s[2]), EXP2(s[3]));
      pa.z = pack_bf16(EXP2(s[4]), EXP2(s[5]));
      pa.w = pack_bf16(EXP2(s[6]), EXP2(s[7]));
      o = MFMA32(vf0, __builtin_bit_cast(bf16x8, pa), o);
      pb.x = pack_bf16(EXP2(s[8]), EXP2(s[9]));
      pb.y = pack_bf16(EXP2(s[10]), EXP2(s[11]));
      pb.z = pack_bf16(EXP2(s[12]), EXP2(s[13]));
      pb.w = pack_bf16(EXP2(s[14]), EXP2(s[15]));
      o = MFMA32(vf1, __builtin_bit_cast(bf16x8, pb), o);
    };
    f32x16 sA, sB;
    bf16x8 kf0, kf1, vf0, vf1;
    {
      const uint4* base = lds + kq * TPC * 256 + lane;
      kf0 = __builtin_bit_cast(bf16x8, base[0]); kf1 = __builtin_bit_cast(bf16x8, base[64]);
      vf0 = __builtin_bit_cast(bf16x8, base[128]); vf1 = __builtin_bit_cast(bf16x8, base[192]);
      sA = MFMA32(kf0, q0[0], zero);
      sA = MFMA32(kf1, q1m[0], sA);
    }
    int slot = 0;
#pragma nounroll
    for (int c = 0; c < NCH; ++c) {
#pragma unroll
      for (int t = 0; t < TPC; ++t) {
        sB = MFMA32(kf0, q0[1], zero);
        sB = MFMA32(kf1, q1m[1], sB);
        const bool more = (t + 1 < TPC) || (c + 1 < NCH);
        if (t + 1 == TPC) {
          slot = (slot == 2) ? 0 : slot + 1;
          if (c + 1 < NCH) {
            if (c + 2 < NCH) wait_vm<4>(); else wait_vm<0>();
            raw_barrier();
            if (c + 3 < NCH) issue2(c + 3, slot == 0 ? 2 : slot - 1);
          }
        }
        bf16x8 nk0 = kf0, nk1 = kf1, nv0 = vf0, nv1 = vf1;
        if (more) {
          const uint4* base = lds + slot * CH_U4 + kq * TPC * 256 + ((t + 1) & (TPC - 1)) * 256 + lane;
          nk0 = __builtin_bit_cast(bf16x8, base[0]); nk1 = __builtin_bit_cast(bf16x8, base[64]);
          nv0 = __builtin_bit_cast(bf16x8, base[128]); nv1 = __builtin_bit_cast(bf16x8, base[192]);
        }
        __builtin_amdgcn_sched_barrier(0);
        softmax_pv(sA, vf0, vf1, o0);
        __builtin_amdgcn_sched_barrier(0);
        if (more) {
          sA = MFMA32(nk0, q0[0], zero);
          sA = MFMA32(nk1, q1m[0], sA);
        }
        __builtin_amdgcn_sched_barrier(0);
        softmax_pv(sB, vf0, vf1, o1);
        __builtin_amdgcn_sched_barrier(0);
        kf0 = nk0; kf1 = nk1; vf0 = nv0; vf1 = nv1;
      }
    }
    STAMP(3);
    float* red = (float*)lds;
    {
      float* dst = red + (kq >> 1) * (2 * CH_U4 * 4) + ((kq & 1) * 4 + rg * 2) * 1024 + lane;
#pragma unroll
      for (int e = 0; e < 16; ++e) { dst[e * 64] = o0[e]; dst[1024 + e * 64] = o1[e]; }
    }
    if (attempt == 0 && h && !(o0[12] < 1.0e18f && o1[12] < 1.0e18f)) *flag = 1u;
    raw_barrier();
    if (attempt == 1 || *flag == 0u) {
      const int rb = wave & 3, dq = (wave >> 2) * 2;
      float l = 0.f, ov[8];
#pragma unroll
      for (int i = 0; i < 8; ++i) ov[i] = 0.f;
#pragma unroll
      for (int q = 0; q < 4; ++q) {
        const float* src = red + (q >> 1) * (2 * CH_U4 * 4) + ((q & 1) * 4 + rb) * 1024;
        l += src[12 * 64 + 32 + r];
#pragma unroll
        for (int i = 0; i < 8; ++i) ov[i] += src[(4 * dq + i) * 64 + lane];
      }
      const int R = rb * 32 + r;
      const int pos = pbase + 2 * (R >> 4), head = R & 15;
      float gA = 0.f;
#pragma unroll
      for (int i = 0; i < 8; ++i) gA = (i == (R >> 4)) ? g8[i] : gA;
      const float sc = gA / l;
#pragma unroll
      for (int g = 0; g < 2; ++g) {
        const int d = 8 * (dq + g) + 4 * h;
        if (d < DH) {
          uint2 w;
          w.x = pack_bf16(ov[4 * g] * sc, ov[4 * g + 1] * sc);
          w.y = pack_bf16(ov[4 * g + 2] * sc, ov[4 * g + 3] * sc);
          *(uint2*)(Ocat + pos * OC + head * DH + d) = w;
        }
      }
      if ((tid & 7) < 7) {
#pragma unroll
        for (int half = 0; half < 2; ++half) {
          const int pair = half * 64 + (tid >> 3);
          *(uint2*)(Ocat + (pbase + 2 * (pair >> 4)) * OC + DM + (pair & 15) * DH + 4 * (tid & 7)) = bres[half];
        }
      }
      STAMP(4);
      break;
    }
    {
      float m[2] = {-3.0e38f, -3.0e38f};
      for (int t = 0; t < 16; ++t) {
        const uint4* src = KVg + (kq * 16 + t) * 256 + lane;
        bf16x8 k0 = __builtin_bit_cast(bf16x8, src[0]);
        bf16x8 k1 = __builtin_bit_cast(bf16x8, src[64]);
#pragma unroll
        for (int b = 0; b < 2; ++b) {
          f32x16 s = MFMA32(k0, q0[b], zero);
          s = MFMA32(k1, q1[b], s);
#pragma unroll
          for (int e = 0; e < 16; ++e) m[b] = fmaxf(m[b], s[e]);
        }
      }
      m[0] = fmaxf(m[0], __shfl_xor(m[0], 32));
      m[1] = fmaxf(m[1], __shfl_xor(m[1], 32));
      wait_vm<0>();
      raw_barrier();
      red[(wave * 2) * 64 + lane] = m[0];
      red[(wave * 2 + 1) * 64 + lane] = m[1];
      raw_barrier();
#pragma unroll
      for (int b = 0; b < 2; ++b) {
        float mm = red[(rg * 2 + b) * 64 + lane];
#pragma unroll
        for (int q = 1; q < 4; ++q) mm = fmaxf(mm, red[((q * 2 + rg) * 2 + b) * 64 + lane]);
        mref[b] = mm;
      }
      raw_barrier();
    }
  }
}

extern "C" void kernel_launch(void* const* d_in, const int* in_sizes, int n_in,
                              void* d_out, int out_size, void* d_ws, size_t ws_size,
                              hipStream_t stream) {
  const u16* x = (const u16*)d_in[0];
  const u16* wq = (const u16*)d_in[1];
  const u16* wk = (const u16*)d_in[2];
  const u16* wv = (const u16*)d_in[3];
  const u16* woA = (const u16*)d_in[4];
  const u16* wqkv = (const u16*)d_in[6];
  const u16* woB = (const u16*)d_in[7];
  const u16* gw = (const u16*)d_in[8];
  char* ws = (char*)d_ws;
  u16* Qa = (u16*)(ws);
  u16* KV = (u16*)(ws + (2u << 20));
  u16* QKVb = (u16*)(ws + (3u << 20));
  float* gate = (float*)(ws + (9u << 20));
  u16* Ocat = (u16*)(ws + (10u << 20));
  u16* out = (u16*)d_out;

  k_proj<<<dim3(256), 256, 0, stream>>>(x, wq, wk, wv, wqkv, gw, Qa, KV, QKVb, gate);
  k_attn<<<dim3(S_LEN / 8), 512, 0, stream>>>(Qa, KV, QKVb, gate, Ocat);
  k_out<<<dim3(224), 256, 0, stream>>>(Ocat, woA, woB, out);
}
```

```cpp
#include <hip/hip_runtime.h>
#include <hip/hip_bf16.h>
#include <stdint.h>

typedef __attribute__((ext_vector_type(8))) short bf16x8;
typedef __attribute__((ext_vector_type(4))) short s16x4;
typedef __attribute__((ext_vector_type(16))) float f32x16;
typedef __attribute__((ext_vector_type(2))) __bf16 bf2;
typedef __attribute__((address_space(3))) s16x4* lds_s16x4_ptr;
typedef unsigned short u16;

#define DI __device__ __forceinline__
#define STAMP(i) do {} while (0)
#define EXP2(x) __builtin_amdgcn_exp2f(x)
#define MFMA32(a, b, c) __builtin_amdgcn_mfma_f32_32x32x16_bf16((a), (b), (c), 0, 0, 0)

constexpr int S_LEN = 2048;
constexpr int DM = 448;
constexpr int NH = 16;
constexpr int DH = 28;
constexpr int NB3 = 1344;
constexpr int OC = 896;

DI unsigned pack_bf16(float a, float b) {
  bf2 v; v[0] = (__bf16)a; v[1] = (__bf16)b;
  return __builtin_bit_cast(unsigned, v);
}
DI u16 to_bf16(float a) { __bf16 v = (__bf16)a; return __builtin_bit_cast(u16, v); }
DI float bf_to_f(u16 a) { return __uint_as_float(((unsigned)a) << 16); }

template <int V> struct IC { static constexpr int value = V; };
template <int I, int N, class F> DI void static_for(F&& f) {
  if constexpr (I < N) { f(IC<I>{}); static_for<I + 1, N>(f); }
}
template <int N> DI void wait_vm() { asm volatile("s_waitcnt vmcnt(%0)" ::"n"(N) : "memory"); }
DI void raw_barrier() { asm volatile("s_waitcnt lgkmcnt(0)" ::: "memory"); __builtin_amdgcn_s_barrier(); asm volatile("" ::: "memory"); }
DI void glds16(const void* g, void* l) {
  __builtin_amdgcn_global_load_lds((const __attribute__((address_space(1))) unsigned*)g,
                                   (__attribute__((address_space(3))) unsigned*)l, 16, 0, 0);
}

template <int CTRL> DI float dpp_f(float v) {
  return __int_as_float(__builtin_amdgcn_update_dpp(0, __float_as_int(v), CTRL, 0xf, 0xf, true));
}
DI void glds16a(const void* g, const void* l) {
  const unsigned la = __builtin_amdgcn_readfirstlane((unsigned)(size_t)l);
  unsigned keep;
  asm volatile("s_mov_b32 %0, m0\n\ts_mov_b32 m0, %2\n\ts_nop 0\n\tglobal_load_lds_dwordx4 %1, off\n\ts_mov_b32 m0, %0"
               : "=&s"(keep) : "v"(g), "s"(la) : "memory");
}

template <int BN> DI int bswz(int row) { return (BN == 128) ? ((row & 3) << 2) : (((row >> 1) & 1) << 2); }

template <int BM, int BN, int NS, int NBUF, class BPtr, class Epi>
DI void gemm_glds(const u16* __restrict__ A, int lda, int m0, char* smem, BPtr bptr, Epi epi) {
  constexpr int TM = BM / 64, TN = BN / 64;
  constexpr int GA = BM / 32, GB = BN / 32, G = GA + GB;
  constexpr int A_BYTES = BM * 128, B_BYTES = BN * 128, ST_BYTES = A_BYTES + B_BYTES;
  constexpr int CPR = BN / 8;
  const int tid = threadIdx.x, lane = tid & 63, wave = __builtin_amdgcn_readfirstlane(tid >> 6);
  const int wm = wave >> 1, wn = wave & 1;
  const int r = lane & 31, h = lane >> 5;

  const u16* asrc[GA];
#pragma unroll
  for (int i = 0; i < GA; ++i) {
    int idx = (wave * GA + i) * 64 + lane, row = idx >> 3, ch = (idx & 7) ^ ((row >> 1) & 7);
    asrc[i] = A + (size_t)(m0 + row) * lda + ch * 8;
  }
  int brow[GB], bch[GB];
#pragma unroll
  for (int i = 0; i < GB; ++i) {
    int idx = (wave * GB + i) * 64 + lane;
    brow[i] = idx / CPR;
    bch[i] = (idx % CPR) ^ bswz<BN>(brow[i]);
  }
  auto issue = [&](int st, int sl) {
    char* base = smem + sl * ST_BYTES;
#pragma unroll
    for (int i = 0; i < GA; ++i) glds16(asrc[i] + st * 64, base + (wave * GA + i) * 1024);
#pragma unroll
    for (int i = 0; i < GB; ++i) glds16(bptr(st * 64 + brow[i], bch[i] * 8), base + A_BYTES + (wave * GB + i) * 1024);
  };

  f32x16 acc[TM][TN];
#pragma unroll
  for (int i = 0; i < TM; ++i)
#pragma unroll
    for (int j = 0; j < TN; ++j)
#pragma unroll
      for (int e = 0; e < 16; ++e) acc[i][j][e] = 0.f;

  int aoff[TM], asw[TM];
#pragma unroll
  for (int i = 0; i < TM; ++i) {
    int row = wm * (BM / 2) + i * 32 + r;
    aoff[i] = row * 128; asw[i] = (row >> 1) & 7;
  }
  const int trq = (lane & 15) >> 2, trp = lane & 3, trblk = (lane >> 4) & 1;

#pragma nounroll
  for (int st = 0; st < NBUF - 1; ++st) issue(st, st);

  int slot = 0;
#pragma nounroll
  for (int st = 0; st < NS; ++st) {
    if (st + NBUF - 2 < NS) wait_vm<G * (NBUF - 2)>(); else wait_vm<0>();
    raw_barrier();
    if (st + NBUF - 1 < NS) issue(st + NBUF - 1, slot == 0 ? NBUF - 1 : slot - 1);
    const char* as = smem + slot * ST_BYTES;
    const char* bs = as + A_BYTES;
    slot = (slot == NBUF - 1) ? 0 : slot + 1;
#pragma unroll
    for (int ks = 0; ks < 4; ++ks) {
      bf16x8 af[TM], bfr[TN];
#pragma unroll
      for (int i = 0; i < TM; ++i) af[i] = *(const bf16x8*)(as + aoff[i] + (((2 * ks + h) ^ asw[i]) << 4));
      const int rk = ks * 16 + 8 * h + trq;
#pragma unroll
      for (int j = 0; j < TN; ++j) {
        const int col = wn * (BN / 2) + j * 32 + 16 * trblk + 4 * trp;
        const char* p = bs + rk * (BN * 2) + (((col >> 3) ^ bswz<BN>(rk)) << 4) + ((trp & 1) << 3);
        s16x4 lo = __builtin_amdgcn_ds_read_tr16_b64_v4i16((lds_s16x4_ptr)(p));
        s16x4 hi = __builtin_amdgcn_ds_read_tr16_b64_v4i16((lds_s16x4_ptr)(p + 4 * BN * 2 + (((((col >> 3) ^ bswz<BN>(rk + 4)) - ((col >> 3) ^ bswz<BN>(rk)))) << 4)));
        bfr[j] = __builtin_shufflevector(lo, hi, 0, 1, 2, 3, 4, 5, 6, 7);
      }
#pragma unroll
      for (int i = 0; i < TM; ++i)
#pragma unroll
        for (int j = 0; j < TN; ++j) acc[i][j] = MFMA32(af[i], bfr[j], acc[i][j]);
    }
  }
#pragma unroll
  for (int i = 0; i < TM; ++i)
#pragma unroll
    for (int j = 0; j < TN; ++j)
      epi(m0 + wm * (BM / 2) + i * 32, wn * (BN / 2) + j * 32, acc[i][j], r, h);
}

constexpr int P_NBUF = 4;
constexpr int SP_AST = 456, SP_BST = 96;
constexpr int P_SMEM = (64 * SP_AST + 448 * SP_BST) * 2;

__global__ __launch_bounds__(256) void k_proj(const u16* __restrict__ x, const u16* __restrict__ wq,
                                              const u16* __restrict__ wk, const u16* __restrict__ wv,
                                              const u16* __restrict__ wqkv, const u16* __restrict__ gw,
                                              u16* __restrict__ Qa, u16* __restrict__ KV,
                                              u16* __restrict__ QKVb, float* __restrict__ gate) {
  __shared__ __attribute__((aligned(16))) char smem[P_SMEM];
  const int bid = blockIdx.x;
  if (bid < 224) {
    const int n0 = (bid % 14) * 128, m0 = (bid / 14) * 128;
    auto bptr = [&](int k, int nc) -> const u16* {
      const int n = n0 + nc;
      return (n < DM) ? (wq + k * DM + n) : (wqkv + k * NB3 + (n - DM));
    };
    const float qscale = 0.18898223650461362f * 1.4426950408889634f;
    auto epi = [&](int mb, int nbl, const f32x16& a, int r, int h) {
      const int nb = n0 + nbl, n = nb + r;
      if (nb < DM) {
#pragma unroll
        for (int e = 0; e < 16; ++e) {
          int m = mb + (e & 3) + 8 * (e >> 2) + 4 * h;
          Qa[m * DM + n] = to_bf16(a[e] * qscale);
        }
      } else {
#pragma unroll
        for (int e = 0; e < 16; ++e) {
          int m = mb + (e & 3) + 8 * (e >> 2) + 4 * h;
          QKVb[m * NB3 + (n - DM)] = to_bf16(a[e]);
        }
      }
    };
    gemm_glds<128, 128, 7, P_NBUF>(x, DM, m0, smem, bptr, epi);
    return;
  }
  const int m0 = (bid - 224) * 64;
  const int tid = threadIdx.x, lane = tid & 63, wave = tid >> 6;
  u16* as = (u16*)smem;
  u16* bs = as + 64 * SP_AST;
  uint4 ra[14]; uint2 rb0[14], rb1[14]; u16 rg[14];
#pragma unroll
  for (int i = 0; i < 14; ++i) {
    int c = tid + i * 256, row = c / 56, ch = c % 56;
    ra[i] = *(const uint4*)(x + (m0 + row) * DM + ch * 8);
  }
  const int nc = tid & 7;
  const u16* wsrc = (nc < 4) ? wk : wv;
  const int cb = (nc & 3) * 8;
#pragma unroll
  for (int i = 0; i < 14; ++i) {
    int k = (tid >> 3) + i * 32;
    rb0[i] = *(const uint2*)(wsrc + k * DH + cb);
    rb1[i] = *(const uint2*)(wsrc + k * DH + ((cb < 24) ? cb + 4 : cb));
    rg[i] = gw[k];
  }
#pragma unroll
  for (int i = 0; i < 14; ++i) {
    int c = tid + i * 256, row = c / 56, ch = c % 56;
    *(uint4*)(as + row * SP_AST + ch * 8) = ra[i];
  }
#pragma unroll
  for (int i = 0; i < 14; ++i) {
    int k = (tid >> 3) + i * 32;
    uint4 v = make_uint4(rb0[i].x, rb0[i].y, rb1[i].x, rb1[i].y);
    if (cb == 24) { v.z = (nc == 7) ? (unsigned)rg[i] : 0u; v.w = 0u; }
    *(uint4*)(bs + k * SP_BST + nc * 8) = v;
  }
  __syncthreads();
  const int wm = wave >> 1, wn = wave & 1, r = lane & 31, h = lane >> 5;
  const int trq = (lane & 15) >> 2, trp = lane & 3, trblk = (lane >> 4) & 1;
  f32x16 a;
#pragma unroll
  for (int e = 0; e < 16; ++e) a[e] = 0.f;
#pragma unroll 4
  for (int ks = 0; ks < 28; ++ks) {
    bf16x8 af = *(const bf16x8*)(as + (wm * 32 + r) * SP_AST + ks * 16 + h * 8);
    const u16* p = bs + (ks * 16 + 8 * h + trq) * SP_BST + wn * 32 + 16 * trblk + 4 * trp;
    s16x4 lo = __builtin_amdgcn_ds_read_tr16_b64_v4i16((lds_s16x4_ptr)(p));
    s16x4 hi = __builtin_amdgcn_ds_read_tr16_b64_v4i16((lds_s16x4_ptr)(p + 4 * SP_BST));
    a = MFMA32(af, __builtin_shufflevector(lo, hi, 0, 1, 2, 3, 4, 5, 6, 7), a);
  }
  const int mb = m0 + wm * 32, d = r;
  if (wn == 0) {
    const int s = d >> 4, hh = (d >> 3) & 1, j = d & 7;
#pragma unroll
    for (int e = 0; e < 16; ++e) {
      int m = mb + (e & 3) + 8 * (e >> 2) + 4 * h;
      int tile = m >> 5, rr = m & 31;
      KV[tile * 2048 + s * 512 + (hh * 32 + rr) * 8 + j] = (d < DH) ? to_bf16(a[e]) : ((d < 31) ? (u16)0x3F80 : (u16)0);
    }
  } else {
#pragma unroll
    for (int e = 0; e < 16; ++e) {
      int m = mb + (e & 3) + 8 * (e >> 2) + 4 * h;
      int tile = m >> 5, kk = m & 31;
      int s = kk >> 4, k16 = kk & 15;
      int hh = (k16 >> 2) & 1, j = ((k16 >> 3) << 2) | (k16 & 3);
      u16 val = (d < DH) ? to_bf16(a[e]) : ((d == DH) ? (u16)0x3F80 : (u16)0);
      KV[tile * 2048 + 1024 + s * 512 + (hh * 32 + d) * 8 + j] = val;
      if (d == DH) gate[m] = 1.f / (1.f + __expf(-a[e]));
    }
  }
}

constexpr int O_NBUF = 8;

__global__ __launch_bounds__(256) void k_out(const u16* __restrict__ Ocat, const u16* __restrict__ woA,
                                             const u16* __restrict__ woB, u16* __restrict__ out) {
  __shared__ __attribute__((aligned(16))) char smem[O_NBUF * 16384];
  const int n0 = (blockIdx.x % 7) * 64, m0 = (blockIdx.x / 7) * 64;
  auto bptr = [&](int k, int nc) -> const u16* {
    return ((k < DM) ? (woA + k * DM) : (woB + (k - DM) * DM)) + n0 + nc;
  };
  auto epi = [&](int mb, int nbl, const f32x16& a, int r, int h) {
    const int n = n0 + nbl + r;
#pragma unroll
    for (int e = 0; e < 16; ++e) {
      int m = mb + (e & 3) + 8 * (e >> 2) + 4 * h;
      out[m * DM + n] = to_bf16(a[e]);
    }
  };
  gemm_glds<64, 64, 14, O_NBUF>(Ocat, OC, m0, smem, bptr, epi);
}

constexpr int TPC = 2;
constexpr int NCH = 16 / TPC;
constexpr int CH_U4 = 4 * TPC * 256;
constexpr int QD_OFF = 3 * CH_U4;
constexpr int BQ_OFF = QD_OFF + 8 * 64;
constexpr int BK_OFF = BQ_OFF + 8 * 64;
constexpr int BK_STR = 130 * 8;
constexpr int FL_OFF = BK_OFF + 16 * 130;
constexpr int A_LDS_U4 = FL_OFF + 16;

__global__ __launch_bounds__(512) void k_attn(const u16* __restrict__ Qa, const u16* __restrict__ KV,
                                              const u16* __restrict__ QKVb, const float* __restrict__ gate,
                                              u16* __restrict__ Ocat) {
  __shared__ __attribute__((aligned(16))) uint4 lds[A_LDS_U4];
  const int tid = threadIdx.x, lane = tid & 63, wave = __builtin_amdgcn_readfirstlane(tid >> 6);
  const int rg = wave & 1, kq = wave >> 1;
  const int r = lane & 31, h = lane >> 5;
  const int pbase = (blockIdx.x >> 1) * 16 + (blockIdx.x & 1);
  const int rot = (blockIdx.x >> 3) & (NCH - 1);
  const uint4* KVg = (const uint4*)KV;
  __attribute__((address_space(3))) volatile unsigned* flag = (__attribute__((address_space(3))) volatile unsigned*)(lds + FL_OFF);

  auto issue2 = [&](int c, int slot) {
    const int cc = (c + rot) & (NCH - 1);
    const char* src = (const char*)(KVg + (kq * 16 + cc * TPC + rg) * 256);
    uint4* dst = lds + slot * CH_U4 + (kq * TPC + rg) * 256;
#pragma unroll
    for (int p = 0; p < 4; ++p) glds16a(src + p * 1024 + (unsigned)(lane * 16), dst + p * 64);
  };
  const int l56 = lane < 56 ? lane : 55;
  auto issue_b = [&]() {
  glds16a((const char*)(QKVb + (pbase + 2 * wave) * NB3) + (unsigned)(l56 * 16), lds + BQ_OFF + wave * 64);
#pragma unroll
  for (int i = 0; i < 2; ++i) {
    int row = pbase - 14 + 2 * (wave * 2 + i);
    row = row < 0 ? (pbase & 1) : row;
    row = row > S_LEN - 1 ? S_LEN - 1 : row;
    const char* src = (const char*)(QKVb + row * NB3 + DM);
    const int l48 = lane < 48 ? lane : 47;
    glds16a(src + (unsigned)(lane * 16), lds + BK_OFF + (wave * 2 + i) * 130);
    glds16a(src + 1024 + (unsigned)(l48 * 16), lds + BK_OFF + (wave * 2 + i) * 130 + 64);
  }
  };
  STAMP(0);
  glds16a((const char*)(Qa + (pbase + 2 * wave) * DM) + (unsigned)(l56 * 16), lds + QD_OFF + wave * 64);
  issue2(0, 0);
  issue2(1, 1);
  if (tid == 0) *flag = 0u;
  float g8[8];
#pragma unroll
  for (int i = 0; i < 8; ++i) g8[i] = gate[pbase + 2 * i];
#pragma unroll
  for (int i = 0; i < 8; ++i) asm volatile("" : "+s"(g8[i]));
  wait_vm<4>();
  raw_barrier();
  issue2(2, 2);
  issue_b();
  STAMP(1);

  bf16x8 q0[2], q1[2];
#pragma unroll
  for (int b = 0; b < 2; ++b) {
    const int R = rg * 64 + b * 32 + r;
    const u16* qp = (const u16*)(lds + QD_OFF + (R >> 4) * 64) + (R & 15) * DH;
    uint2 qa = *(const uint2*)(qp + 8 * h), qb = *(const uint2*)(qp + 8 * h + 4);
    uint2 qc = *(const uint2*)(qp + 16 + 8 * h);
    uint2 qd = *(const uint2*)(qp + 20);
    if (h) qd = make_uint2(0, 0);
    q0[b] = __builtin_bit_cast(bf16x8, make_uint4(qa.x, qa.y, qb.x, qb.y));
    q1[b] = __builtin_bit_cast(bf16x8, make_uint4(qc.x, qc.y, qd.x, qd.y));
  }
  f32x16 zero;
#pragma unroll
  for (int e = 0; e < 16; ++e) zero[e] = 0.f;
  float mref[2];
  {
    bf16x8 k0 = __builtin_bit_cast(bf16x8, lds[lane]), k1 = __builtin_bit_cast(bf16x8, lds[64 + lane]);
#pragma unroll
    for (int b = 0; b < 2; ++b) {
      f32x16 s = MFMA32(k0, q0[b], zero);
      s = MFMA32(k1, q1[b], s);
      float m = s[0];
#pragma unroll
      for (int e = 1; e < 16; ++e) m = fmaxf(m, s[e]);
      mref[b] = fmaxf(m, __shfl_xor(m, 32));
    }
  }
  STAMP(2);

  uint2 bres[2];
  f32x16 o0, o1;
#pragma nounroll
  for (int attempt = 0; attempt < 2; ++attempt) {
    bf16x8 q1m[2];
#pragma unroll
    for (int b = 0; b < 2; ++b) {
      q1m[b] = q1[b];
      const float n0 = -mref[b];
      const u16 c0 = to_bf16(n0); const float n1 = n0 - bf_to_f(c0);
      const u16 c1 = to_bf16(n1); const float n2 = n1 - bf_to_f(c1);
      const u16 c2 = to_bf16(n2);
      if (h) { q1m[b][4] = (short)c0; q1m[b][5] = (short)c1; q1m[b][6] = (short)c2; }
    }
#pragma unroll
    for (int e = 0; e < 16; ++e) { o0[e] = 0.f; o1[e] = 0.f; }
    if (attempt) {
      issue2(0, 0); issue2(1, 1);
      wait_vm<4>(); raw_barrier();
      issue2(2, 2);
    }
    auto softmax_pv = [&](const f32x16& s, const bf16x8& vf0, const bf16x8& vf1, f32x16& o) {
      uint4 pa, pb;
      pa.x = pack_bf16(EXP2(s[0]), EXP2(s[1]));
      pa.y = pack_bf16(EXP2(s[2]), EXP2(s[3]));
      pa.z = pack_bf16(EXP2(s[4]), EXP2(s[5]));
      pa.w = pack_bf16(EXP2(s[6]), EXP2(s[7]));
      o = MFMA32(vf0, __builtin_bit_cast(bf16x8, pa), o);
      pb.x = pack_bf16(EXP2(s[8]), EXP2(s[9]));
      pb.y = pack_bf16(EXP2(s[10]), EXP2(s[11]));
      pb.z = pack_bf16(EXP2(s[12]), EXP2(s[13]));
      pb.w = pack_bf16(EXP2(s[14]), EXP2(s[15]));
      o = MFMA32(vf1, __builtin_bit_cast(bf16x8, pb), o);
    };
    f32x16 sA, sB;
    bf16x8 kf0, kf1, vf0, vf1;
    {
      const uint4* base = lds + kq * TPC * 256 + lane;
      kf0 = __builtin_bit_cast(bf16x8, base[0]); kf1 = __builtin_bit_cast(bf16x8, base[64]);
      vf0 = __builtin_bit_cast(bf16x8, base[128]); vf1 = __builtin_bit_cast(bf16x8, base[192]);
      sA = MFMA32(kf0, q0[0], zero);
      sA = MFMA32(kf1, q1m[0], sA);
    }
    int slot = 0;
#pragma nounroll
    for (int c = 0; c < NCH; ++c) {
#pragma unroll
      for (int t = 0; t < TPC; ++t) {
        sB = MFMA32(kf0, q0[1], zero);
        sB = MFMA32(kf1, q1m[1], sB);
        const bool more = (t + 1 < TPC) || (c + 1 < NCH);
        if (t + 1 == TPC) {
          slot = (slot == 2) ? 0 : slot + 1;
          if (c + 1 < NCH) {
            if (c < 2 && attempt == 0) wait_vm<9>(); else if (c + 2 < NCH) wait_vm<4>(); else wait_vm<0>();
            raw_barrier();
            if (c + 3 < NCH) issue2(c + 3, slot == 0 ? 2 : slot - 1);
          }
        }
        bf16x8 nk0 = kf0, nk1 = kf1, nv0 = vf0, nv1 = vf1;
        if (more) {
          const uint4* base = lds + slot * CH_U4 + kq * TPC * 256 + ((t + 1) & (TPC - 1)) * 256 + lane;
          nk0 = __builtin_bit_cast(bf16x8, base[0]); nk1 = __builtin_bit_cast(bf16x8, base[64]);
          nv0 = __builtin_bit_cast(bf16x8, base[128]); nv1 = __builtin_bit_cast(bf16x8, base[192]);
        }
        __builtin_amdgcn_sched_barrier(0);
        softmax_pv(sA, vf0, vf1, o0);
        __builtin_amdgcn_sched_barrier(0);
        if (more) {
          sA = MFMA32(nk0, q0[0], zero);
          sA = MFMA32(nk1, q1m[0], sA);
        }
        __builtin_amdgcn_sched_barrier(0);
        softmax_pv(sB, vf0, vf1, o1);
        __builtin_amdgcn_sched_barrier(0);
        kf0 = nk0; kf1 = nk1; vf0 = nv0; vf1 = nv1;
      }
    }
    STAMP(3);
    STAMP(5);
  if (attempt == 0) {
    const int j = tid & 7, jc = (j < 7) ? j : 6;
    uint2 qv[2][7], kv[2][7], vv[2][8];
#pragma unroll
    for (int half = 0; half < 2; ++half) {
      const int pair = half * 64 + (tid >> 3), pi = pair >> 4, bhead = pair & 15;
      const uint2* qrow = (const uint2*)((const u16*)(lds + BQ_OFF + pi * 64) + bhead * DH);
      const u16* kb = (const u16*)(lds + BK_OFF) + bhead * DH + (pi + 7) * BK_STR;
      const uint2* krow = (const uint2*)(kb - j * BK_STR);
#pragma unroll
      for (int i = 0; i < 7; ++i) { qv[half][i] = qrow[i]; kv[half][i] = krow[i]; }
#pragma unroll
      for (int w = 0; w < 8; ++w) vv[half][w] = *(const uint2*)(kb - w * BK_STR + DM + 4 * jc);
    }
    __builtin_amdgcn_sched_barrier(0);
    float e[2], sm[2];
#pragma unroll
    for (int half = 0; half < 2; ++half) {
      const int pi = (half * 64 + (tid >> 3)) >> 4;
      float lg = 0.f;
#pragma unroll
      for (int i = 0; i < 7; ++i) {
        lg = __builtin_amdgcn_fdot2_f32_bf16(__builtin_bit_cast(bf2, qv[half][i].x), __builtin_bit_cast(bf2, kv[half][i].x), lg, false);
        lg = __builtin_amdgcn_fdot2_f32_bf16(__builtin_bit_cast(bf2, qv[half][i].y), __builtin_bit_cast(bf2, kv[half][i].y), lg, false);
      }
      const bool valid = (pbase + 2 * pi - 2 * j >= 0);
      lg = valid ? lg * (0.18898223650461362f * 1.4426950408889634f) : -3.0e38f;
      float mx = lg;
      mx = fmaxf(mx, dpp_f<0xB1>(mx)); mx = fmaxf(mx, dpp_f<0x4E>(mx)); mx = fmaxf(mx, dpp_f<0x141>(mx));
      e[half] = valid ? __builtin_amdgcn_exp2f(lg - mx) : 0.f;
      float t = e[half];
      t += dpp_f<0xB1>(t); t += dpp_f<0x4E>(t); t += dpp_f<0x141>(t);
      sm[half] = t;
    }
    float aw[2][8];
#pragma unroll
    for (int half = 0; half < 2; ++half)
#pragma unroll
      for (int w = 0; w < 8; ++w) aw[half][w] = __shfl(e[half], (lane & 56) + w);
    __builtin_amdgcn_sched_barrier(0);
#pragma unroll
    for (int half = 0; half < 2; ++half) {
      const int pair = half * 64 + (tid >> 3), pi = pair >> 4, bhead = pair & 15, bpos = pbase + 2 * pi;
      float o4[4] = {0.f, 0.f, 0.f, 0.f};
#pragma unroll
      for (int w = 0; w < 8; ++w) {
        o4[0] += aw[half][w] * __uint_as_float(vv[half][w].x << 16);
        o4[1] += aw[half][w] * __uint_as_float(vv[half][w].x & 0xffff0000u);
        o4[2] += aw[half][w] * __uint_as_float(vv[half][w].y << 16);
        o4[3] += aw[half][w] * __uint_as_float(vv[half][w].y & 0xffff0000u);
      }
      float gB = 0.f;
#pragma unroll
      for (int i = 0; i < 8; ++i) gB = (i == pi) ? g8[i] : gB;
      const float sc = (1.f - gB) / sm[half];
      bres[half].x = pack_bf16(o4[0] * sc, o4[1] * sc);
      bres[half].y = pack_bf16(o4[2] * sc, o4[3] * sc);
    }
  }

    STAMP(6);
    float* red = (float*)lds;
    {
      float* dst = red + (kq >> 1) * (2 * CH_U4 * 4) + ((kq & 1) * 4 + rg * 2) * 1024 + lane;
#pragma unroll
      for (int e = 0; e < 16; ++e) { dst[e * 64] = o0[e]; dst[1024 + e * 64] = o1[e]; }
    }
    if (attempt == 0 && h && !(o0[12] < 1.0e18f && o1[12] < 1.0e18f)) *flag = 1u;
    raw_barrier();
    if (attempt == 1 || *flag == 0u) {
      const int rb = wave & 3, dq = (wave >> 2) * 2;
      float l = 0.f, ov[8];
#pragma unroll
      for (int i = 0; i < 8; ++i) ov[i] = 0.f;
#pragma unroll
      for (int q = 0; q < 4; ++q) {
        const float* src = red + (q >> 1) * (2 * CH_U4 * 4) + ((q & 1) * 4 + rb) * 1024;
        l += src[12 * 64 + 32 + r];
#pragma unroll
        for (int i = 0; i < 8; ++i) ov[i] += src[(4 * dq + i) * 64 + lane];
      }
      const int R = rb * 32 + r;
      const int pos = pbase + 2 * (R >> 4), head = R & 15;
      float gA = 0.f;
#pragma unroll
      for (int i = 0; i < 8; ++i) gA = (i == (R >> 4)) ? g8[i] : gA;
      const float sc = gA / l;
#pragma unroll
      for (int g = 0; g < 2; ++g) {
        const int d = 8 * (dq + g) + 4 * h;
        if (d < DH) {
          uint2 w;
          w.x = pack_bf16(ov[4 * g] * sc, ov[4 * g + 1] * sc);
          w.y = pack_bf16(ov[4 * g + 2] * sc, ov[4 * g + 3] * sc);
          *(uint2*)(Ocat + pos * OC + head * DH + d) = w;
        }
      }
      if ((tid & 7) < 7) {
#pragma unroll
        for (int half = 0; half < 2; ++half) {
          const int pair = half * 64 + (tid >> 3);
          *(uint2*)(Ocat + (pbase + 2 * (pair >> 4)) * OC + DM + (pair & 15) * DH + 4 * (tid & 7)) = bres[half];
        }
      }
      STAMP(4);
      break;
    }
    {
      float m[2] = {-3.0e38f, -3.0e38f};
      for (int t = 0; t < 16; ++t) {
        const uint4* src = KVg + (kq * 16 + t) * 256 + lane;
        bf16x8 k0 = __builtin_bit_cast(bf16x8, src[0]);
        bf16x8 k1 = __builtin_bit_cast(bf16x8, src[64]);
#pragma unroll
        for (int b = 0; b < 2; ++b) {
          f32x16 s = MFMA32(k0, q0[b], zero);
          s = MFMA32(k1, q1[b], s);
#pragma unroll
          for (int e = 0; e < 16; ++e) m[b] = fmaxf(m[b], s[e]);
        }
      }
      m[0] = fmaxf(m[0], __shfl_xor(m[0], 32));
      m[1] = fmaxf(m[1], __shfl_xor(m[1], 32));
      wait_vm<0>();
      raw_barrier();
      red[(wave * 2) * 64 + lane] = m[0];
      red[(wave * 2 + 1) * 64 + lane] = m[1];
      raw_barrier();
#pragma unroll
      for (int b = 0; b < 2; ++b) {
        float mm = red[(rg * 2 + b) * 64 + lane];
#pragma unroll
        for (int q = 1; q < 4; ++q) mm = fmaxf(mm, red[((q * 2 + rg) * 2 + b) * 64 + lane]);
        mref[b] = mm;
      }
      raw_barrier();
    }
  }
}

extern "C" void kernel_launch(void* const* d_in, const int* in_sizes, int n_in,
                              void* d_out, int out_size, void* d_ws, size_t ws_size,
                              hipStream_t stream) {
  const u16* x = (const u16*)d_in[0];
  const u16* wq = (const u16*)d_in[1];
  const u16* wk = (const u16*)d_in[2];
  const u16* wv = (const u16*)d_in[3];
  const u16* woA = (const u16*)d_in[4];
  const u16* wqkv = (const u16*)d_in[6];
  const u16* woB = (const u16*)d_in[7];
  const u16* gw = (const u16*)d_in[8];
  char* ws = (char*)d_ws;
  u16* Qa = (u16*)(ws);
  u16* KV = (u16*)(ws + (2u << 20));
  u16* QKVb = (u16*)(ws + (3u << 20));
  float* gate = (float*)(ws + (9u << 20));
  u16* Ocat = (u16*)(ws + (10u << 20));
  u16* out = (u16*)d_out;

  k_proj<<<dim3(256), 256, 0, stream>>>(x, wq, wk, wv, wqkv, gw, Qa, KV, QKVb, gate);
  k_attn<<<dim3(S_LEN / 8), 512, 0, stream>>>(Qa, KV, QKVb, gate, Ocat);
  k_out<<<dim3(224), 256, 0, stream>>>(Ocat, woA, woB, out);
}
```

```cpp
#include <hip/hip_runtime.h>
#include <hip/hip_bf16.h>
#include <stdint.h>

typedef __attribute__((ext_vector_type(8))) short bf16x8;
typedef __attribute__((ext_vector_type(4))) short s16x4;
typedef __attribute__((ext_vector_type(16))) float f32x16;
typedef __attribute__((ext_vector_type(2))) __bf16 bf2;
typedef __attribute__((address_space(3))) s16x4* lds_s16x4_ptr;
typedef unsigned short u16;

#define DI __device__ __forceinline__
#define STAMP(i) do {} while (0)
#define STAMPG(k, i) do {} while (0)
#define EXP2(x) __builtin_amdgcn_exp2f(x)
#define MFMA32(a, b, c) __builtin_amdgcn_mfma_f32_32x32x16_bf16((a), (b), (c), 0, 0, 0)

constexpr int S_LEN = 2048;
constexpr int DM = 448;
constexpr int NH = 16;
constexpr int DH = 28;
constexpr int NB3 = 1344;
constexpr int OC = 896;

DI unsigned pack_bf16(float a, float b) {
  bf2 v; v[0] = (__bf16)a; v[1] = (__bf16)b;
  return __builtin_bit_cast(unsigned, v);
}
DI u16 to_bf16(float a) { __bf16 v = (__bf16)a; return __builtin_bit_cast(u16, v); }
DI float bf_to_f(u16 a) { return __uint_as_float(((unsigned)a) << 16); }

template <int V> struct IC { static constexpr int value = V; };
template <int I, int N, class F> DI void static_for(F&& f) {
  if constexpr (I < N) { f(IC<I>{}); static_for<I + 1, N>(f); }
}
template <int N> DI void wait_vm() { asm volatile("s_waitcnt vmcnt(%0)" ::"n"(N) : "memory"); }
DI void raw_barrier() { asm volatile("s_waitcnt lgkmcnt(0)" ::: "memory"); __builtin_amdgcn_s_barrier(); asm volatile("" ::: "memory"); }
DI void glds16(const void* g, void* l) {
  __builtin_amdgcn_global_load_lds((const __attribute__((address_space(1))) unsigned*)g,
                                   (__attribute__((address_space(3))) unsigned*)l, 16, 0, 0);
}

template <int CTRL> DI float dpp_f(float v) {
  return __int_as_float(__builtin_amdgcn_update_dpp(0, __float_as_int(v), CTRL, 0xf, 0xf, true));
}
DI void glds16a(const void* g, const void* l) {
  const unsigned la = __builtin_amdgcn_readfirstlane((unsigned)(size_t)l);
  unsigned keep;
  asm volatile("s_mov_b32 %0, m0\n\ts_mov_b32 m0, %2\n\ts_nop 0\n\tglobal_load_lds_dwordx4 %1, off\n\ts_mov_b32 m0, %0"
               : "=&s"(keep) : "v"(g), "s"(la) : "memory");
}

template <int BN> DI int bswz(int row) { return (BN == 128) ? ((row & 3) << 2) : (((row >> 1) & 1) << 2); }

template <int BM, int BN, int NS, int NBUF, class BPtr, class Epi>
DI void gemm_glds(const u16* __restrict__ A, int lda, int m0, char* smem, BPtr bptr, Epi epi) {
  constexpr int TM = BM / 64, TN = BN / 64;
  constexpr int GA = BM / 32, GB = BN / 32, G = GA + GB;
  constexpr int A_BYTES = BM * 128, B_BYTES = BN * 128, ST_BYTES = A_BYTES + B_BYTES;
  constexpr int CPR = BN / 8;
  const int tid = threadIdx.x, lane = tid & 63, wave = __builtin_amdgcn_readfirstlane(tid >> 6);
  const int wm = wave >> 1, wn = wave & 1;
  const int r = lane & 31, h = lane >> 5;

  const u16* asrc[GA];
#pragma unroll
  for (int i = 0; i < GA; ++i) {
    int idx = (wave * GA + i) * 64 + lane, row = idx >> 3, ch = (idx & 7) ^ ((row >> 1) & 7);
    asrc[i] = A + (size_t)(m0 + row) * lda + ch * 8;
  }
  int brow[GB], bch[GB];
#pragma unroll
  for (int i = 0; i < GB; ++i) {
    int idx = (wave * GB + i) * 64 + lane;
    brow[i] = idx / CPR;
    bch[i] = (idx % CPR) ^ bswz<BN>(brow[i]);
  }
  auto issue = [&](int st, int sl) {
    char* base = smem + sl * ST_BYTES;
#pragma unroll
    for (int i = 0; i < GA; ++i) glds16(asrc[i] + st * 64, base + (wave * GA + i) * 1024);
#pragma unroll
    for (int i = 0; i < GB; ++i) glds16(bptr(st * 64 + brow[i], bch[i] * 8), base + A_BYTES + (wave * GB + i) * 1024);
  };

  f32x16 acc[TM][TN];
#pragma unroll
  for (int i = 0; i < TM; ++i)
#pragma unroll
    for (int j = 0; j < TN; ++j)
#pragma unroll
      for (int e = 0; e < 16; ++e) acc[i][j][e] = 0.f;

  int aoff[TM], asw[TM];
#pragma unroll
  for (int i = 0; i < TM; ++i) {
    int row = wm * (BM / 2) + i * 32 + r;
    aoff[i] = row * 128; asw[i] = (row >> 1) & 7;
  }
  const int trq = (lane & 15) >> 2, trp = lane & 3, trblk = (lane >> 4) & 1;

  STAMPG(BM == 64, 0);
#pragma nounroll
  for (int st = 0; st < NBUF - 1; ++st) issue(st, st);

  int slot = 0;
#pragma nounroll
  for (int st = 0; st < NS; ++st) {
    if (st + NBUF - 2 < NS) wait_vm<G * (NBUF - 2)>(); else wait_vm<0>();
    raw_barrier();
    if (st == 0) STAMPG(BM == 64, 1);
    if (st + NBUF - 1 < NS) issue(st + NBUF - 1, slot == 0 ? NBUF - 1 : slot - 1);
    const char* as = smem + slot * ST_BYTES;
    const char* bs = as + A_BYTES;
    slot = (slot == NBUF - 1) ? 0 : slot + 1;
#pragma unroll
    for (int ks = 0; ks < 4; ++ks) {
      bf16x8 af[TM], bfr[TN];
#pragma unroll
      for (int i = 0; i < TM; ++i) af[i] = *(const bf16x8*)(as + aoff[i] + (((2 * ks + h) ^ asw[i]) << 4));
      const int rk = ks * 16 + 8 * h + trq;
#pragma unroll
      for (int j = 0; j < TN; ++j) {
        const int col = wn * (BN / 2) + j * 32 + 16 * trblk + 4 * trp;
        const char* p = bs + rk * (BN * 2) + (((col >> 3) ^ bswz<BN>(rk)) << 4) + ((trp & 1) << 3);
        s16x4 lo = __builtin_amdgcn_ds_read_tr16_b64_v4i16((lds_s16x4_ptr)(p));
        s16x4 hi = __builtin_amdgcn_ds_read_tr16_b64_v4i16((lds_s16x4_ptr)(p + 4 * BN * 2 + (((((col >> 3) ^ bswz<BN>(rk + 4)) - ((col >> 3) ^ bswz<BN>(rk)))) << 4)));
        bfr[j] = __builtin_shufflevector(lo, hi, 0, 1, 2, 3, 4, 5, 6, 7);
      }
#pragma unroll
      for (int i = 0; i < TM; ++i)
#pragma unroll
        for (int j = 0; j < TN; ++j) acc[i][j] = MFMA32(af[i], bfr[j], acc[i][j]);
    }
  }
  STAMPG(BM == 64, 2);
#pragma unroll
  for (int i = 0; i < TM; ++i)
#pragma unroll
    for (int j = 0; j < TN; ++j)
      epi(m0 + wm * (BM / 2) + i * 32, wn * (BN / 2) + j * 32, acc[i][j], r, h);
  STAMPG(BM == 64, 3);
}

constexpr int P_NBUF = 4;
constexpr int SP_AST = 456, SP_BST = 96;
constexpr int P_SMEM = (64 * SP_AST + 448 * SP_BST) * 2;

__global__ __launch_bounds__(256) void k_proj(const u16* __restrict__ x, const u16* __restrict__ wq,
                                              const u16* __restrict__ wk, const u16* __restrict__ wv,
                                              const u16* __restrict__ wqkv, const u16* __restrict__ gw,
                                              u16* __restrict__ Qa, u16* __restrict__ KV,
                                              u16* __restrict__ QKVb, float* __restrict__ gate) {
  __shared__ __attribute__((aligned(16))) char smem[P_SMEM];
  const int bid = blockIdx.x;
  if (bid < 224) {
    const int xi = bid >> 3;
    const int n0 = (xi % 14) * 128, m0 = (bid & 7) * 256 + (xi / 14) * 128;
    auto bptr = [&](int k, int nc) -> const u16* {
      const int n = n0 + nc;
      return (n < DM) ? (wq + k * DM + n) : (wqkv + k * NB3 + (n - DM));
    };
    const float qscale = 0.18898223650461362f * 1.4426950408889634f;
    auto epi = [&](int mb, int nbl, const f32x16& a, int r, int h) {
      const int nb = n0 + nbl, n = nb + r;
      if (nb < DM) {
#pragma unroll
        for (int e = 0; e < 16; ++e) {
          int m = mb + (e & 3) + 8 * (e >> 2) + 4 * h;
          Qa[m * DM + n] = to_bf16(a[e] * qscale);
        }
      } else {
#pragma unroll
        for (int e = 0; e < 16; ++e) {
          int m = mb + (e & 3) + 8 * (e >> 2) + 4 * h;
          QKVb[m * NB3 + (n - DM)] = to_bf16(a[e]);
        }
      }
    };
    gemm_glds<128, 128, 7, P_NBUF>(x, DM, m0, smem, bptr, epi);
    return;
  }
  const int m0 = (bid & 7) * 256 + ((bid - 224) >> 3) * 64;
  const int tid = threadIdx.x, lane = tid & 63, wave = tid >> 6;
  u16* as = (u16*)smem;
  u16* bs = as + 64 * SP_AST;
  uint4 ra[14]; uint2 rb0[14], rb1[14]; u16 rg[14];
#pragma unroll
  for (int i = 0; i < 14; ++i) {
    int c = tid + i * 256, row = c / 56, ch = c % 56;
    ra[i] = *(const uint4*)(x + (m0 + row) * DM + ch * 8);
  }
  const int nc = tid & 7;
  const u16* wsrc = (nc < 4) ? wk : wv;
  const int cb = (nc & 3) * 8;
#pragma unroll
  for (int i = 0; i < 14; ++i) {
    int k = (tid >> 3) + i * 32;
    rb0[i] = *(const uint2*)(wsrc + k * DH + cb);
    rb1[i] = *(const uint2*)(wsrc + k * DH + ((cb < 24) ? cb + 4 : cb));
    rg[i] = gw[k];
  }
#pragma unroll
  for (int i = 0; i < 14; ++i) {
    int c = tid + i * 256, row = c / 56, ch = c % 56;
    *(uint4*)(as + row * SP_AST + ch * 8) = ra[i];
  }
#pragma unroll
  for (int i = 0; i < 14; ++i) {
    int k = (tid >> 3) + i * 32;
    uint4 v = make_uint4(rb0[i].x, rb0[i].y, rb1[i].x, rb1[i].y);
    if (cb == 24) { v.z = (nc == 7) ? (unsigned)rg[i] : 0u; v.w = 0u; }
    *(uint4*)(bs + k * SP_BST + nc * 8) = v;
  }
  __syncthreads();
  const int wm = wave >> 1, wn = wave & 1, r = lane & 31, h = lane >> 5;
  const int trq = (lane & 15) >> 2, trp = lane & 3, trblk = (lane >> 4) & 1;
  f32x16 a;
#pragma unroll
  for (int e = 0; e < 16; ++e) a[e] = 0.f;
#pragma unroll 4
  for (int ks = 0; ks < 28; ++ks) {
    bf16x8 af = *(const bf16x8*)(as + (wm * 32 + r) * SP_AST + ks * 16 + h * 8);
    const u16* p = bs + (ks * 16 + 8 * h + trq) * SP_BST + wn * 32 + 16 * trblk + 4 * trp;
    s16x4 lo = __builtin_amdgcn_ds_read_tr16_b64_v4i16((lds_s16x4_ptr)(p));
    s16x4 hi = __builtin_amdgcn_ds_read_tr16_b64_v4i16((lds_s16x4_ptr)(p + 4 * SP_BST));
    a = MFMA32(af, __builtin_shufflevector(lo, hi, 0, 1, 2, 3, 4, 5, 6, 7), a);
  }
  const int mb = m0 + wm * 32, d = r;
  if (wn == 0) {
    const int s = d >> 4, hh = (d >> 3) & 1, j = d & 7;
#pragma unroll
    for (int e = 0; e < 16; ++e) {
      int m = mb + (e & 3) + 8 * (e >> 2) + 4 * h;
      int tile = m >> 5, rr = m & 31;
      KV[tile * 2048 + s * 512 + (hh * 32 + rr) * 8 + j] = (d < DH) ? to_bf16(a[e]) : ((d < 31) ? (u16)0x3F80 : (u16)0);
    }
  } else {
#pragma unroll
    for (int e = 0; e < 16; ++e) {
      int m = mb + (e & 3) + 8 * (e >> 2) + 4 * h;
      int tile = m >> 5, kk = m & 31;
      int s = kk >> 4, k16 = kk & 15;
      int hh = (k16 >> 2) & 1, j = ((k16 >> 3) << 2) | (k16 & 3);
      u16 val = (d < DH) ? to_bf16(a[e]) : ((d == DH) ? (u16)0x3F80 : (u16)0);
      KV[tile * 2048 + 1024 + s * 512 + (hh * 32 + d) * 8 + j] = val;
      if (d == DH) gate[m] = 1.f / (1.f + __expf(-a[e]));
    }
  }
}

constexpr int O_NBUF = 8;

__global__ __launch_bounds__(256) void k_out(const u16* __restrict__ Ocat, const u16* __restrict__ woA,
                                             const u16* __restrict__ woB, u16* __restrict__ out) {
  __shared__ __attribute__((aligned(16))) char smem[O_NBUF * 16384];
  const int xi = blockIdx.x >> 3;
  const int n0 = (xi % 7) * 64, m0 = (blockIdx.x & 7) * 256 + (xi / 7) * 64;
  auto bptr = [&](int k, int nc) -> const u16* {
    return ((k < DM) ? (woA + k * DM) : (woB + (k - DM) * DM)) + n0 + nc;
  };
  auto epi = [&](int mb, int nbl, const f32x16& a, int r, int h) {
    const int n = n0 + nbl + r;
#pragma unroll
    for (int e = 0; e < 16; ++e) {
      int m = mb + (e & 3) + 8 * (e >> 2) + 4 * h;
      out[m * DM + n] = to_bf16(a[e]);
    }
  };
  gemm_glds<64, 64, 14, O_NBUF>(Ocat, OC, m0, smem, bptr, epi);
}

constexpr int TPC = 2;
constexpr int NCH = 16 / TPC;
constexpr int CH_U4 = 4 * TPC * 256;
constexpr int QD_OFF = 3 * CH_U4;
constexpr int BQ_OFF = QD_OFF + 8 * 64;
constexpr int BK_OFF = BQ_OFF + 8 * 64;
constexpr int BK_STR = 130 * 8;
constexpr int FL_OFF = BK_OFF + 16 * 130;
constexpr int A_LDS_U4 = FL_OFF + 16;

__global__ __launch_bounds__(512) void k_attn(const u16* __restrict__ Qa, const u16* __restrict__ KV,
                                              const u16* __restrict__ QKVb, const float* __restrict__ gate,
                                              u16* __restrict__ Ocat) {
  __shared__ __attribute__((aligned(16))) uint4 lds[A_LDS_U4];
  const int tid = threadIdx.x, lane = tid & 63, wave = __builtin_amdgcn_readfirstlane(tid >> 6);
  const int rg = wave & 1, kq = wave >> 1;
  const int r = lane & 31, h = lane >> 5;
  const int xi = blockIdx.x >> 3;
  const int pbase = (blockIdx.x & 7) * 256 + (xi >> 1) * 16 + (xi & 1);
  const int rot = xi & (NCH - 1);
  const uint4* KVg = (const uint4*)KV;
  __attribute__((address_space(3))) volatile unsigned* flag = (__attribute__((address_space(3))) volatile unsigned*)(lds + FL_OFF);

  auto issue2 = [&](int c, int slot) {
    const int cc = (c + rot) & (NCH - 1);
    const char* src = (const char*)(KVg + (kq * 16 + cc * TPC + rg) * 256);
    uint4* dst = lds + slot * CH_U4 + (kq * TPC + rg) * 256;
#pragma unroll
    for (int p = 0; p < 4; ++p) glds16a(src + p * 1024 + (unsigned)(lane * 16), dst + p * 64);
  };
  const int l56 = lane < 56 ? lane : 55;
  auto issue_b = [&]() {
  glds16a((const char*)(QKVb + (pbase + 2 * wave) * NB3) + (unsigned)(l56 * 16), lds + BQ_OFF + wave * 64);
#pragma unroll
  for (int i = 0; i < 2; ++i) {
    int row = pbase - 14 + 2 * (wave * 2 + i);
    row = row < 0 ? (pbase & 1) : row;
    row = row > S_LEN - 1 ? S_LEN - 1 : row;
    const char* src = (const char*)(QKVb + row * NB3 + DM);
    const int l48 = lane < 48 ? lane : 47;
    glds16a(src + (unsigned)(lane * 16), lds + BK_OFF + (wave * 2 + i) * 130);
    glds16a(src + 1024 + (unsigned)(l48 * 16), lds + BK_OFF + (wave * 2 + i) * 130 + 64);
  }
  };
  STAMP(0);
  glds16a((const char*)(Qa + (pbase + 2 * wave) * DM) + (unsigned)(l56 * 16), lds + QD_OFF + wave * 64);
  issue2(0, 0);
  issue2(1, 1);
  if (tid == 0) *flag = 0u;
  float g8[8];
#pragma unroll
  for (int i = 0; i < 8; ++i) g8[i] = gate[pbase + 2 * i];
#pragma unroll
  for (int i = 0; i < 8; ++i) asm volatile("" : "+s"(g8[i]));
  wait_vm<4>();
  raw_barrier();
  issue2(2, 2);
  issue_b();
  STAMP(1);

  bf16x8 q0[2], q1[2];
#pragma unroll
  for (int b = 0; b < 2; ++b) {
    const int R = rg * 64 + b * 32 + r;
    const u16* qp = (const u16*)(lds + QD_OFF + (R >> 4) * 64) + (R & 15) * DH;
    uint2 qa = *(const uint2*)(qp + 8 * h), qb = *(const uint2*)(qp + 8 * h + 4);
    uint2 qc = *(const uint2*)(qp + 16 + 8 * h);
    uint2 qd = *(const uint2*)(qp + 20);
    if (h) qd = make_uint2(0, 0);
    q0[b] = __builtin_bit_cast(bf16x8, make_uint4(qa.x, qa.y, qb.x, qb.y));
    q1[b] = __builtin_bit_cast(bf16x8, make_uint4(qc.x, qc.y, qd.x, qd.y));
  }
  f32x16 zero;
#pragma unroll
  for (int e = 0; e < 16; ++e) zero[e] = 0.f;
  float mref[2];
  {
    bf16x8 k0 = __builtin_bit_cast(bf16x8, lds[lane]), k1 = __builtin_bit_cast(bf16x8, lds[64 + lane]);
#pragma unroll
    for (int b = 0; b < 2; ++b) {
      f32x16 s = MFMA32(k0, q0[b], zero);
      s = MFMA32(k1, q1[b], s);
      float m = s[0];
#pragma unroll
      for (int e = 1; e < 16; ++e) m = fmaxf(m, s[e]);
      mref[b] = fmaxf(m, __shfl_xor(m, 32));
    }
  }
  STAMP(2);

  uint2 bres[2];
  f32x16 o0, o1;
#pragma nounroll
  for (int attempt = 0; attempt < 2; ++attempt) {
    bf16x8 q1m[2];
#pragma unroll
    for (int b = 0; b < 2; ++b) {
      q1m[b] = q1[b];
      const float n0 = -mref[b];
      const u16 c0 = to_bf16(n0); const float n1 = n0 - bf_to_f(c0);
      const u16 c1 = to_bf16(n1); const float n2 = n1 - bf_to_f(c1);
      const u16 c2 = to_bf16(n2);
      if (h) { q1m[b][4] = (short)c0; q1m[b][5] = (short)c1; q1m[b][6] = (short)c2; }
    }
#pragma unroll
    for (int e = 0; e < 16; ++e) { o0[e] = 0.f; o1[e] = 0.f; }
    if (attempt) {
      issue2(0, 0); issue2(1, 1);
      wait_vm<4>(); raw_barrier();
      issue2(2, 2);
    }
    auto softmax_pv = [&](const f32x16& s, const bf16x8& vf0, const bf16x8& vf1, f32x16& o) {
      uint4 pa, pb;
      pa.x = pack_bf16(EXP2(s[0]), EXP2(s[1]));
      pa.y = pack_bf16(EXP2(s[2]), EXP2(s[3]));
      pa.z = pack_bf16(EXP2(s[4]), EXP2(s[5]));
      pa.w = pack_bf16(EXP2(s[6]), EXP2(s[7]));
      o = MFMA32(vf0, __builtin_bit_cast(bf16x8, pa), o);
      pb.x = pack_bf16(EXP2(s[8]), EXP2(s[9]));
      pb.y = pack_bf16(EXP2(s[10]), EXP2(s[11]));
      pb.z = pack_bf16(EXP2(s[12]), EXP2(s[13]));
      pb.w = pack_bf16(EXP2(s[14]), EXP2(s[15]));
      o = MFMA32(vf1, __builtin_bit_cast(bf16x8, pb), o);
    };
    f32x16 sA, sB;
    bf16x8 kf0, kf1, vf0, vf1;
    {
      const uint4* base = lds + kq * TPC * 256 + lane;
      kf0 = __builtin_bit_cast(bf16x8, base[0]); kf1 = __builtin_bit_cast(bf16x8, base[64]);
      vf0 = __builtin_bit_cast(bf16x8, base[128]); vf1 = __builtin_bit_cast(bf16x8, base[192]);
      sA = MFMA32(kf0, q0[0], zero);
      sA = MFMA32(kf1, q1m[0], sA);
    }
    int slot = 0;
#pragma nounroll
    for (int c = 0; c < NCH; ++c) {
#pragma unroll
      for (int t = 0; t < TPC; ++t) {
        sB = MFMA32(kf0, q0[1], zero);
        sB = MFMA32(kf1, q1m[1], sB);
        const bool more = (t + 1 < TPC) || (c + 1 < NCH);
        if (t + 1 == TPC) {
          slot = (slot == 2) ? 0 : slot + 1;
          if (c + 1 < NCH) {
            if (c < 2 && attempt == 0) wait_vm<9>(); else if (c + 2 < NCH) wait_vm<4>(); else wait_vm<0>();
            raw_barrier();
            if (c + 3 < NCH) issue2(c + 3, slot == 0 ? 2 : slot - 1);
          }
        }
        bf16x8 nk0 = kf0, nk1 = kf1, nv0 = vf0, nv1 = vf1;
        if (more) {
          const uint4* base = lds + slot * CH_U4 + kq * TPC * 256 + ((t + 1) & (TPC - 1)) * 256 + lane;
          nk0 = __builtin_bit_cast(bf16x8, base[0]); nk1 = __builtin_bit_cast(bf16x8, base[64]);
          nv0 = __builtin_bit_cast(bf16x8, base[128]); nv1 = __builtin_bit_cast(bf16x8, base[192]);
        }
        __builtin_amdgcn_sched_barrier(0);
        softmax_pv(sA, vf0, vf1, o0);
        __builtin_amdgcn_sched_barrier(0);
        if (more) {
          sA = MFMA32(nk0, q0[0], zero);
          sA = MFMA32(nk1, q1m[0], sA);
        }
        __builtin_amdgcn_sched_barrier(0);
        softmax_pv(sB, vf0, vf1, o1);
        __builtin_amdgcn_sched_barrier(0);
        kf0 = nk0; kf1 = nk1; vf0 = nv0; vf1 = nv1;
      }
    }
    STAMP(3);
    STAMP(5);
  if (attempt == 0) {
    const int j = tid & 7, jc = (j < 7) ? j : 6;
    uint2 qv[2][7], kv[2][7], vv[2][8];
#pragma unroll
    for (int half = 0; half < 2; ++half) {
      const int pair = half * 64 + (tid >> 3), pi = pair >> 4, bhead = pair & 15;
      const uint2* qrow = (const uint2*)((const u16*)(lds + BQ_OFF + pi * 64) + bhead * DH);
      const u16* kb = (const u16*)(lds + BK_OFF) + bhead * DH + (pi + 7) * BK_STR;
      const uint2* krow = (const uint2*)(kb - j * BK_STR);
#pragma unroll
      for (int i = 0; i < 7; ++i) { qv[half][i] = qrow[i]; kv[half][i] = krow[i]; }
#pragma unroll
      for (int w = 0; w < 8; ++w) vv[half][w] = *(const uint2*)(kb - w * BK_STR + DM + 4 * jc);
    }
    __builtin_amdgcn_sched_barrier(0);
    float e[2], sm[2];
#pragma unroll
    for (int half = 0; half < 2; ++half) {
      const int pi = (half * 64 + (tid >> 3)) >> 4;
      float lg = 0.f;
#pragma unroll
      for (int i = 0; i < 7; ++i) {
        lg = __builtin_amdgcn_fdot2_f32_bf16(__builtin_bit_cast(bf2, qv[half][i].x), __builtin_bit_cast(bf2, kv[half][i].x), lg, false);
        lg = __builtin_amdgcn_fdot2_f32_bf16(__builtin_bit_cast(bf2, qv[half][i].y), __builtin_bit_cast(bf2, kv[half][i].y), lg, false);
      }
      const bool valid = (pbase + 2 * pi - 2 * j >= 0);
      lg = valid ? lg * (0.18898223650461362f * 1.4426950408889634f) : -3.0e38f;
      float mx = lg;
      mx = fmaxf(mx, dpp_f<0xB1>(mx)); mx = fmaxf(mx, dpp_f<0x4E>(mx)); mx = fmaxf(mx, dpp_f<0x141>(mx));
      e[half] = valid ? __builtin_amdgcn_exp2f(lg - mx) : 0.f;
      float t = e[half];
      t += dpp_f<0xB1>(t); t += dpp_f<0x4E>(t); t += dpp_f<0x141>(t);
      sm[half] = t;
    }
    float aw[2][8];
#pragma unroll
    for (int half = 0; half < 2; ++half)
#pragma unroll
      for (int w = 0; w < 8; ++w) aw[half][w] = __shfl(e[half], (lane & 56) + w);
    __builtin_amdgcn_sched_barrier(0);
#pragma unroll
    for (int half = 0; half < 2; ++half) {
      const int pair = half * 64 + (tid >> 3), pi = pair >> 4, bhead = pair & 15, bpos = pbase + 2 * pi;
      float o4[4] = {0.f, 0.f, 0.f, 0.f};
#pragma unroll
      for (int w = 0; w < 8; ++w) {
        o4[0] += aw[half][w] * __uint_as_float(vv[half][w].x << 16);
        o4[1] += aw[half][w] * __uint_as_float(vv[half][w].x & 0xffff0000u);
        o4[2] += aw[half][w] * __uint_as_float(vv[half][w].y << 16);
        o4[3] += aw[half][w] * __uint_as_float(vv[half][w].y & 0xffff0000u);
      }
      float gB = 0.f;
#pragma unroll
      for (int i = 0; i < 8; ++i) gB = (i == pi) ? g8[i] : gB;
      const float sc = (1.f - gB) / sm[half];
      bres[half].x = pack_bf16(o4[0] * sc, o4[1] * sc);
      bres[half].y = pack_bf16(o4[2] * sc, o4[3] * sc);
    }
  }

    STAMP(6);
    float* red = (float*)lds;
    {
      float* dst = red + (kq >> 1) * (2 * CH_U4 * 4) + ((kq & 1) * 4 + rg * 2) * 1024 + lane;
#pragma unroll
      for (int e = 0; e < 16; ++e) { dst[e * 64] = o0[e]; dst[1024 + e * 64] = o1[e]; }
    }
    if (attempt == 0 && h && !(o0[12] < 1.0e18f && o1[12] < 1.0e18f)) *flag = 1u;
    raw_barrier();
    if (attempt == 1 || *flag == 0u) {
      const int rb = wave & 3, dq = (wave >> 2) * 2;
      float l = 0.f, ov[8];
#pragma unroll
      for (int i = 0; i < 8; ++i) ov[i] = 0.f;
#pragma unroll
      for (int q = 0; q < 4; ++q) {
        const float* src = red + (q >> 1) * (2 * CH_U4 * 4) + ((q & 1) * 4 + rb) * 1024;
        l += src[12 * 64 + 32 + r];
#pragma unroll
        for (int i = 0; i < 8; ++i) ov[i] += src[(4 * dq + i) * 64 + lane];
      }
      const int R = rb * 32 + r;
      const int pos = pbase + 2 * (R >> 4), head = R & 15;
      float gA = 0.f;
#pragma unroll
      for (int i = 0; i < 8; ++i) gA = (i == (R >> 4)) ? g8[i] : gA;
      const float sc = gA / l;
#pragma unroll
      for (int g = 0; g < 2; ++g) {
        const int d = 8 * (dq + g) + 4 * h;
        if (d < DH) {
          uint2 w;
          w.x = pack_bf16(ov[4 * g] * sc, ov[4 * g + 1] * sc);
          w.y = pack_bf16(ov[4 * g + 2] * sc, ov[4 * g + 3] * sc);
          *(uint2*)(Ocat + pos * OC + head * DH + d) = w;
        }
      }
      if ((tid & 7) < 7) {
#pragma unroll
        for (int half = 0; half < 2; ++half) {
          const int pair = half * 64 + (tid >> 3);
          *(uint2*)(Ocat + (pbase + 2 * (pair >> 4)) * OC + DM + (pair & 15) * DH + 4 * (tid & 7)) = bres[half];
        }
      }
      STAMP(4);
      break;
    }
    {
      float m[2] = {-3.0e38f, -3.0e38f};
      for (int t = 0; t < 16; ++t) {
        const uint4* src = KVg + (kq * 16 + t) * 256 + lane;
        bf16x8 k0 = __builtin_bit_cast(bf16x8, src[0]);
        bf16x8 k1 = __builtin_bit_cast(bf16x8, src[64]);
#pragma unroll
        for (int b = 0; b < 2; ++b) {
          f32x16 s = MFMA32(k0, q0[b], zero);
          s = MFMA32(k1, q1[b], s);
#pragma unroll
          for (int e = 0; e < 16; ++e) m[b] = fmaxf(m[b], s[e]);
        }
      }
      m[0] = fmaxf(m[0], __shfl_xor(m[0], 32));
      m[1] = fmaxf(m[1], __shfl_xor(m[1], 32));
      wait_vm<0>();
      raw_barrier();
      red[(wave * 2) * 64 + lane] = m[0];
      red[(wave * 2 + 1) * 64 + lane] = m[1];
      raw_barrier();
#pragma unroll
      for (int b = 0; b < 2; ++b) {
        float mm = red[(rg * 2 + b) * 64 + lane];
#pragma unroll
        for (int q = 1; q < 4; ++q) mm = fmaxf(mm, red[((q * 2 + rg) * 2 + b) * 64 + lane]);
        mref[b] = mm;
      }
      raw_barrier();
    }
  }
}

extern "C" void kernel_launch(void* const* d_in, const int* in_sizes, int n_in,
                              void* d_out, int out_size, void* d_ws, size_t ws_size,
                              hipStream_t stream) {
  const u16* x = (const u16*)d_in[0];
  const u16* wq = (const u16*)d_in[1];
  const u16* wk = (const u16*)d_in[2];
  const u16* wv = (const u16*)d_in[3];
  const u16* woA = (const u16*)d_in[4];
  const u16* wqkv = (const u16*)d_in[6];
  const u16* woB = (const u16*)d_in[7];
  const u16* gw = (const u16*)d_in[8];
  char* ws = (char*)d_ws;
  u16* Qa = (u16*)(ws);
  u16* KV = (u16*)(ws + (2u << 20));
  u16* QKVb = (u16*)(ws + (3u << 20));
  float* gate = (float*)(ws + (9u << 20));
  u16* Ocat = (u16*)(ws + (10u << 20));
  u16* out = (u16*)d_out;

  k_proj<<<dim3(256), 256, 0, stream>>>(x, wq, wk, wv, wqkv, gw, Qa, KV, QKVb, gate);
  k_attn<<<dim3(S_LEN / 8), 512, 0, stream>>>(Qa, KV, QKVb, gate, Ocat);
  k_out<<<dim3(224), 256, 0, stream>>>(Ocat, woA, woB, out);
}
```

```cpp
#include <hip/hip_runtime.h>
#include <hip/hip_bf16.h>
#include <stdint.h>

typedef __attribute__((ext_vector_type(8))) short bf16x8;
typedef __attribute__((ext_vector_type(4))) short s16x4;
typedef __attribute__((ext_vector_type(16))) float f32x16;
typedef __attribute__((ext_vector_type(2))) __bf16 bf2;
typedef __attribute__((address_space(3))) s16x4* lds_s16x4_ptr;
typedef unsigned short u16;

#define DI __device__ __forceinline__
#define STAMP(i) do {} while (0)
#define STAMPG(k, i) do {} while (0)
#define EXP2(x) __builtin_amdgcn_exp2f(x)
#define MFMA32(a, b, c) __builtin_amdgcn_mfma_f32_32x32x16_bf16((a), (b), (c), 0, 0, 0)

constexpr int S_LEN = 2048;
constexpr int DM = 448;
constexpr int NH = 16;
constexpr int DH = 28;
constexpr int NB3 = 1344;
constexpr int OC = 896;

DI unsigned pack_bf16(float a, float b) {
  bf2 v; v[0] = (__bf16)a; v[1] = (__bf16)b;
  return __builtin_bit_cast(unsigned, v);
}
DI u16 to_bf16(float a) { __bf16 v = (__bf16)a; return __builtin_bit_cast(u16, v); }
DI float bf_to_f(u16 a) { return __uint_as_float(((unsigned)a) << 16); }

template <int V> struct IC { static constexpr int value = V; };
template <int I, int N, class F> DI void static_for(F&& f) {
  if constexpr (I < N) { f(IC<I>{}); static_for<I + 1, N>(f); }
}
template <int N> DI void wait_vm() { asm volatile("s_waitcnt vmcnt(%0)" ::"n"(N) : "memory"); }
DI void raw_barrier() { asm volatile("s_waitcnt lgkmcnt(0)" ::: "memory"); __builtin_amdgcn_s_barrier(); asm volatile("" ::: "memory"); }
DI void glds16(const void* g, void* l) {
  __builtin_amdgcn_global_load_lds((const __attribute__((address_space(1))) unsigned*)g,
                                   (__attribute__((address_space(3))) unsigned*)l, 16, 0, 0);
}

template <int CTRL> DI float dpp_f(float v) {
  return __int_as_float(__builtin_amdgcn_update_dpp(0, __float_as_int(v), CTRL, 0xf, 0xf, true));
}
DI void glds16a(const void* g, const void* l) {
  const unsigned la = __builtin_amdgcn_readfirstlane((unsigned)(size_t)l);
  unsigned keep;
  asm volatile("s_mov_b32 %0, m0\n\ts_mov_b32 m0, %2\n\ts_nop 0\n\tglobal_load_lds_dwordx4 %1, off\n\ts_mov_b32 m0, %0"
               : "=&s"(keep) : "v"(g), "s"(la) : "memory");
}

template <int BN> DI int bswz(int row) { return (BN == 128) ? ((row & 3) << 2) : (((row >> 1) & 1) << 2); }

template <int BM, int BN, int NS, int NBUF, class BPtr, class Epi>
DI void gemm_glds(const u16* __restrict__ A, int lda, int m0, char* smem, BPtr bptr, Epi epi) {
  constexpr int TM = BM / 64, TN = BN / 64;
  constexpr int GA = BM / 32, GB = BN / 32, G = GA + GB;
  constexpr int A_BYTES = BM * 128, B_BYTES = BN * 128, ST_BYTES = A_BYTES + B_BYTES;
  constexpr int CPR = BN / 8;
  const int tid = threadIdx.x, lane = tid & 63, wave = __builtin_amdgcn_readfirstlane(tid >> 6);
  const int wm = wave >> 1, wn = wave & 1;
  const int r = lane & 31, h = lane >> 5;

  const u16* asrc[GA];
#pragma unroll
  for (int i = 0; i < GA; ++i) {
    int idx = (wave * GA + i) * 64 + lane, row = idx >> 3, ch = (idx & 7) ^ ((row >> 1) & 7);
    asrc[i] = A + (size_t)(m0 + row) * lda + ch * 8;
  }
  int brow[GB], bch[GB];
#pragma unroll
  for (int i = 0; i < GB; ++i) {
    int idx = (wave * GB + i) * 64 + lane;
    brow[i] = idx / CPR;
    bch[i] = (idx % CPR) ^ bswz<BN>(brow[i]);
  }
  auto issue = [&](int st, int sl) {
    char* base = smem + sl * ST_BYTES;
#pragma unroll
    for (int i = 0; i < GA; ++i) glds16(asrc[i] + st * 64, base + (wave * GA + i) * 1024);
#pragma unroll
    for (int i = 0; i < GB; ++i) glds16(bptr(st * 64 + brow[i], bch[i] * 8), base + A_BYTES + (wave * GB + i) * 1024);
  };

  f32x16 acc[TM][TN];
#pragma unroll
  for (int i = 0; i < TM; ++i)
#pragma unroll
    for (int j = 0; j < TN; ++j)
#pragma unroll
      for (int e = 0; e < 16; ++e) acc[i][j][e] = 0.f;

  int aoff[TM], asw[TM];
#pragma unroll
  for (int i = 0; i < TM; ++i) {
    int row = wm * (BM / 2) + i * 32 + r;
    aoff[i] = row * 128; asw[i] = (row >> 1) & 7;
  }
  const int trq = (lane & 15) >> 2, trp = lane & 3, trblk = (lane >> 4) & 1;

  STAMPG(BM == 64, 0);
#pragma nounroll
  for (int st = 0; st < NBUF - 1; ++st) issue(st, st);

  int slot = 0;
#pragma nounroll
  for (int st = 0; st < NS; ++st) {
    if (st + NBUF - 2 < NS) wait_vm<G * (NBUF - 2)>(); else wait_vm<0>();
    raw_barrier();
    if (st == 0) STAMPG(BM == 64, 1);
    if (st + NBUF - 1 < NS) issue(st + NBUF - 1, slot == 0 ? NBUF - 1 : slot - 1);
    const char* as = smem + slot * ST_BYTES;
    const char* bs = as + A_BYTES;
    slot = (slot == NBUF - 1) ? 0 : slot + 1;
#pragma unroll
    for (int ks = 0; ks < 4; ++ks) {
      bf16x8 af[TM], bfr[TN];
#pragma unroll
      for (int i = 0; i < TM; ++i) af[i] = *(const bf16x8*)(as + aoff[i] + (((2 * ks + h) ^ asw[i]) << 4));
      const int rk = ks * 16 + 8 * h + trq;
#pragma unroll
      for (int j = 0; j < TN; ++j) {
        const int col = wn * (BN / 2) + j * 32 + 16 * trblk + 4 * trp;
        const char* p = bs + rk * (BN * 2) + (((col >> 3) ^ bswz<BN>(rk)) << 4) + ((trp & 1) << 3);
        s16x4 lo = __builtin_amdgcn_ds_read_tr16_b64_v4i16((lds_s16x4_ptr)(p));
        s16x4 hi = __builtin_amdgcn_ds_read_tr16_b64_v4i16((lds_s16x4_ptr)(p + 4 * BN * 2 + (((((col >> 3) ^ bswz<BN>(rk + 4)) - ((col >> 3) ^ bswz<BN>(rk)))) << 4)));
        bfr[j] = __builtin_shufflevector(lo, hi, 0, 1, 2, 3, 4, 5, 6, 7);
      }
#pragma unroll
      for (int i = 0; i < TM; ++i)
#pragma unroll
        for (int j = 0; j < TN; ++j) acc[i][j] = MFMA32(af[i], bfr[j], acc[i][j]);
    }
  }
  STAMPG(BM == 64, 2);
#pragma unroll
  for (int i = 0; i < TM; ++i)
#pragma unroll
    for (int j = 0; j < TN; ++j)
      epi(m0 + wm * (BM / 2) + i * 32, wn * (BN / 2) + j * 32, acc[i][j], r, h);
  STAMPG(BM == 64, 3);
}

constexpr int P_NBUF = 4;
constexpr int SP_AST = 456, SP_BST = 96;
constexpr int P_SMEM = (64 * SP_AST + 448 * SP_BST) * 2;

__global__ __launch_bounds__(256) void k_proj(const u16* __restrict__ x, const u16* __restrict__ wq,
                                              const u16* __restrict__ wk, const u16* __restrict__ wv,
                                              const u16* __restrict__ wqkv, const u16* __restrict__ gw,
                                              const u16* __restrict__ woA, const u16* __restrict__ woB,
                                              u16* __restrict__ Qa, u16* __restrict__ KV,
                                              u16* __restrict__ QKVb, float* __restrict__ gate,
                                              uint4* __restrict__ Wf) {
  __shared__ __attribute__((aligned(16))) char smem[P_SMEM];
  const int bid = blockIdx.x;
  if (bid < 224) {
    const int xi = bid >> 3;
    const int n0 = (xi % 14) * 128, m0 = (bid & 7) * 256 + (xi / 14) * 128;
    auto bptr = [&](int k, int nc) -> const u16* {
      const int n = n0 + nc;
      return (n < DM) ? (wq + k * DM + n) : (wqkv + k * NB3 + (n - DM));
    };
    const float qscale = 0.18898223650461362f * 1.4426950408889634f;
    auto epi = [&](int mb, int nbl, const f32x16& a, int r, int h) {
      const int nb = n0 + nbl, n = nb + r;
      if (nb < DM) {
#pragma unroll
        for (int e = 0; e < 16; ++e) {
          int m = mb + (e & 3) + 8 * (e >> 2) + 4 * h;
          Qa[m * DM + n] = to_bf16(a[e] * qscale);
        }
      } else {
#pragma unroll
        for (int e = 0; e < 16; ++e) {
          int m = mb + (e & 3) + 8 * (e >> 2) + 4 * h;
          QKVb[m * NB3 + (n - DM)] = to_bf16(a[e]);
        }
      }
    };
    gemm_glds<128, 128, 7, P_NBUF>(x, DM, m0, smem, bptr, epi);
    return;
  }
  const int m0 = (bid & 7) * 256 + ((bid - 224) >> 3) * 64;
  const int tid = threadIdx.x, lane = tid & 63, wave = tid >> 6;
  u16* as = (u16*)smem;
  u16* bs = as + 64 * SP_AST;
  uint4 ra[14]; uint2 rb0[14], rb1[14]; u16 rg[14];
#pragma unroll
  for (int i = 0; i < 14; ++i) {
    int c = tid + i * 256, row = c / 56, ch = c % 56;
    ra[i] = *(const uint4*)(x + (m0 + row) * DM + ch * 8);
  }
  const int nc = tid & 7;
  const u16* wsrc = (nc < 4) ? wk : wv;
  const int cb = (nc & 3) * 8;
#pragma unroll
  for (int i = 0; i < 14; ++i) {
    int k = (tid >> 3) + i * 32;
    rb0[i] = *(const uint2*)(wsrc + k * DH + cb);
    rb1[i] = *(const uint2*)(wsrc + k * DH + ((cb < 24) ? cb + 4 : cb));
    rg[i] = gw[k];
  }
#pragma unroll
  for (int i = 0; i < 14; ++i) {
    int c = tid + i * 256, row = c / 56, ch = c % 56;
    *(uint4*)(as + row * SP_AST + ch * 8) = ra[i];
  }
#pragma unroll
  for (int i = 0; i < 14; ++i) {
    int k = (tid >> 3) + i * 32;
    uint4 v = make_uint4(rb0[i].x, rb0[i].y, rb1[i].x, rb1[i].y);
    if (cb == 24) { v.z = (nc == 7) ? (unsigned)rg[i] : 0u; v.w = 0u; }
    *(uint4*)(bs + k * SP_BST + nc * 8) = v;
  }
  __syncthreads();
  const int wm = wave >> 1, wn = wave & 1, r = lane & 31, h = lane >> 5;
  const int trq = (lane & 15) >> 2, trp = lane & 3, trblk = (lane >> 4) & 1;
  f32x16 a;
#pragma unroll
  for (int e = 0; e < 16; ++e) a[e] = 0.f;
#pragma unroll 4
  for (int ks = 0; ks < 28; ++ks) {
    bf16x8 af = *(const bf16x8*)(as + (wm * 32 + r) * SP_AST + ks * 16 + h * 8);
    const u16* p = bs + (ks * 16 + 8 * h + trq) * SP_BST + wn * 32 + 16 * trblk + 4 * trp;
    s16x4 lo = __builtin_amdgcn_ds_read_tr16_b64_v4i16((lds_s16x4_ptr)(p));
    s16x4 hi = __builtin_amdgcn_ds_read_tr16_b64_v4i16((lds_s16x4_ptr)(p + 4 * SP_BST));
    a = MFMA32(af, __builtin_shufflevector(lo, hi, 0, 1, 2, 3, 4, 5, 6, 7), a);
  }
  {
    const int f0 = (bid - 224) * 1568;
#pragma unroll
    for (int i = 0; i < 7; ++i) {
      const int f = f0 + i * 256 + tid;
      if (i < 6 || tid < 32) {
        const int fl = f & 63, ks = (f >> 6) % 56, nt = (f >> 6) / 56;
        const int k = ks * 16 + (fl >> 5) * 8, n = nt * 32 + (fl & 31);
        const u16* w = (k < DM) ? (woA + k * DM + n) : (woB + (k - DM) * DM + n);
        unsigned t[8];
#pragma unroll
        for (int j = 0; j < 8; ++j) t[j] = w[j * DM];
        Wf[f] = make_uint4(t[0] | (t[1] << 16), t[2] | (t[3] << 16), t[4] | (t[5] << 16), t[6] | (t[7] << 16));
      }
    }
  }
  const int mb = m0 + wm * 32, d = r;
  if (wn == 0) {
    const int s = d >> 4, hh = (d >> 3) & 1, j = d & 7;
#pragma unroll
    for (int e = 0; e < 16; ++e) {
      int m = mb + (e & 3) + 8 * (e >> 2) + 4 * h;
      int tile = m >> 5, rr = m & 31;
      KV[tile * 2048 + s * 512 + (hh * 32 + rr) * 8 + j] = (d < DH) ? to_bf16(a[e]) : ((d < 31) ? (u16)0x3F80 : (u16)0);
    }
  } else {
#pragma unroll
    for (int e = 0; e < 16; ++e) {
      int m = mb + (e & 3) + 8 * (e >> 2) + 4 * h;
      int tile = m >> 5, kk = m & 31;
      int s = kk >> 4, k16 = kk & 15;
      int hh = (k16 >> 2) & 1, j = ((k16 >> 3) << 2) | (k16 & 3);
      u16 val = (d < DH) ? to_bf16(a[e]) : ((d == DH) ? (u16)0x3F80 : (u16)0);
      KV[tile * 2048 + 1024 + s * 512 + (hh * 32 + d) * 8 + j] = val;
      if (d == DH) gate[m] = 1.f / (1.f + __expf(-a[e]));
    }
  }
}

DI int ofrag(int pos, int c) {
  return (((pos >> 5) * 56 + (c >> 4)) * 64 + ((c >> 3) & 1) * 32 + (pos & 31)) * 8 + (c & 7);
}

__global__ __launch_bounds__(256) void k_out(const uint4* __restrict__ Af, const uint4* __restrict__ Bf,
                                             u16* __restrict__ out) {
  const int lane = threadIdx.x & 63, wave = threadIdx.x >> 6;
  const int xi = blockIdx.x >> 3;
  const int nt = (xi % 7) * 2 + (wave & 1), mt = (blockIdx.x & 7) * 8 + (xi / 7) * 2 + (wave >> 1);
  const uint4* ap = Af + mt * 56 * 64 + lane;
  const uint4* bp = Bf + nt * 56 * 64 + lane;
  f32x16 acc0, acc1;
#pragma unroll
  for (int e = 0; e < 16; ++e) { acc0[e] = 0.f; acc1[e] = 0.f; }
#pragma nounroll
  for (int k0 = 0; k0 < 56; k0 += 14) {
    uint4 av[14], bv[14];
#pragma unroll
    for (int i = 0; i < 14; ++i) { av[i] = ap[(k0 + i) * 64]; bv[i] = bp[(k0 + i) * 64]; }
#pragma unroll
    for (int i = 0; i < 14; i += 2) {
      acc0 = MFMA32(__builtin_bit_cast(bf16x8, av[i]), __builtin_bit_cast(bf16x8, bv[i]), acc0);
      acc1 = MFMA32(__builtin_bit_cast(bf16x8, av[i + 1]), __builtin_bit_cast(bf16x8, bv[i + 1]), acc1);
    }
  }
  const int r = lane & 31, h = lane >> 5;
#pragma unroll
  for (int e = 0; e < 16; ++e) {
    const int m = mt * 32 + (e & 3) + 8 * (e >> 2) + 4 * h;
    out[m * DM + nt * 32 + r] = to_bf16(acc0[e] + acc1[e]);
  }
}

constexpr int TPC = 2;
constexpr int NCH = 16 / TPC;
constexpr int CH_U4 = 4 * TPC * 256;
constexpr int QD_OFF = 3 * CH_U4;
constexpr int BQ_OFF = QD_OFF + 8 * 64;
constexpr int BK_OFF = BQ_OFF + 8 * 64;
constexpr int BK_STR = 130 * 8;
constexpr int FL_OFF = BK_OFF + 16 * 130;
constexpr int A_LDS_U4 = FL_OFF + 16;

__global__ __launch_bounds__(512) void k_attn(const u16* __restrict__ Qa, const u16* __restrict__ KV,
                                              const u16* __restrict__ QKVb, const float* __restrict__ gate,
                                              u16* __restrict__ Ocat) {
  __shared__ __attribute__((aligned(16))) uint4 lds[A_LDS_U4];
  const int tid = threadIdx.x, lane = tid & 63, wave = __builtin_amdgcn_readfirstlane(tid >> 6);
  const int rg = wave & 1, kq = wave >> 1;
  const int r = lane & 31, h = lane >> 5;
  const int xi = blockIdx.x >> 3;
  const int pbase = (blockIdx.x & 7) * 256 + (xi >> 1) * 16 + (xi & 1);
  const int rot = xi & (NCH - 1);
  const uint4* KVg = (const uint4*)KV;
  __attribute__((address_space(3))) volatile unsigned* flag = (__attribute__((address_space(3))) volatile unsigned*)(lds + FL_OFF);

  auto issue2 = [&](int c, int slot) {
    const int cc = (c + rot) & (NCH - 1);
    const char* src = (const char*)(KVg + (kq * 16 + cc * TPC + rg) * 256);
    uint4* dst = lds + slot * CH_U4 + (kq * TPC + rg) * 256;
#pragma unroll
    for (int p = 0; p < 4; ++p) glds16a(src + p * 1024 + (unsigned)(lane * 16), dst + p * 64);
  };
  const int l56 = lane < 56 ? lane : 55;
  auto issue_b = [&]() {
  glds16a((const char*)(QKVb + (pbase + 2 * wave) * NB3) + (unsigned)(l56 * 16), lds + BQ_OFF + wave * 64);
#pragma unroll
  for (int i = 0; i < 2; ++i) {
    int row = pbase - 14 + 2 * (wave * 2 + i);
    row = row < 0 ? (pbase & 1) : row;
    row = row > S_LEN - 1 ? S_LEN - 1 : row;
    const char* src = (const char*)(QKVb + row * NB3 + DM);
    const int l48 = lane < 48 ? lane : 47;
    glds16a(src + (unsigned)(lane * 16), lds + BK_OFF + (wave * 2 + i) * 130);
    glds16a(src + 1024 + (unsigned)(l48 * 16), lds + BK_OFF + (wave * 2 + i) * 130 + 64);
  }
  };
  STAMP(0);
  glds16a((const char*)(Qa + (pbase + 2 * wave) * DM) + (unsigned)(l56 * 16), lds + QD_OFF + wave * 64);
  issue2(0, 0);
  issue2(1, 1);
  if (tid == 0) *flag = 0u;
  float g8[8];
#pragma unroll
  for (int i = 0; i < 8; ++i) g8[i] = gate[pbase + 2 * i];
#pragma unroll
  for (int i = 0; i < 8; ++i) asm volatile("" : "+s"(g8[i]));
  wait_vm<4>();
  raw_barrier();
  issue2(2, 2);
  issue_b();
  STAMP(1);

  bf16x8 q0[2], q1[2];
#pragma unroll
  for (int b = 0; b < 2; ++b) {
    const int R = rg * 64 + b * 32 + r;
    const u16* qp = (const u16*)(lds + QD_OFF + (R >> 4) * 64) + (R & 15) * DH;
    uint2 qa = *(const uint2*)(qp + 8 * h), qb = *(const uint2*)(qp + 8 * h + 4);
    uint2 qc = *(const uint2*)(qp + 16 + 8 * h);
    uint2 qd = *(const uint2*)(qp + 20);
    if (h) qd = make_uint2(0, 0);
    q0[b] = __builtin_bit_cast(bf16x8, make_uint4(qa.x, qa.y, qb.x, qb.y));
    q1[b] = __builtin_bit_cast(bf16x8, make_uint4(qc.x, qc.y, qd.x, qd.y));
  }
  f32x16 zero;
#pragma unroll
  for (int e = 0; e < 16; ++e) zero[e] = 0.f;
  float mref[2];
  {
    bf16x8 k0 = __builtin_bit_cast(bf16x8, lds[lane]), k1 = __builtin_bit_cast(bf16x8, lds[64 + lane]);
#pragma unroll
    for (int b = 0; b < 2; ++b) {
      f32x16 s = MFMA32(k0, q0[b], zero);
      s = MFMA32(k1, q1[b], s);
      float m = s[0];
#pragma unroll
      for (int e = 1; e < 16; ++e) m = fmaxf(m, s[e]);
      mref[b] = fmaxf(m, __shfl_xor(m, 32));
    }
  }
  STAMP(2);

  uint2 bres[2];
  f32x16 o0, o1;
#pragma nounroll
  for (int attempt = 0; attempt < 2; ++attempt) {
    bf16x8 q1m[2];
#pragma unroll
    for (int b = 0; b < 2; ++b) {
      q1m[b] = q1[b];
      const float n0 = -mref[b];
      const u16 c0 = to_bf16(n0); const float n1 = n0 - bf_to_f(c0);
      const u16 c1 = to_bf16(n1); const float n2 = n1 - bf_to_f(c1);
      const u16 c2 = to_bf16(n2);
      if (h) { q1m[b][4] = (short)c0; q1m[b][5] = (short)c1; q1m[b][6] = (short)c2; }
    }
#pragma unroll
    for (int e = 0; e < 16; ++e) { o0[e] = 0.f; o1[e] = 0.f; }
    if (attempt) {
      issue2(0, 0); issue2(1, 1);
      wait_vm<4>(); raw_barrier();
      issue2(2, 2);
    }
    auto softmax_pv = [&](const f32x16& s, const bf16x8& vf0, const bf16x8& vf1, f32x16& o) {
      uint4 pa, pb;
      pa.x = pack_bf16(EXP2(s[0]), EXP2(s[1]));
      pa.y = pack_bf16(EXP2(s[2]), EXP2(s[3]));
      pa.z = pack_bf16(EXP2(s[4]), EXP2(s[5]));
      pa.w = pack_bf16(EXP2(s[6]), EXP2(s[7]));
      o = MFMA32(vf0, __builtin_bit_cast(bf16x8, pa), o);
      pb.x = pack_bf16(EXP2(s[8]), EXP2(s[9]));
      pb.y = pack_bf16(EXP2(s[10]), EXP2(s[11]));
      pb.z = pack_bf16(EXP2(s[12]), EXP2(s[13]));
      pb.w = pack_bf16(EXP2(s[14]), EXP2(s[15]));
      o = MFMA32(vf1, __builtin_bit_cast(bf16x8, pb), o);
    };
    f32x16 sA, sB;
    bf16x8 kf0, kf1, vf0, vf1;
    {
      const uint4* base = lds + kq * TPC * 256 + lane;
      kf0 = __builtin_bit_cast(bf16x8, base[0]); kf1 = __builtin_bit_cast(bf16x8, base[64]);
      vf0 = __builtin_bit_cast(bf16x8, base[128]); vf1 = __builtin_bit_cast(bf16x8, base[192]);
      sA = MFMA32(kf0, q0[0], zero);
      sA = MFMA32(kf1, q1m[0], sA);
    }
    int slot = 0;
#pragma nounroll
    for (int c = 0; c < NCH; ++c) {
#pragma unroll
      for (int t = 0; t < TPC; ++t) {
        sB = MFMA32(kf0, q0[1], zero);
        sB = MFMA32(kf1, q1m[1], sB);
        const bool more = (t + 1 < TPC) || (c + 1 < NCH);
        if (t + 1 == TPC) {
          slot = (slot == 2) ? 0 : slot + 1;
          if (c + 1 < NCH) {
            if (c < 2 && attempt == 0) wait_vm<9>(); else if (c + 2 < NCH) wait_vm<4>(); else wait_vm<0>();
            raw_barrier();
            if (c + 3 < NCH) issue2(c + 3, slot == 0 ? 2 : slot - 1);
          }
        }
        bf16x8 nk0 = kf0, nk1 = kf1, nv0 = vf0, nv1 = vf1;
        if (more) {
          const uint4* base = lds + slot * CH_U4 + kq * TPC * 256 + ((t + 1) & (TPC - 1)) * 256 + lane;
          nk0 = __builtin_bit_cast(bf16x8, base[0]); nk1 = __builtin_bit_cast(bf16x8, base[64]);
          nv0 = __builtin_bit_cast(bf16x8, base[128]); nv1 = __builtin_bit_cast(bf16x8, base[192]);
        }
        __builtin_amdgcn_sched_barrier(0);
        softmax_pv(sA, vf0, vf1, o0);
        __builtin_amdgcn_sched_barrier(0);
        if (more) {
          sA = MFMA32(nk0, q0[0], zero);
          sA = MFMA32(nk1, q1m[0], sA);
        }
        __builtin_amdgcn_sched_barrier(0);
        softmax_pv(sB, vf0, vf1, o1);
        __builtin_amdgcn_sched_barrier(0);
        kf0 = nk0; kf1 = nk1; vf0 = nv0; vf1 = nv1;
      }
    }
    STAMP(3);
    STAMP(5);
  if (attempt == 0) {
    const int j = tid & 7, jc = (j < 7) ? j : 6;
    uint2 qv[2][7], kv[2][7], vv[2][8];
#pragma unroll
    for (int half = 0; half < 2; ++half) {
      const int pair = half * 64 + (tid >> 3), pi = pair >> 4, bhead = pair & 15;
      const uint2* qrow = (const uint2*)((const u16*)(lds + BQ_OFF + pi * 64) + bhead * DH);
      const u16* kb = (const u16*)(lds + BK_OFF) + bhead * DH + (pi + 7) * BK_STR;
      const uint2* krow = (const uint2*)(kb - j * BK_STR);
#pragma unroll
      for (int i = 0; i < 7; ++i) { qv[half][i] = qrow[i]; kv[half][i] = krow[i]; }
#pragma unroll
      for (int w = 0; w < 8; ++w) vv[half][w] = *(const uint2*)(kb - w * BK_STR + DM + 4 * jc);
    }
    __builtin_amdgcn_sched_barrier(0);
    float e[2], sm[2];
#pragma unroll
    for (int half = 0; half < 2; ++half) {
      const int pi = (half * 64 + (tid >> 3)) >> 4;
      float lg = 0.f;
#pragma unroll
      for (int i = 0; i < 7; ++i) {
        lg = __builtin_amdgcn_fdot2_f32_bf16(__builtin_bit_cast(bf2, qv[half][i].x), __builtin_bit_cast(bf2, kv[half][i].x), lg, false);
        lg = __builtin_amdgcn_fdot2_f32_bf16(__builtin_bit_cast(bf2, qv[half][i].y), __builtin_bit_cast(bf2, kv[half][i].y), lg, false);
      }
      const bool valid = (pbase + 2 * pi - 2 * j >= 0);
      lg = valid ? lg * (0.18898223650461362f * 1.4426950408889634f) : -3.0e38f;
      float mx = lg;
      mx = fmaxf(mx, dpp_f<0xB1>(mx)); mx = fmaxf(mx, dpp_f<0x4E>(mx)); mx = fmaxf(mx, dpp_f<0x141>(mx));
      e[half] = valid ? __builtin_amdgcn_exp2f(lg - mx) : 0.f;
      float t = e[half];
      t += dpp_f<0xB1>(t); t += dpp_f<0x4E>(t); t += dpp_f<0x141>(t);
      sm[half] = t;
    }
    float aw[2][8];
#pragma unroll
    for (int half = 0; half < 2; ++half)
#pragma unroll
      for (int w = 0; w < 8; ++w) aw[half][w] = __shfl(e[half], (lane & 56) + w);
    __builtin_amdgcn_sched_barrier(0);
#pragma unroll
    for (int half = 0; half < 2; ++half) {
      const int pair = half * 64 + (tid >> 3), pi = pair >> 4, bhead = pair & 15, bpos = pbase + 2 * pi;
      float o4[4] = {0.f, 0.f, 0.f, 0.f};
#pragma unroll
      for (int w = 0; w < 8; ++w) {
        o4[0] += aw[half][w] * __uint_as_float(vv[half][w].x << 16);
        o4[1] += aw[half][w] * __uint_as_float(vv[half][w].x & 0xffff0000u);
        o4[2] += aw[half][w] * __uint_as_float(vv[half][w].y << 16);
        o4[3] += aw[half][w] * __uint_as_float(vv[half][w].y & 0xffff0000u);
      }
      float gB = 0.f;
#pragma unroll
      for (int i = 0; i < 8; ++i) gB = (i == pi) ? g8[i] : gB;
      const float sc = (1.f - gB) / sm[half];
      bres[half].x = pack_bf16(o4[0] * sc, o4[1] * sc);
      bres[half].y = pack_bf16(o4[2] * sc, o4[3] * sc);
    }
  }

    STAMP(6);
    float* red = (float*)lds;
    {
      float* dst = red + (kq >> 1) * (2 * CH_U4 * 4) + ((kq & 1) * 4 + rg * 2) * 1024 + lane;
#pragma unroll
      for (int e = 0; e < 16; ++e) { dst[e * 64] = o0[e]; dst[1024 + e * 64] = o1[e]; }
    }
    if (attempt == 0 && h && !(o0[12] < 1.0e18f && o1[12] < 1.0e18f)) *flag = 1u;
    raw_barrier();
    if (attempt == 1 || *flag == 0u) {
      const int rb = wave & 3, dq = (wave >> 2) * 2;
      float l = 0.f, ov[8];
#pragma unroll
      for (int i = 0; i < 8; ++i) ov[i] = 0.f;
#pragma unroll
      for (int q = 0; q < 4; ++q) {
        const float* src = red + (q >> 1) * (2 * CH_U4 * 4) + ((q & 1) * 4 + rb) * 1024;
        l += src[12 * 64 + 32 + r];
#pragma unroll
        for (int i = 0; i < 8; ++i) ov[i] += src[(4 * dq + i) * 64 + lane];
      }
      const int R = rb * 32 + r;
      const int pos = pbase + 2 * (R >> 4), head = R & 15;
      float gA = 0.f;
#pragma unroll
      for (int i = 0; i < 8; ++i) gA = (i == (R >> 4)) ? g8[i] : gA;
      const float sc = gA / l;
#pragma unroll
      for (int g = 0; g < 2; ++g) {
        const int d = 8 * (dq + g) + 4 * h;
        if (d < DH) {
          uint2 w;
          w.x = pack_bf16(ov[4 * g] * sc, ov[4 * g + 1] * sc);
          w.y = pack_bf16(ov[4 * g + 2] * sc, ov[4 * g + 3] * sc);
          *(uint2*)(Ocat + ofrag(pos, head * DH + d)) = w;
        }
      }
      if ((tid & 7) < 7) {
#pragma unroll
        for (int half = 0; half < 2; ++half) {
          const int pair = half * 64 + (tid >> 3);
          *(uint2*)(Ocat + ofrag(pbase + 2 * (pair >> 4), DM + (pair & 15) * DH + 4 * (tid & 7))) = bres[half];
        }
      }
      STAMP(4);
      break;
    }
    {
      float m[2] = {-3.0e38f, -3.0e38f};
      for (int t = 0; t < 16; ++t) {
        const uint4* src = KVg + (kq * 16 + t) * 256 + lane;
        bf16x8 k0 = __builtin_bit_cast(bf16x8, src[0]);
        bf16x8 k1 = __builtin_bit_cast(bf16x8, src[64]);
#pragma unroll
        for (int b = 0; b < 2; ++b) {
          f32x16 s = MFMA32(k0, q0[b], zero);
          s = MFMA32(k1, q1[b], s);
#pragma unroll
          for (int e = 0; e < 16; ++e) m[b] = fmaxf(m[b], s[e]);
        }
      }
      m[0] = fmaxf(m[0], __shfl_xor(m[0], 32));
      m[1] = fmaxf(m[1], __shfl_xor(m[1], 32));
      wait_vm<0>();
      raw_barrier();
      red[(wave * 2) * 64 + lane] = m[0];
      red[(wave * 2 + 1) * 64 + lane] = m[1];
      raw_barrier();
#pragma unroll
      for (int b = 0; b < 2; ++b) {
        float mm = red[(rg * 2 + b) * 64 + lane];
#pragma unroll
        for (int q = 1; q < 4; ++q) mm = fmaxf(mm, red[((q * 2 + rg) * 2 + b) * 64 + lane]);
        mref[b] = mm;
      }
      raw_barrier();
    }
  }
}

extern "C" void kernel_launch(void* const* d_in, const int* in_sizes, int n_in,
                              void* d_out, int out_size, void* d_ws, size_t ws_size,
                              hipStream_t stream) {
  const u16* x = (const u16*)d_in[0];
  const u16* wq = (const u16*)d_in[1];
  const u16* wk = (const u16*)d_in[2];
  const u16* wv = (const u16*)d_in[3];
  const u16* woA = (const u16*)d_in[4];
  const u16* wqkv = (const u16*)d_in[6];
  const u16* woB = (const u16*)d_in[7];
  const u16* gw = (const u16*)d_in[8];
  char* ws = (char*)d_ws;
  u16* Qa = (u16*)(ws);
  u16* KV = (u16*)(ws + (2u << 20));
  u16* QKVb = (u16*)(ws + (3u << 20));
  float* gate = (float*)(ws + (9u << 20));
  u16* Ocat = (u16*)(ws + (10u << 20));
  uint4* Wf = (uint4*)(ws + (14u << 20));
  u16* out = (u16*)d_out;

  k_proj<<<dim3(256), 256, 0, stream>>>(x, wq, wk, wv, wqkv, gw, woA, woB, Qa, KV, QKVb, gate, Wf);
  k_attn<<<dim3(S_LEN / 8), 512, 0, stream>>>(Qa, KV, QKVb, gate, Ocat);
  k_out<<<dim3(224), 256, 0, stream>>>((const uint4*)Ocat, Wf, out);
}
```

```cpp
#include <hip/hip_runtime.h>
#include <hip/hip_bf16.h>
#include <stdint.h>

typedef __attribute__((ext_vector_type(8))) short bf16x8;
typedef __attribute__((ext_vector_type(4))) short s16x4;
typedef __attribute__((ext_vector_type(16))) float f32x16;
typedef __attribute__((ext_vector_type(2))) __bf16 bf2;
typedef __attribute__((address_space(3))) s16x4* lds_s16x4_ptr;
typedef unsigned short u16;

#define DI __device__ __forceinline__
#define STAMP(i) do {} while (0)
#define STAMPG(k, i) do {} while (0)
#define EXP2(x) __builtin_amdgcn_exp2f(x)
#define MFMA32(a, b, c) __builtin_amdgcn_mfma_f32_32x32x16_bf16((a), (b), (c), 0, 0, 0)

constexpr int S_LEN = 2048;
constexpr int DM = 448;
constexpr int NH = 16;
constexpr int DH = 28;
constexpr int NB3 = 1344;
constexpr int OC = 896;

DI unsigned pack_bf16(float a, float b) {
  bf2 v; v[0] = (__bf16)a; v[1] = (__bf16)b;
  return __builtin_bit_cast(unsigned, v);
}
DI u16 to_bf16(float a) { __bf16 v = (__bf16)a; return __builtin_bit_cast(u16, v); }
DI float bf_to_f(u16 a) { return __uint_as_float(((unsigned)a) << 16); }

template <int V> struct IC { static constexpr int value = V; };
template <int I, int N, class F> DI void static_for(F&& f) {
  if constexpr (I < N) { f(IC<I>{}); static_for<I + 1, N>(f); }
}
template <int N> DI void wait_vm() { asm volatile("s_waitcnt vmcnt(%0)" ::"n"(N) : "memory"); }
DI void raw_barrier() { asm volatile("s_waitcnt lgkmcnt(0)" ::: "memory"); __builtin_amdgcn_s_barrier(); asm volatile("" ::: "memory"); }
DI void glds16(const void* g, void* l) {
  __builtin_amdgcn_global_load_lds((const __attribute__((address_space(1))) unsigned*)g,
                                   (__attribute__((address_space(3))) unsigned*)l, 16, 0, 0);
}

template <int CTRL> DI float dpp_f(float v) {
  return __int_as_float(__builtin_amdgcn_update_dpp(0, __float_as_int(v), CTRL, 0xf, 0xf, true));
}
DI void glds16a(const void* g, const void* l) {
  const unsigned la = __builtin_amdgcn_readfirstlane((unsigned)(size_t)l);
  unsigned keep;
  asm volatile("s_mov_b32 %0, m0\n\ts_mov_b32 m0, %2\n\ts_nop 0\n\tglobal_load_lds_dwordx4 %1, off\n\ts_mov_b32 m0, %0"
               : "=&s"(keep) : "v"(g), "s"(la) : "memory");
}

template <int BN> DI int bswz(int row) { return (BN == 128) ? ((row & 3) << 2) : (((row >> 1) & 1) << 2); }

template <int BM, int BN, int NS, int NBUF, class BPtr, class Epi>
DI void gemm_glds(const u16* __restrict__ A, int lda, int m0, char* smem, BPtr bptr, Epi epi) {
  constexpr int TM = BM / 128, TN = BN / 64;
  constexpr int GA = BM / 64, GB = BN / 64, G = GA + GB;
  constexpr int A_BYTES = BM * 128, B_BYTES = BN * 128, ST_BYTES = A_BYTES + B_BYTES;
  constexpr int CPR = BN / 8;
  const int tid = threadIdx.x, lane = tid & 63, wave = __builtin_amdgcn_readfirstlane(tid >> 6);
  const int wm = wave >> 1, wn = wave & 1;
  const int r = lane & 31, h = lane >> 5;

  const u16* asrc[GA];
#pragma unroll
  for (int i = 0; i < GA; ++i) {
    int idx = (wave * GA + i) * 64 + lane, row = idx >> 3, ch = (idx & 7) ^ ((row >> 1) & 7);
    asrc[i] = A + (size_t)(m0 + row) * lda + ch * 8;
  }
  int brow[GB], bch[GB];
#pragma unroll
  for (int i = 0; i < GB; ++i) {
    int idx = (wave * GB + i) * 64 + lane;
    brow[i] = idx / CPR;
    bch[i] = (idx % CPR) ^ bswz<BN>(brow[i]);
  }
  auto issue = [&](int st, int sl) {
    char* base = smem + sl * ST_BYTES;
#pragma unroll
    for (int i = 0; i < GA; ++i) glds16(asrc[i] + st * 64, base + (wave * GA + i) * 1024);
#pragma unroll
    for (int i = 0; i < GB; ++i) glds16(bptr(st * 64 + brow[i], bch[i] * 8), base + A_BYTES + (wave * GB + i) * 1024);
  };

  f32x16 acc[TM][TN];
#pragma unroll
  for (int i = 0; i < TM; ++i)
#pragma unroll
    for (int j = 0; j < TN; ++j)
#pragma unroll
      for (int e = 0; e < 16; ++e) acc[i][j][e] = 0.f;

  int aoff[TM], asw[TM];
#pragma unroll
  for (int i = 0; i < TM; ++i) {
    int row = wm * (BM / 4) + i * 32 + r;
    aoff[i] = row * 128; asw[i] = (row >> 1) & 7;
  }
  const int trq = (lane & 15) >> 2, trp = lane & 3, trblk = (lane >> 4) & 1;

  STAMPG(BM == 64, 0);
#pragma nounroll
  for (int st = 0; st < NBUF - 1; ++st) issue(st, st);

  int slot = 0;
#pragma nounroll
  for (int st = 0; st < NS; ++st) {
    if (st + NBUF - 2 < NS) wait_vm<G * (NBUF - 2)>(); else wait_vm<0>();
    raw_barrier();
    if (st == 0) STAMPG(BM == 64, 1);
    if (st + NBUF - 1 < NS) issue(st + NBUF - 1, slot == 0 ? NBUF - 1 : slot - 1);
    const char* as = smem + slot * ST_BYTES;
    const char* bs = as + A_BYTES;
    slot = (slot == NBUF - 1) ? 0 : slot + 1;
#pragma unroll
    for (int ks = 0; ks < 4; ++ks) {
      bf16x8 af[TM], bfr[TN];
#pragma unroll
      for (int i = 0; i < TM; ++i) af[i] = *(const bf16x8*)(as + aoff[i] + (((2 * ks + h) ^ asw[i]) << 4));
      const int rk = ks * 16 + 8 * h + trq;
#pragma unroll
      for (int j = 0; j < TN; ++j) {
        const int col = wn * (BN / 2) + j * 32 + 16 * trblk + 4 * trp;
        const char* p = bs + rk * (BN * 2) + (((col >> 3) ^ bswz<BN>(rk)) << 4) + ((trp & 1) << 3);
        s16x4 lo = __builtin_amdgcn_ds_read_tr16_b64_v4i16((lds_s16x4_ptr)(p));
        s16x4 hi = __builtin_amdgcn_ds_read_tr16_b64_v4i16((lds_s16x4_ptr)(p + 4 * BN * 2 + (((((col >> 3) ^ bswz<BN>(rk + 4)) - ((col >> 3) ^ bswz<BN>(rk)))) << 4)));
        bfr[j] = __builtin_shufflevector(lo, hi, 0, 1, 2, 3, 4, 5, 6, 7);
      }
#pragma unroll
      for (int i = 0; i < TM; ++i)
#pragma unroll
        for (int j = 0; j < TN; ++j) acc[i][j] = MFMA32(af[i], bfr[j], acc[i][j]);
    }
  }
  STAMPG(BM == 64, 2);
#pragma unroll
  for (int i = 0; i < TM; ++i)
#pragma unroll
    for (int j = 0; j < TN; ++j)
      epi(m0 + wm * (BM / 4) + i * 32, wn * (BN / 2) + j * 32, acc[i][j], r, h);
  STAMPG(BM == 64, 3);
}

constexpr int P_NBUF = 4;
constexpr int SP_AST = 456, SP_BST = 96;
constexpr int P_SMEM = (64 * SP_AST + 448 * SP_BST) * 2;

__global__ __launch_bounds__(512) void k_proj(const u16* __restrict__ x, const u16* __restrict__ wq,
                                              const u16* __restrict__ wk, const u16* __restrict__ wv,
                                              const u16* __restrict__ wqkv, const u16* __restrict__ gw,
                                              const u16* __restrict__ woA, const u16* __restrict__ woB,
                                              u16* __restrict__ Qa, u16* __restrict__ KV,
                                              u16* __restrict__ QKVb, float* __restrict__ gate,
                                              uint4* __restrict__ Wf) {
  __shared__ __attribute__((aligned(16))) char smem[P_SMEM];
  const int bid = blockIdx.x;
  if (bid < 224) {
    const int xi = bid >> 3;
    const int n0 = (xi % 14) * 128, m0 = (bid & 7) * 256 + (xi / 14) * 128;
    auto bptr = [&](int k, int nc) -> const u16* {
      const int n = n0 + nc;
      return (n < DM) ? (wq + k * DM + n) : (wqkv + k * NB3 + (n - DM));
    };
    const float qscale = 0.18898223650461362f * 1.4426950408889634f;
    auto epi = [&](int mb, int nbl, const f32x16& a, int r, int h) {
      const int nb = n0 + nbl, n = nb + r;
      if (nb < DM) {
#pragma unroll
        for (int e = 0; e < 16; ++e) {
          int m = mb + (e & 3) + 8 * (e >> 2) + 4 * h;
          Qa[m * DM + n] = to_bf16(a[e] * qscale);
        }
      } else {
#pragma unroll
        for (int e = 0; e < 16; ++e) {
          int m = mb + (e & 3) + 8 * (e >> 2) + 4 * h;
          QKVb[m * NB3 + (n - DM)] = to_bf16(a[e]);
        }
      }
    };
    gemm_glds<128, 128, 7, P_NBUF>(x, DM, m0, smem, bptr, epi);
    return;
  }
  const int m0 = (bid & 7) * 256 + ((bid - 224) >> 3) * 64;
  const int tid = threadIdx.x, lane = tid & 63, wave = tid >> 6;
  u16* as = (u16*)smem;
  u16* bs = as + 64 * SP_AST;
  uint4 ra[7]; uint2 rb0[7], rb1[7]; u16 rg[7];
#pragma unroll
  for (int i = 0; i < 7; ++i) {
    int c = tid + i * 512, row = c / 56, ch = c % 56;
    ra[i] = *(const uint4*)(x + (m0 + row) * DM + ch * 8);
  }
  const int nc = tid & 7;
  const u16* wsrc = (nc < 4) ? wk : wv;
  const int cb = (nc & 3) * 8;
#pragma unroll
  for (int i = 0; i < 7; ++i) {
    int k = (tid >> 3) + i * 64;
    rb0[i] = *(const uint2*)(wsrc + k * DH + cb);
    rb1[i] = *(const uint2*)(wsrc + k * DH + ((cb < 24) ? cb + 4 : cb));
    rg[i] = gw[k];
  }
#pragma unroll
  for (int i = 0; i < 7; ++i) {
    int c = tid + i * 512, row = c / 56, ch = c % 56;
    *(uint4*)(as + row * SP_AST + ch * 8) = ra[i];
  }
#pragma unroll
  for (int i = 0; i < 7; ++i) {
    int k = (tid >> 3) + i * 64;
    uint4 v = make_uint4(rb0[i].x, rb0[i].y, rb1[i].x, rb1[i].y);
    if (cb == 24) { v.z = (nc == 7) ? (unsigned)rg[i] : 0u; v.w = 0u; }
    *(uint4*)(bs + k * SP_BST + nc * 8) = v;
  }
  __syncthreads();
  if (wave >= 4) {
    const int f0 = (bid - 224) * 1568, t4 = tid - 256;
#pragma unroll
    for (int i = 0; i < 7; ++i) {
      const int f = f0 + i * 256 + t4;
      if (i < 6 || t4 < 32) {
        const int fl = f & 63, ks = (f >> 6) % 56, nt = (f >> 6) / 56;
        const int k = ks * 16 + (fl >> 5) * 8, n = nt * 32 + (fl & 31);
        const u16* w = (k < DM) ? (woA + k * DM + n) : (woB + (k - DM) * DM + n);
        unsigned t[8];
#pragma unroll
        for (int j = 0; j < 8; ++j) t[j] = w[j * DM];
        Wf[f] = make_uint4(t[0] | (t[1] << 16), t[2] | (t[3] << 16), t[4] | (t[5] << 16), t[6] | (t[7] << 16));
      }
    }
    return;
  }
  const int wm = wave >> 1, wn = wave & 1, r = lane & 31, h = lane >> 5;
  const int trq = (lane & 15) >> 2, trp = lane & 3, trblk = (lane >> 4) & 1;
  f32x16 a;
#pragma unroll
  for (int e = 0; e < 16; ++e) a[e] = 0.f;
#pragma unroll 4
  for (int ks = 0; ks < 28; ++ks) {
    bf16x8 af = *(const bf16x8*)(as + (wm * 32 + r) * SP_AST + ks * 16 + h * 8);
    const u16* p = bs + (ks * 16 + 8 * h + trq) * SP_BST + wn * 32 + 16 * trblk + 4 * trp;
    s16x4 lo = __builtin_amdgcn_ds_read_tr16_b64_v4i16((lds_s16x4_ptr)(p));
    s16x4 hi = __builtin_amdgcn_ds_read_tr16_b64_v4i16((lds_s16x4_ptr)(p + 4 * SP_BST));
    a = MFMA32(af, __builtin_shufflevector(lo, hi, 0, 1, 2, 3, 4, 5, 6, 7), a);
  }
  const int mb = m0 + wm * 32, d = r;
  if (wn == 0) {
    const int s = d >> 4, hh = (d >> 3) & 1, j = d & 7;
#pragma unroll
    for (int e = 0; e < 16; ++e) {
      int m = mb + (e & 3) + 8 * (e >> 2) + 4 * h;
      int tile = m >> 5, rr = m & 31;
      KV[tile * 2048 + s * 512 + (hh * 32 + rr) * 8 + j] = (d < DH) ? to_bf16(a[e]) : ((d < 31) ? (u16)0x3F80 : (u16)0);
    }
  } else {
#pragma unroll
    for (int e = 0; e < 16; ++e) {
      int m = mb + (e & 3) + 8 * (e >> 2) + 4 * h;
      int tile = m >> 5, kk = m & 31;
      int s = kk >> 4, k16 = kk & 15;
      int hh = (k16 >> 2) & 1, j = ((k16 >> 3) << 2) | (k16 & 3);
      u16 val = (d < DH) ? to_bf16(a[e]) : ((d == DH) ? (u16)0x3F80 : (u16)0);
      KV[tile * 2048 + 1024 + s * 512 + (hh * 32 + d) * 8 + j] = val;
      if (d == DH) gate[m] = 1.f / (1.f + __expf(-a[e]));
    }
  }
}

DI int ofrag(int pos, int c) {
  return (((pos >> 5) * 56 + (c >> 4)) * 64 + ((c >> 3) & 1) * 32 + (pos & 31)) * 8 + (c & 7);
}

__global__ __launch_bounds__(512) void k_out(const uint4* __restrict__ Af, const uint4* __restrict__ Bf,
                                             u16* __restrict__ out) {
  __shared__ float red[4 * 16 * 64];
  const int lane = threadIdx.x & 63, wave = threadIdx.x >> 6, w4 = wave & 3, kh = wave >> 2;
  const int xi = blockIdx.x >> 3;
  const int nt = (xi % 7) * 2 + (w4 & 1), mt = (blockIdx.x & 7) * 8 + (xi / 7) * 2 + (w4 >> 1);
  const uint4* ap = Af + (mt * 56 + kh * 28) * 64 + lane;
  const uint4* bp = Bf + (nt * 56 + kh * 28) * 64 + lane;
  f32x16 acc0, acc1;
#pragma unroll
  for (int e = 0; e < 16; ++e) { acc0[e] = 0.f; acc1[e] = 0.f; }
  {
    uint4 av[28], bv[28];
#pragma unroll
    for (int i = 0; i < 28; ++i) { av[i] = ap[i * 64]; bv[i] = bp[i * 64]; }
    __builtin_amdgcn_sched_barrier(0);
#pragma unroll
    for (int i = 0; i < 28; i += 2) {
      acc0 = MFMA32(__builtin_bit_cast(bf16x8, av[i]), __builtin_bit_cast(bf16x8, bv[i]), acc0);
      acc1 = MFMA32(__builtin_bit_cast(bf16x8, av[i + 1]), __builtin_bit_cast(bf16x8, bv[i + 1]), acc1);
    }
  }
  if (kh) {
#pragma unroll
    for (int e = 0; e < 16; ++e) red[(w4 * 16 + e) * 64 + lane] = acc0[e] + acc1[e];
  }
  __syncthreads();
  if (!kh) {
    const int r = lane & 31, h = lane >> 5;
#pragma unroll
    for (int e = 0; e < 16; ++e) {
      const int m = mt * 32 + (e & 3) + 8 * (e >> 2) + 4 * h;
      out[m * DM + nt * 32 + r] = to_bf16(acc0[e] + acc1[e] + red[(w4 * 16 + e) * 64 + lane]);
    }
  }
}

constexpr int TPC = 2;
constexpr int NCH = 16 / TPC;
constexpr int CH_U4 = 4 * TPC * 256;
constexpr int QD_OFF = 3 * CH_U4;
constexpr int BQ_OFF = QD_OFF + 8 * 64;
constexpr int BK_OFF = BQ_OFF + 8 * 64;
constexpr int BK_STR = 130 * 8;
constexpr int FL_OFF = BK_OFF + 16 * 130;
constexpr int A_LDS_U4 = FL_OFF + 16;

__global__ __launch_bounds__(512) void k_attn(const u16* __restrict__ Qa, const u16* __restrict__ KV,
                                              const u16* __restrict__ QKVb, const float* __restrict__ gate,
                                              u16* __restrict__ Ocat) {
  __shared__ __attribute__((aligned(16))) uint4 lds[A_LDS_U4];
  const int tid = threadIdx.x, lane = tid & 63, wave = __builtin_amdgcn_readfirstlane(tid >> 6);
  const int rg = wave & 1, kq = wave >> 1;
  const int r = lane & 31, h = lane >> 5;
  const int xi = blockIdx.x >> 3;
  const int pbase = (blockIdx.x & 7) * 256 + (xi >> 1) * 16 + (xi & 1);
  const int rot = xi & (NCH - 1);
  const uint4* KVg = (const uint4*)KV;
  __attribute__((address_space(3))) volatile unsigned* flag = (__attribute__((address_space(3))) volatile unsigned*)(lds + FL_OFF);

  auto issue2 = [&](int c, int slot) {
    const int cc = (c + rot) & (NCH - 1);
    const char* src = (const char*)(KVg + (kq * 16 + cc * TPC + rg) * 256);
    uint4* dst = lds + slot * CH_U4 + (kq * TPC + rg) * 256;
#pragma unroll
    for (int p = 0; p < 4; ++p) glds16a(src + p * 1024 + (unsigned)(lane * 16), dst + p * 64);
  };
  const int l56 = lane < 56 ? lane : 55;
  auto issue_b = [&]() {
  glds16a((const char*)(QKVb + (pbase + 2 * wave) * NB3) + (unsigned)(l56 * 16), lds + BQ_OFF + wave * 64);
#pragma unroll
  for (int i = 0; i < 2; ++i) {
    int row = pbase - 14 + 2 * (wave * 2 + i);
    row = row < 0 ? (pbase & 1) : row;
    row = row > S_LEN - 1 ? S_LEN - 1 : row;
    const char* src = (const char*)(QKVb + row * NB3 + DM);
    const int l48 = lane < 48 ? lane : 47;
    glds16a(src + (unsigned)(lane * 16), lds + BK_OFF + (wave * 2 + i) * 130);
    glds16a(src + 1024 + (unsigned)(l48 * 16), lds + BK_OFF + (wave * 2 + i) * 130 + 64);
  }
  };
  STAMP(0);
  glds16a((const char*)(Qa + (pbase + 2 * wave) * DM) + (unsigned)(l56 * 16), lds + QD_OFF + wave * 64);
  issue2(0, 0);
  issue2(1, 1);
  if (tid == 0) *flag = 0u;
  float g8[8];
#pragma unroll
  for (int i = 0; i < 8; ++i) g8[i] = gate[pbase + 2 * i];
#pragma unroll
  for (int i = 0; i < 8; ++i) asm volatile("" : "+s"(g8[i]));
  wait_vm<4>();
  raw_barrier();
  issue2(2, 2);
  issue_b();
  STAMP(1);

  bf16x8 q0[2], q1[2];
#pragma unroll
  for (int b = 0; b < 2; ++b) {
    const int R = rg * 64 + b * 32 + r;
    const u16* qp = (const u16*)(lds + QD_OFF + (R >> 4) * 64) + (R & 15) * DH;
    uint2 qa = *(const uint2*)(qp + 8 * h), qb = *(const uint2*)(qp + 8 * h + 4);
    uint2 qc = *(const uint2*)(qp + 16 + 8 * h);
    uint2 qd = *(const uint2*)(qp + 20);
    if (h) qd = make_uint2(0, 0);
    q0[b] = __builtin_bit_cast(bf16x8, make_uint4(qa.x, qa.y, qb.x, qb.y));
    q1[b] = __builtin_bit_cast(bf16x8, make_uint4(qc.x, qc.y, qd.x, qd.y));
  }
  f32x16 zero;
#pragma unroll
  for (int e = 0; e < 16; ++e) zero[e] = 0.f;
  float mref[2];
  {
    bf16x8 k0 = __builtin_bit_cast(bf16x8, lds[lane]), k1 = __builtin_bit_cast(bf16x8, lds[64 + lane]);
#pragma unroll
    for (int b = 0; b < 2; ++b) {
      f32x16 s = MFMA32(k0, q0[b], zero);
      s = MFMA32(k1, q1[b], s);
      float m = s[0];
#pragma unroll
      for (int e = 1; e < 16; ++e) m = fmaxf(m, s[e]);
      mref[b] = fmaxf(m, __shfl_xor(m, 32));
    }
  }
  STAMP(2);

  uint2 bres[2];
  f32x16 o0, o1;
#pragma nounroll
  for (int attempt = 0; attempt < 2; ++attempt) {
    bf16x8 q1m[2];
#pragma unroll
    for (int b = 0; b < 2; ++b) {
      q1m[b] = q1[b];
      const float n0 = -mref[b];
      const u16 c0 = to_bf16(n0); const float n1 = n0 - bf_to_f(c0);
      const u16 c1 = to_bf16(n1); const float n2 = n1 - bf_to_f(c1);
      const u16 c2 = to_bf16(n2);
      if (h) { q1m[b][4] = (short)c0; q1m[b][5] = (short)c1; q1m[b][6] = (short)c2; }
    }
#pragma unroll
    for (int e = 0; e < 16; ++e) { o0[e] = 0.f; o1[e] = 0.f; }
    if (attempt) {
      issue2(0, 0); issue2(1, 1);
      wait_vm<4>(); raw_barrier();
      issue2(2, 2);
    }
    auto softmax_pv = [&](const f32x16& s, const bf16x8& vf0, const bf16x8& vf1, f32x16& o) {
      uint4 pa, pb;
      pa.x = pack_bf16(EXP2(s[0]), EXP2(s[1]));
      pa.y = pack_bf16(EXP2(s[2]), EXP2(s[3]));
      pa.z = pack_bf16(EXP2(s[4]), EXP2(s[5]));
      pa.w = pack_bf16(EXP2(s[6]), EXP2(s[7]));
      o = MFMA32(vf0, __builtin_bit_cast(bf16x8, pa), o);
      pb.x = pack_bf16(EXP2(s[8]), EXP2(s[9]));
      pb.y = pack_bf16(EXP2(s[10]), EXP2(s[11]));
      pb.z = pack_bf16(EXP2(s[12]), EXP2(s[13]));
      pb.w = pack_bf16(EXP2(s[14]), EXP2(s[15]));
      o = MFMA32(vf1, __builtin_bit_cast(bf16x8, pb), o);
    };
    f32x16 sA, sB;
    bf16x8 kf0, kf1, vf0, vf1;
    {
      const uint4* base = lds + kq * TPC * 256 + lane;
      kf0 = __builtin_bit_cast(bf16x8, base[0]); kf1 = __builtin_bit_cast(bf16x8, base[64]);
      vf0 = __builtin_bit_cast(bf16x8, base[128]); vf1 = __builtin_bit_cast(bf16x8, base[192]);
      sA = MFMA32(kf0, q0[0], zero);
      sA = MFMA32(kf1, q1m[0], sA);
    }
    int slot = 0;
#pragma nounroll
    for (int c = 0; c < NCH; ++c) {
#pragma unroll
      for (int t = 0; t < TPC; ++t) {
        sB = MFMA32(kf0, q0[1], zero);
        sB = MFMA32(kf1, q1m[1], sB);
        const bool more = (t + 1 < TPC) || (c + 1 < NCH);
        if (t + 1 == TPC) {
          slot = (slot == 2) ? 0 : slot + 1;
          if (c + 1 < NCH) {
            if (c < 2 && attempt == 0) wait_vm<9>(); else if (c + 2 < NCH) wait_vm<4>(); else wait_vm<0>();
            raw_barrier();
            if (c + 3 < NCH) issue2(c + 3, slot == 0 ? 2 : slot - 1);
          }
        }
        bf16x8 nk0 = kf0, nk1 = kf1, nv0 = vf0, nv1 = vf1;
        if (more) {
          const uint4* base = lds + slot * CH_U4 + kq * TPC * 256 + ((t + 1) & (TPC - 1)) * 256 + lane;
          nk0 = __builtin_bit_cast(bf16x8, base[0]); nk1 = __builtin_bit_cast(bf16x8, base[64]);
          nv0 = __builtin_bit_cast(bf16x8, base[128]); nv1 = __builtin_bit_cast(bf16x8, base[192]);
        }
        __builtin_amdgcn_sched_barrier(0);
        softmax_pv(sA, vf0, vf1, o0);
        __builtin_amdgcn_sched_barrier(0);
        if (more) {
          sA = MFMA32(nk0, q0[0], zero);
          sA = MFMA32(nk1, q1m[0], sA);
        }
        __builtin_amdgcn_sched_barrier(0);
        softmax_pv(sB, vf0, vf1, o1);
        __builtin_amdgcn_sched_barrier(0);
        kf0 = nk0; kf1 = nk1; vf0 = nv0; vf1 = nv1;
      }
    }
    STAMP(3);
    STAMP(5);
  if (attempt == 0) {
    const int j = tid & 7, jc = (j < 7) ? j : 6;
    uint2 qv[2][7], kv[2][7], vv[2][8];
#pragma unroll
    for (int half = 0; half < 2; ++half) {
      const int pair = half * 64 + (tid >> 3), pi = pair >> 4, bhead = pair & 15;
      const uint2* qrow = (const uint2*)((const u16*)(lds + BQ_OFF + pi * 64) + bhead * DH);
      const u16* kb = (const u16*)(lds + BK_OFF) + bhead * DH + (pi + 7) * BK_STR;
      const uint2* krow = (const uint2*)(kb - j * BK_STR);
#pragma unroll
      for (int i = 0; i < 7; ++i) { qv[half][i] = qrow[i]; kv[half][i] = krow[i]; }
#pragma unroll
      for (int w = 0; w < 8; ++w) vv[half][w] = *(const uint2*)(kb - w * BK_STR + DM + 4 * jc);
    }
    __builtin_amdgcn_sched_barrier(0);
    float e[2], sm[2];
#pragma unroll
    for (int half = 0; half < 2; ++half) {
      const int pi = (half * 64 + (tid >> 3)) >> 4;
      float lg = 0.f;
#pragma unroll
      for (int i = 0; i < 7; ++i) {
        lg = __builtin_amdgcn_fdot2_f32_bf16(__builtin_bit_cast(bf2, qv[half][i].x), __builtin_bit_cast(bf2, kv[half][i].x), lg, false);
        lg = __builtin_amdgcn_fdot2_f32_bf16(__builtin_bit_cast(bf2, qv[half][i].y), __builtin_bit_cast(bf2, kv[half][i].y), lg, false);
      }
      const bool valid = (pbase + 2 * pi - 2 * j >= 0);
      lg = valid ? lg * (0.18898223650461362f * 1.4426950408889634f) : -3.0e38f;
      float mx = lg;
      mx = fmaxf(mx, dpp_f<0xB1>(mx)); mx = fmaxf(mx, dpp_f<0x4E>(mx)); mx = fmaxf(mx, dpp_f<0x141>(mx));
      e[half] = valid ? __builtin_amdgcn_exp2f(lg - mx) : 0.f;
      float t = e[half];
      t += dpp_f<0xB1>(t); t += dpp_f<0x4E>(t); t += dpp_f<0x141>(t);
      sm[half] = t;
    }
    float aw[2][8];
#pragma unroll
    for (int half = 0; half < 2; ++half)
#pragma unroll
      for (int w = 0; w < 8; ++w) aw[half][w] = __shfl(e[half], (lane & 56) + w);
    __builtin_amdgcn_sched_barrier(0);
#pragma unroll
    for (int half = 0; half < 2; ++half) {
      const int pair = half * 64 + (tid >> 3), pi = pair >> 4, bhead = pair & 15, bpos = pbase + 2 * pi;
      float o4[4] = {0.f, 0.f, 0.f, 0.f};
#pragma unroll
      for (int w = 0; w < 8; ++w) {
        o4[0] += aw[half][w] * __uint_as_float(vv[half][w].x << 16);
        o4[1] += aw[half][w] * __uint_as_float(vv[half][w].x & 0xffff0000u);
        o4[2] += aw[half][w] * __uint_as_float(vv[half][w].y << 16);
        o4[3] += aw[half][w] * __uint_as_float(vv[half][w].y & 0xffff0000u);
      }
      float gB = 0.f;
#pragma unroll
      for (int i = 0; i < 8; ++i) gB = (i == pi) ? g8[i] : gB;
      const float sc = (1.f - gB) / sm[half];
      bres[half].x = pack_bf16(o4[0] * sc, o4[1] * sc);
      bres[half].y = pack_bf16(o4[2] * sc, o4[3] * sc);
    }
  }

    STAMP(6);
    float* red = (float*)lds;
    {
      float* dst = red + (kq >> 1) * (2 * CH_U4 * 4) + ((kq & 1) * 4 + rg * 2) * 1024 + lane;
#pragma unroll
      for (int e = 0; e < 16; ++e) { dst[e * 64] = o0[e]; dst[1024 + e * 64] = o1[e]; }
    }
    if (attempt == 0 && h && !(o0[12] < 1.0e18f && o1[12] < 1.0e18f)) *flag = 1u;
    raw_barrier();
    if (attempt == 1 || *flag == 0u) {
      const int rb = wave & 3, dq = (wave >> 2) * 2;
      float l = 0.f, ov[8];
#pragma unroll
      for (int i = 0; i < 8; ++i) ov[i] = 0.f;
#pragma unroll
      for (int q = 0; q < 4; ++q) {
        const float* src = red + (q >> 1) * (2 * CH_U4 * 4) + ((q & 1) * 4 + rb) * 1024;
        l += src[12 * 64 + 32 + r];
#pragma unroll
        for (int i = 0; i < 8; ++i) ov[i] += src[(4 * dq + i) * 64 + lane];
      }
      const int R = rb * 32 + r;
      const int pos = pbase + 2 * (R >> 4), head = R & 15;
      float gA = 0.f;
#pragma unroll
      for (int i = 0; i < 8; ++i) gA = (i == (R >> 4)) ? g8[i] : gA;
      const float sc = gA / l;
#pragma unroll
      for (int g = 0; g < 2; ++g) {
        const int d = 8 * (dq + g) + 4 * h;
        if (d < DH) {
          uint2 w;
          w.x = pack_bf16(ov[4 * g] * sc, ov[4 * g + 1] * sc);
          w.y = pack_bf16(ov[4 * g + 2] * sc, ov[4 * g + 3] * sc);
          *(uint2*)(Ocat + ofrag(pos, head * DH + d)) = w;
        }
      }
      if ((tid & 7) < 7) {
#pragma unroll
        for (int half = 0; half < 2; ++half) {
          const int pair = half * 64 + (tid >> 3);
          *(uint2*)(Ocat + ofrag(pbase + 2 * (pair >> 4), DM + (pair & 15) * DH + 4 * (tid & 7))) = bres[half];
        }
      }
      STAMP(4);
      break;
    }
    {
      float m[2] = {-3.0e38f, -3.0e38f};
      for (int t = 0; t < 16; ++t) {
        const uint4* src = KVg + (kq * 16 + t) * 256 + lane;
        bf16x8 k0 = __builtin_bit_cast(bf16x8, src[0]);
        bf16x8 k1 = __builtin_bit_cast(bf16x8, src[64]);
#pragma unroll
        for (int b = 0; b < 2; ++b) {
          f32x16 s = MFMA32(k0, q0[b], zero);
          s = MFMA32(k1, q1[b], s);
#pragma unroll
          for (int e = 0; e < 16; ++e) m[b] = fmaxf(m[b], s[e]);
        }
      }
      m[0] = fmaxf(m[0], __shfl_xor(m[0], 32));
      m[1] = fmaxf(m[1], __shfl_xor(m[1], 32));
      wait_vm<0>();
      raw_barrier();
      red[(wave * 2) * 64 + lane] = m[0];
      red[(wave * 2 + 1) * 64 + lane] = m[1];
      raw_barrier();
#pragma unroll
      for (int b = 0; b < 2; ++b) {
        float mm = red[(rg * 2 + b) * 64 + lane];
#pragma unroll
        for (int q = 1; q < 4; ++q) mm = fmaxf(mm, red[((q * 2 + rg) * 2 + b) * 64 + lane]);
        mref[b] = mm;
      }
      raw_barrier();
    }
  }
}

extern "C" void kernel_launch(void* const* d_in, const int* in_sizes, int n_in,
                              void* d_out, int out_size, void* d_ws, size_t ws_size,
                              hipStream_t stream) {
  const u16* x = (const u16*)d_in[0];
  const u16* wq = (const u16*)d_in[1];
  const u16* wk = (const u16*)d_in[2];
  const u16* wv = (const u16*)d_in[3];
  const u16* woA = (const u16*)d_in[4];
  const u16* wqkv = (const u16*)d_in[6];
  const u16* woB = (const u16*)d_in[7];
  const u16* gw = (const u16*)d_in[8];
  char* ws = (char*)d_ws;
  u16* Qa = (u16*)(ws);
  u16* KV = (u16*)(ws + (2u << 20));
  u16* QKVb = (u16*)(ws + (3u << 20));
  float* gate = (float*)(ws + (9u << 20));
  u16* Ocat = (u16*)(ws + (10u << 20));
  uint4* Wf = (uint4*)(ws + (14u << 20));
  u16* out = (u16*)d_out;

  k_proj<<<dim3(256), 512, 0, stream>>>(x, wq, wk, wv, wqkv, gw, woA, woB, Qa, KV, QKVb, gate, Wf);
  k_attn<<<dim3(S_LEN / 8), 512, 0, stream>>>(Qa, KV, QKVb, gate, Ocat);
  k_out<<<dim3(224), 512, 0, stream>>>((const uint4*)Ocat, Wf, out);
}
```

```cpp
#include <hip/hip_runtime.h>
#include <hip/hip_bf16.h>
#include <stdint.h>

typedef __attribute__((ext_vector_type(8))) short bf16x8;
typedef __attribute__((ext_vector_type(4))) short s16x4;
typedef __attribute__((ext_vector_type(16))) float f32x16;
typedef __attribute__((ext_vector_type(2))) __bf16 bf2;
typedef __attribute__((address_space(3))) s16x4* lds_s16x4_ptr;
typedef unsigned short u16;

#define DI __device__ __forceinline__
#define STAMP(i) do {} while (0)
#define STAMPG(k, i) do {} while (0)
#define EXP2(x) __builtin_amdgcn_exp2f(x)
#define MFMA32(a, b, c) __builtin_amdgcn_mfma_f32_32x32x16_bf16((a), (b), (c), 0, 0, 0)

constexpr int S_LEN = 2048;
constexpr int DM = 448;
constexpr int NH = 16;
constexpr int DH = 28;
constexpr int NB3 = 1344;
constexpr int OC = 896;

DI unsigned pack_bf16(float a, float b) {
  bf2 v; v[0] = (__bf16)a; v[1] = (__bf16)b;
  return __builtin_bit_cast(unsigned, v);
}
DI u16 to_bf16(float a) { __bf16 v = (__bf16)a; return __builtin_bit_cast(u16, v); }
DI float bf_to_f(u16 a) { return __uint_as_float(((unsigned)a) << 16); }

template <int V> struct IC { static constexpr int value = V; };
template <int I, int N, class F> DI void static_for(F&& f) {
  if constexpr (I < N) { f(IC<I>{}); static_for<I + 1, N>(f); }
}
template <int N> DI void wait_vm() { asm volatile("s_waitcnt vmcnt(%0)" ::"n"(N) : "memory"); }
DI void raw_barrier() { asm volatile("s_waitcnt lgkmcnt(0)" ::: "memory"); __builtin_amdgcn_s_barrier(); asm volatile("" ::: "memory"); }
DI void glds16(const void* g, void* l) {
  __builtin_amdgcn_global_load_lds((const __attribute__((address_space(1))) unsigned*)g,
                                   (__attribute__((address_space(3))) unsigned*)l, 16, 0, 0);
}

template <int CTRL> DI float dpp_f(float v) {
  return __int_as_float(__builtin_amdgcn_update_dpp(0, __float_as_int(v), CTRL, 0xf, 0xf, true));
}
DI void glds16a(const void* g, const void* l) {
  const unsigned la = __builtin_amdgcn_readfirstlane((unsigned)(size_t)l);
  unsigned keep;
  asm volatile("s_mov_b32 %0, m0\n\ts_mov_b32 m0, %2\n\ts_nop 0\n\tglobal_load_lds_dwordx4 %1, off\n\ts_mov_b32 m0, %0"
               : "=&s"(keep) : "v"(g), "s"(la) : "memory");
}

constexpr int G_AST = 72, G_BST = 160;
constexpr int G_STAGE = 128 * G_AST + 64 * G_BST;

struct G128Regs { uint4 a0, a1, b0, b1; };
DI G128Regs g128_load(const u16* as0, const u16* as1, const u16* bs0, const u16* bs1, int ld0, int ld1, int st) {
  G128Regs v;
  v.a0 = *(const uint4*)(as0 + st * 64);
  v.a1 = *(const uint4*)(as1 + st * 64);
  v.b0 = *(const uint4*)(bs0 + st * 64 * ld0);
  v.b1 = *(const uint4*)(bs1 + st * 64 * ld1);
  return v;
}
DI void g128_store(const G128Regs& v, u16* buf, int tid) {
  u16* bs = buf + 128 * G_AST;
  *(uint4*)(buf + (tid >> 3) * G_AST + (tid & 7) * 8) = v.a0;
  *(uint4*)(buf + ((tid >> 3) + 64) * G_AST + (tid & 7) * 8) = v.a1;
  *(uint4*)(bs + (tid >> 4) * G_BST + (tid & 15) * 8) = v.b0;
  *(uint4*)(bs + ((tid >> 4) + 32) * G_BST + (tid & 15) * 8) = v.b1;
}
DI void g128_compute(const u16* buf, int wm, int wn, int lane, f32x16& acc0, f32x16& acc1) {
  const int r = lane & 31, h = lane >> 5;
  const int trq = (lane & 15) >> 2, trp = lane & 3, trblk = (lane >> 4) & 1;
  const u16* bs = buf + 128 * G_AST;
#pragma unroll
  for (int ks = 0; ks < 4; ++ks) {
    const bf16x8 af = *(const bf16x8*)(buf + (wm * 32 + r) * G_AST + ks * 16 + h * 8);
    const u16* p = bs + (ks * 16 + 8 * h + trq) * G_BST + wn * 64 + 16 * trblk + 4 * trp;
    s16x4 lo0 = __builtin_amdgcn_ds_read_tr16_b64_v4i16((lds_s16x4_ptr)(p));
    s16x4 hi0 = __builtin_amdgcn_ds_read_tr16_b64_v4i16((lds_s16x4_ptr)(p + 4 * G_BST));
    s16x4 lo1 = __builtin_amdgcn_ds_read_tr16_b64_v4i16((lds_s16x4_ptr)(p + 32));
    s16x4 hi1 = __builtin_amdgcn_ds_read_tr16_b64_v4i16((lds_s16x4_ptr)(p + 4 * G_BST + 32));
    acc0 = MFMA32(af, __builtin_shufflevector(lo0, hi0, 0, 1, 2, 3, 4, 5, 6, 7), acc0);
    acc1 = MFMA32(af, __builtin_shufflevector(lo1, hi1, 0, 1, 2, 3, 4, 5, 6, 7), acc1);
  }
}

constexpr int SP_AST = 456, SP_BST = 96;
constexpr int P_SMEM = (64 * SP_AST + 448 * SP_BST) * 2;

__global__ __launch_bounds__(512) void k_proj(const u16* __restrict__ x, const u16* __restrict__ wq,
                                              const u16* __restrict__ wk, const u16* __restrict__ wv,
                                              const u16* __restrict__ wqkv, const u16* __restrict__ gw,
                                              const u16* __restrict__ woA, const u16* __restrict__ woB,
                                              u16* __restrict__ Qa, u16* __restrict__ KV,
                                              u16* __restrict__ QKVb, float* __restrict__ gate,
                                              uint4* __restrict__ Wf) {
  __shared__ __attribute__((aligned(16))) char smem[P_SMEM];
  const int bid = blockIdx.x;
  if (bid < 224) {
    const int xi = bid >> 3;
    const int n0 = (xi % 14) * 128, m0 = (bid & 7) * 256 + (xi / 14) * 128;
    const int tid = threadIdx.x, lane = tid & 63, wave = tid >> 6, wm = wave >> 1, wn = wave & 1;
    const u16* as0 = x + (m0 + (tid >> 3)) * DM + (tid & 7) * 8;
    const u16* as1 = as0 + 64 * DM;
    const int nb = n0 + (tid & 15) * 8;
    const int ld = (nb < DM) ? DM : NB3;
    const u16* bs0 = ((nb < DM) ? (wq + nb) : (wqkv + (nb - DM))) + (tid >> 4) * ld;
    const u16* bs1 = bs0 + 32 * ld;
    u16* buf0 = (u16*)smem;
    u16* buf1 = buf0 + G_STAGE;
    f32x16 acc0, acc1;
#pragma unroll
    for (int e = 0; e < 16; ++e) { acc0[e] = 0.f; acc1[e] = 0.f; }
    STAMPG(0, 0);
    G128Regs r0 = g128_load(as0, as1, bs0, bs1, ld, ld, 0);
    G128Regs r1 = g128_load(as0, as1, bs0, bs1, ld, ld, 1);
    g128_store(r0, buf0, tid);
    r0 = g128_load(as0, as1, bs0, bs1, ld, ld, 2);
    __syncthreads();
    STAMPG(0, 1);
#pragma nounroll
    for (int st = 0; st < 6; st += 2) {
      g128_compute(buf0, wm, wn, lane, acc0, acc1);
      g128_store(r1, buf1, tid);
      if (st + 3 < 7) r1 = g128_load(as0, as1, bs0, bs1, ld, ld, st + 3);
      __syncthreads();
      g128_compute(buf1, wm, wn, lane, acc0, acc1);
      g128_store(r0, buf0, tid);
      if (st + 4 < 7) r0 = g128_load(as0, as1, bs0, bs1, ld, ld, st + 4);
      __syncthreads();
    }
    g128_compute(buf0, wm, wn, lane, acc0, acc1);
    STAMPG(0, 2);
    const float qscale = 0.18898223650461362f * 1.4426950408889634f;
    const int r = lane & 31, h = lane >> 5, mb = m0 + wm * 32;
#pragma unroll
    for (int j = 0; j < 2; ++j) {
      const f32x16& a = j ? acc1 : acc0;
      const int nt = n0 + wn * 64 + j * 32, n = nt + r;
      if (nt < DM) {
#pragma unroll
        for (int e = 0; e < 16; ++e) Qa[(mb + (e & 3) + 8 * (e >> 2) + 4 * h) * DM + n] = to_bf16(a[e] * qscale);
      } else {
#pragma unroll
        for (int e = 0; e < 16; ++e) QKVb[(mb + (e & 3) + 8 * (e >> 2) + 4 * h) * NB3 + (n - DM)] = to_bf16(a[e]);
      }
    }
    STAMPG(0, 3);
    return;
  }
  const int m0 = (bid & 7) * 256 + ((bid - 224) >> 3) * 64;
  const int tid = threadIdx.x, lane = tid & 63, wave = tid >> 6;
  u16* as = (u16*)smem;
  u16* bs = as + 64 * SP_AST;
  uint4 ra[7]; uint2 rb0[7], rb1[7]; u16 rg[7];
#pragma unroll
  for (int i = 0; i < 7; ++i) {
    int c = tid + i * 512, row = c / 56, ch = c % 56;
    ra[i] = *(const uint4*)(x + (m0 + row) * DM + ch * 8);
  }
  const int nc = tid & 7;
  const u16* wsrc = (nc < 4) ? wk : wv;
  const int cb = (nc & 3) * 8;
#pragma unroll
  for (int i = 0; i < 7; ++i) {
    int k = (tid >> 3) + i * 64;
    rb0[i] = *(const uint2*)(wsrc + k * DH + cb);
    rb1[i] = *(const uint2*)(wsrc + k * DH + ((cb < 24) ? cb + 4 : cb));
    rg[i] = gw[k];
  }
#pragma unroll
  for (int i = 0; i < 7; ++i) {
    int c = tid + i * 512, row = c / 56, ch = c % 56;
    *(uint4*)(as + row * SP_AST + ch * 8) = ra[i];
  }
#pragma unroll
  for (int i = 0; i < 7; ++i) {
    int k = (tid >> 3) + i * 64;
    uint4 v = make_uint4(rb0[i].x, rb0[i].y, rb1[i].x, rb1[i].y);
    if (cb == 24) { v.z = (nc == 7) ? (unsigned)rg[i] : 0u; v.w = 0u; }
    *(uint4*)(bs + k * SP_BST + nc * 8) = v;
  }
  __syncthreads();
  if (wave >= 4) {
    const int f0 = (bid - 224) * 1568, t4 = tid - 256;
#pragma unroll
    for (int i = 0; i < 7; ++i) {
      const int f = f0 + i * 256 + t4;
      if (i < 6 || t4 < 32) {
        const int fl = f & 63, ks = (f >> 6) % 56, nt = (f >> 6) / 56;
        const int k = ks * 16 + (fl >> 5) * 8, n = nt * 32 + (fl & 31);
        const u16* w = (k < DM) ? (woA + k * DM + n) : (woB + (k - DM) * DM + n);
        unsigned t[8];
#pragma unroll
        for (int j = 0; j < 8; ++j) t[j] = w[j * DM];
        Wf[f] = make_uint4(t[0] | (t[1] << 16), t[2] | (t[3] << 16), t[4] | (t[5] << 16), t[6] | (t[7] << 16));
      }
    }
    return;
  }
  const int wm = wave >> 1, wn = wave & 1, r = lane & 31, h = lane >> 5;
  const int trq = (lane & 15) >> 2, trp = lane & 3, trblk = (lane >> 4) & 1;
  f32x16 a;
#pragma unroll
  for (int e = 0; e < 16; ++e) a[e] = 0.f;
#pragma unroll 4
  for (int ks = 0; ks < 28; ++ks) {
    bf16x8 af = *(const bf16x8*)(as + (wm * 32 + r) * SP_AST + ks * 16 + h * 8);
    const u16* p = bs + (ks * 16 + 8 * h + trq) * SP_BST + wn * 32 + 16 * trblk + 4 * trp;
    s16x4 lo = __builtin_amdgcn_ds_read_tr16_b64_v4i16((lds_s16x4_ptr)(p));
    s16x4 hi = __builtin_amdgcn_ds_read_tr16_b64_v4i16((lds_s16x4_ptr)(p + 4 * SP_BST));
    a = MFMA32(af, __builtin_shufflevector(lo, hi, 0, 1, 2, 3, 4, 5, 6, 7), a);
  }
  const int mb = m0 + wm * 32, d = r;
  if (wn == 0) {
    const int s = d >> 4, hh = (d >> 3) & 1, j = d & 7;
#pragma unroll
    for (int e = 0; e < 16; ++e) {
      int m = mb + (e & 3) + 8 * (e >> 2) + 4 * h;
      int tile = m >> 5, rr = m & 31;
      KV[tile * 2048 + s * 512 + (hh * 32 + rr) * 8 + j] = (d < DH) ? to_bf16(a[e]) : ((d < 31) ? (u16)0x3F80 : (u16)0);
    }
  } else {
#pragma unroll
    for (int e = 0; e < 16; ++e) {
      int m = mb + (e & 3) + 8 * (e >> 2) + 4 * h;
      int tile = m >> 5, kk = m & 31;
      int s = kk >> 4, k16 = kk & 15;
      int hh = (k16 >> 2) & 1, j = ((k16 >> 3) << 2) | (k16 & 3);
      u16 val = (d < DH) ? to_bf16(a[e]) : ((d == DH) ? (u16)0x3F80 : (u16)0);
      KV[tile * 2048 + 1024 + s * 512 + (hh * 32 + d) * 8 + j] = val;
      if (d == DH) gate[m] = 1.f / (1.f + __expf(-a[e]));
    }
  }
}

DI int ofrag(int pos, int c) {
  return (((pos >> 5) * 56 + (c >> 4)) * 64 + ((c >> 3) & 1) * 32 + (pos & 31)) * 8 + (c & 7);
}

__global__ __launch_bounds__(512) void k_out(const uint4* __restrict__ Af, const uint4* __restrict__ Bf,
                                             u16* __restrict__ out) {
  __shared__ float red[4 * 16 * 64];
  const int lane = threadIdx.x & 63, wave = threadIdx.x >> 6, w4 = wave & 3, kh = wave >> 2;
  const int xi = blockIdx.x >> 3;
  const int nt = (xi % 7) * 2 + (w4 & 1), mt = (blockIdx.x & 7) * 8 + (xi / 7) * 2 + (w4 >> 1);
  const uint4* ap = Af + (mt * 56 + kh * 28) * 64 + lane;
  const uint4* bp = Bf + (nt * 56 + kh * 28) * 64 + lane;
  f32x16 acc0, acc1;
#pragma unroll
  for (int e = 0; e < 16; ++e) { acc0[e] = 0.f; acc1[e] = 0.f; }
  {
    uint4 av[28], bv[28];
#pragma unroll
    for (int i = 0; i < 28; ++i) { av[i] = ap[i * 64]; bv[i] = bp[i * 64]; }
    __builtin_amdgcn_sched_barrier(0);
#pragma unroll
    for (int i = 0; i < 28; i += 2) {
      acc0 = MFMA32(__builtin_bit_cast(bf16x8, av[i]), __builtin_bit_cast(bf16x8, bv[i]), acc0);
      acc1 = MFMA32(__builtin_bit_cast(bf16x8, av[i + 1]), __builtin_bit_cast(bf16x8, bv[i + 1]), acc1);
    }
  }
  if (kh) {
#pragma unroll
    for (int e = 0; e < 16; ++e) red[(w4 * 16 + e) * 64 + lane] = acc0[e] + acc1[e];
  }
  __syncthreads();
  if (!kh) {
    const int r = lane & 31, h = lane >> 5;
#pragma unroll
    for (int e = 0; e < 16; ++e) {
      const int m = mt * 32 + (e & 3) + 8 * (e >> 2) + 4 * h;
      out[m * DM + nt * 32 + r] = to_bf16(acc0[e] + acc1[e] + red[(w4 * 16 + e) * 64 + lane]);
    }
  }
}

constexpr int TPC = 2;
constexpr int NCH = 16 / TPC;
constexpr int CH_U4 = 4 * TPC * 256;
constexpr int QD_OFF = 3 * CH_U4;
constexpr int BQ_OFF = QD_OFF + 8 * 64;
constexpr int BK_OFF = BQ_OFF + 8 * 64;
constexpr int BK_STR = 130 * 8;
constexpr int FL_OFF = BK_OFF + 16 * 130;
constexpr int A_LDS_U4 = FL_OFF + 16;

__global__ __launch_bounds__(512) void k_attn(const u16* __restrict__ Qa, const u16* __restrict__ KV,
                                              const u16* __restrict__ QKVb, const float* __restrict__ gate,
                                              u16* __restrict__ Ocat) {
  __shared__ __attribute__((aligned(16))) uint4 lds[A_LDS_U4];
  const int tid = threadIdx.x, lane = tid & 63, wave = __builtin_amdgcn_readfirstlane(tid >> 6);
  const int rg = wave & 1, kq = wave >> 1;
  const int r = lane & 31, h = lane >> 5;
  const int xi = blockIdx.x >> 3;
  const int pbase = (blockIdx.x & 7) * 256 + (xi >> 1) * 16 + (xi & 1);
  const int rot = xi & (NCH - 1);
  const uint4* KVg = (const uint4*)KV;
  __attribute__((address_space(3))) volatile unsigned* flag = (__attribute__((address_space(3))) volatile unsigned*)(lds + FL_OFF);

  uint4 sg0, sg1, sg2, sg3;
#define KV_LOAD(c) do { const uint4* src_ = KVg + (kq * 16 + (((c) + rot) & (NCH - 1)) * TPC + rg) * 256 + lane; \
    sg0 = src_[0]; sg1 = src_[64]; sg2 = src_[128]; sg3 = src_[192]; } while (0)
#define KV_WRITE(slot) do { uint4* dst_ = lds + (slot) * CH_U4 + (kq * TPC + rg) * 256 + lane; \
    dst_[0] = sg0; dst_[64] = sg1; dst_[128] = sg2; dst_[192] = sg3; } while (0)
  const int l56 = lane < 56 ? lane : 55;
  auto issue_b = [&]() {
  glds16a((const char*)(QKVb + (pbase + 2 * wave) * NB3) + (unsigned)(l56 * 16), lds + BQ_OFF + wave * 64);
#pragma unroll
  for (int i = 0; i < 2; ++i) {
    int row = pbase - 14 + 2 * (wave * 2 + i);
    row = row < 0 ? (pbase & 1) : row;
    row = row > S_LEN - 1 ? S_LEN - 1 : row;
    const char* src = (const char*)(QKVb + row * NB3 + DM);
    const int l48 = lane < 48 ? lane : 47;
    glds16a(src + (unsigned)(lane * 16), lds + BK_OFF + (wave * 2 + i) * 130);
    glds16a(src + 1024 + (unsigned)(l48 * 16), lds + BK_OFF + (wave * 2 + i) * 130 + 64);
  }
  };
  STAMP(0);
  glds16a((const char*)(Qa + (pbase + 2 * wave) * DM) + (unsigned)(l56 * 16), lds + QD_OFF + wave * 64);
  KV_LOAD(0);
  if (tid == 0) *flag = 0u;
  float g8[8];
#pragma unroll
  for (int i = 0; i < 8; ++i) g8[i] = gate[pbase + 2 * i];
#pragma unroll
  for (int i = 0; i < 8; ++i) asm volatile("" : "+s"(g8[i]));
  KV_WRITE(0);
  KV_LOAD(1);
  raw_barrier();
  issue_b();
  KV_WRITE(1);
  KV_LOAD(2);
  STAMP(1);

  bf16x8 q0[2], q1[2];
#pragma unroll
  for (int b = 0; b < 2; ++b) {
    const int R = rg * 64 + b * 32 + r;
    const u16* qp = (const u16*)(lds + QD_OFF + (R >> 4) * 64) + (R & 15) * DH;
    uint2 qa = *(const uint2*)(qp + 8 * h), qb = *(const uint2*)(qp + 8 * h + 4);
    uint2 qc = *(const uint2*)(qp + 16 + 8 * h);
    uint2 qd = *(const uint2*)(qp + 20);
    if (h) qd = make_uint2(0, 0);
    q0[b] = __builtin_bit_cast(bf16x8, make_uint4(qa.x, qa.y, qb.x, qb.y));
    q1[b] = __builtin_bit_cast(bf16x8, make_uint4(qc.x, qc.y, qd.x, qd.y));
  }
  f32x16 zero;
#pragma unroll
  for (int e = 0; e < 16; ++e) zero[e] = 0.f;
  float mref[2];
  {
    bf16x8 k0 = __builtin_bit_cast(bf16x8, lds[lane]), k1 = __builtin_bit_cast(bf16x8, lds[64 + lane]);
#pragma unroll
    for (int b = 0; b < 2; ++b) {
      f32x16 s = MFMA32(k0, q0[b], zero);
      s = MFMA32(k1, q1[b], s);
      float m = s[0];
#pragma unroll
      for (int e = 1; e < 16; ++e) m = fmaxf(m, s[e]);
      mref[b] = fmaxf(m, __shfl_xor(m, 32));
    }
  }
  STAMP(2);

  uint2 bres[2];
  f32x16 o0, o1;
#pragma nounroll
  for (int attempt = 0; attempt < 2; ++attempt) {
    bf16x8 q1m[2];
#pragma unroll
    for (int b = 0; b < 2; ++b) {
      q1m[b] = q1[b];
      const float n0 = -mref[b];
      const u16 c0 = to_bf16(n0); const float n1 = n0 - bf_to_f(c0);
      const u16 c1 = to_bf16(n1); const float n2 = n1 - bf_to_f(c1);
      const u16 c2 = to_bf16(n2);
      if (h) { q1m[b][4] = (short)c0; q1m[b][5] = (short)c1; q1m[b][6] = (short)c2; }
    }
#pragma unroll
    for (int e = 0; e < 16; ++e) { o0[e] = 0.f; o1[e] = 0.f; }
    if (attempt) {
      KV_LOAD(0); KV_WRITE(0); KV_LOAD(1);
      raw_barrier();
      KV_WRITE(1); KV_LOAD(2);
    }
    auto softmax_pv = [&](const f32x16& s, const bf16x8& vf0, const bf16x8& vf1, f32x16& o) {
      uint4 pa, pb;
      pa.x = pack_bf16(EXP2(s[0]), EXP2(s[1]));
      pa.y = pack_bf16(EXP2(s[2]), EXP2(s[3]));
      pa.z = pack_bf16(EXP2(s[4]), EXP2(s[5]));
      pa.w = pack_bf16(EXP2(s[6]), EXP2(s[7]));
      o = MFMA32(vf0, __builtin_bit_cast(bf16x8, pa), o);
      pb.x = pack_bf16(EXP2(s[8]), EXP2(s[9]));
      pb.y = pack_bf16(EXP2(s[10]), EXP2(s[11]));
      pb.z = pack_bf16(EXP2(s[12]), EXP2(s[13]));
      pb.w = pack_bf16(EXP2(s[14]), EXP2(s[15]));
      o = MFMA32(vf1, __builtin_bit_cast(bf16x8, pb), o);
    };
    f32x16 sA, sB;
    bf16x8 kf0, kf1, vf0, vf1;
    {
      const uint4* base = lds + kq * TPC * 256 + lane;
      kf0 = __builtin_bit_cast(bf16x8, base[0]); kf1 = __builtin_bit_cast(bf16x8, base[64]);
      vf0 = __builtin_bit_cast(bf16x8, base[128]); vf1 = __builtin_bit_cast(bf16x8, base[192]);
      sA = MFMA32(kf0, q0[0], zero);
      sA = MFMA32(kf1, q1m[0], sA);
    }
    int slot = 0;
#pragma nounroll
    for (int c = 0; c < NCH; ++c) {
#pragma unroll
      for (int t = 0; t < TPC; ++t) {
        sB = MFMA32(kf0, q0[1], zero);
        sB = MFMA32(kf1, q1m[1], sB);
        const bool more = (t + 1 < TPC) || (c + 1 < NCH);
        if (t + 1 == TPC) {
          slot = (slot == 2) ? 0 : slot + 1;
          if (c + 1 < NCH) {
            raw_barrier();
            if (c + 2 < NCH) {
              KV_WRITE(slot == 2 ? 0 : slot + 1);
              if (c + 3 < NCH) KV_LOAD(c + 3);
            }
          }
        }
        bf16x8 nk0 = kf0, nk1 = kf1, nv0 = vf0, nv1 = vf1;
        if (more) {
          const uint4* base = lds + slot * CH_U4 + kq * TPC * 256 + ((t + 1) & (TPC - 1)) * 256 + lane;
          nk0 = __builtin_bit_cast(bf16x8, base[0]); nk1 = __builtin_bit_cast(bf16x8, base[64]);
          nv0 = __builtin_bit_cast(bf16x8, base[128]); nv1 = __builtin_bit_cast(bf16x8, base[192]);
        }
        __builtin_amdgcn_sched_barrier(0);
        softmax_pv(sA, vf0, vf1, o0);
        __builtin_amdgcn_sched_barrier(0);
        if (more) {
          sA = MFMA32(nk0, q0[0], zero);
          sA = MFMA32(nk1, q1m[0], sA);
        }
        __builtin_amdgcn_sched_barrier(0);
        softmax_pv(sB, vf0, vf1, o1);
        __builtin_amdgcn_sched_barrier(0);
        kf0 = nk0; kf1 = nk1; vf0 = nv0; vf1 = nv1;
      }
    }
    STAMP(3);
    STAMP(5);
  if (attempt == 0) {
    const int j = tid & 7, jc = (j < 7) ? j : 6;
    uint2 qv[2][7], kv[2][7], vv[2][8];
#pragma unroll
    for (int half = 0; half < 2; ++half) {
      const int pair = half * 64 + (tid >> 3), pi = pair >> 4, bhead = pair & 15;
      const uint2* qrow = (const uint2*)((const u16*)(lds + BQ_OFF + pi * 64) + bhead * DH);
      const u16* kb = (const u16*)(lds + BK_OFF) + bhead * DH + (pi + 7) * BK_STR;
      const uint2* krow = (const uint2*)(kb - j * BK_STR);
#pragma unroll
      for (int i = 0; i < 7; ++i) { qv[half][i] = qrow[i]; kv[half][i] = krow[i]; }
#pragma unroll
      for (int w = 0; w < 8; ++w) vv[half][w] = *(const uint2*)(kb - w * BK_STR + DM + 4 * jc);
    }
    __builtin_amdgcn_sched_barrier(0);
    float e[2], sm[2];
#pragma unroll
    for (int half = 0; half < 2; ++half) {
      const int pi = (half * 64 + (tid >> 3)) >> 4;
      float lg = 0.f;
#pragma unroll
      for (int i = 0; i < 7; ++i) {
        lg = __builtin_amdgcn_fdot2_f32_bf16(__builtin_bit_cast(bf2, qv[half][i].x), __builtin_bit_cast(bf2, kv[half][i].x), lg, false);
        lg = __builtin_amdgcn_fdot2_f32_bf16(__builtin_bit_cast(bf2, qv[half][i].y), __builtin_bit_cast(bf2, kv[half][i].y), lg, false);
      }
      const bool valid = (pbase + 2 * pi - 2 * j >= 0);
      lg = valid ? lg * (0.18898223650461362f * 1.4426950408889634f) : -3.0e38f;
      float mx = lg;
      mx = fmaxf(mx, dpp_f<0xB1>(mx)); mx = fmaxf(mx, dpp_f<0x4E>(mx)); mx = fmaxf(mx, dpp_f<0x141>(mx));
      e[half] = valid ? __builtin_amdgcn_exp2f(lg - mx) : 0.f;
      float t = e[half];
      t += dpp_f<0xB1>(t); t += dpp_f<0x4E>(t); t += dpp_f<0x141>(t);
      sm[half] = t;
    }
    float aw[2][8];
#pragma unroll
    for (int half = 0; half < 2; ++half)
#pragma unroll
      for (int w = 0; w < 8; ++w) aw[half][w] = __shfl(e[half], (lane & 56) + w);
    __builtin_amdgcn_sched_barrier(0);
#pragma unroll
    for (int half = 0; half < 2; ++half) {
      const int pair = half * 64 + (tid >> 3), pi = pair >> 4, bhead = pair & 15, bpos = pbase + 2 * pi;
      float o4[4] = {0.f, 0.f, 0.f, 0.f};
#pragma unroll
      for (int w = 0; w < 8; ++w) {
        o4[0] += aw[half][w] * __uint_as_float(vv[half][w].x << 16);
        o4[1] += aw[half][w] * __uint_as_float(vv[half][w].x & 0xffff0000u);
        o4[2] += aw[half][w] * __uint_as_float(vv[half][w].y << 16);
        o4[3] += aw[half][w] * __uint_as_float(vv[half][w].y & 0xffff0000u);
      }
      float gB = 0.f;
#pragma unroll
      for (int i = 0; i < 8; ++i) gB = (i == pi) ? g8[i] : gB;
      const float sc = (1.f - gB) / sm[half];
      bres[half].x = pack_bf16(o4[0] * sc, o4[1] * sc);
      bres[half].y = pack_bf16(o4[2] * sc, o4[3] * sc);
    }
  }

    STAMP(6);
    float* red = (float*)lds;
    {
      float* dst = red + (kq >> 1) * (2 * CH_U4 * 4) + ((kq & 1) * 4 + rg * 2) * 1024 + lane;
#pragma unroll
      for (int e = 0; e < 16; ++e) { dst[e * 64] = o0[e]; dst[1024 + e * 64] = o1[e]; }
    }
    if (attempt == 0 && h && !(o0[12] < 1.0e18f && o1[12] < 1.0e18f)) *flag = 1u;
    raw_barrier();
    if (attempt == 1 || *flag == 0u) {
      const int rb = wave & 3, dq = (wave >> 2) * 2;
      float l = 0.f, ov[8];
#pragma unroll
      for (int i = 0; i < 8; ++i) ov[i] = 0.f;
#pragma unroll
      for (int q = 0; q < 4; ++q) {
        const float* src = red + (q >> 1) * (2 * CH_U4 * 4) + ((q & 1) * 4 + rb) * 1024;
        l += src[12 * 64 + 32 + r];
#pragma unroll
        for (int i = 0; i < 8; ++i) ov[i] += src[(4 * dq + i) * 64 + lane];
      }
      const int R = rb * 32 + r;
      const int pos = pbase + 2 * (R >> 4), head = R & 15;
      float gA = 0.f;
#pragma unroll
      for (int i = 0; i < 8; ++i) gA = (i == (R >> 4)) ? g8[i] : gA;
      const float sc = gA / l;
#pragma unroll
      for (int g = 0; g < 2; ++g) {
        const int d = 8 * (dq + g) + 4 * h;
        if (d < DH) {
          uint2 w;
          w.x = pack_bf16(ov[4 * g] * sc, ov[4 * g + 1] * sc);
          w.y = pack_bf16(ov[4 * g + 2] * sc, ov[4 * g + 3] * sc);
          *(uint2*)(Ocat + ofrag(pos, head * DH + d)) = w;
        }
      }
      if ((tid & 7) < 7) {
#pragma unroll
        for (int half = 0; half < 2; ++half) {
          const int pair = half * 64 + (tid >> 3);
          *(uint2*)(Ocat + ofrag(pbase + 2 * (pair >> 4), DM + (pair & 15) * DH + 4 * (tid & 7))) = bres[half];
        }
      }
      STAMP(4);
      break;
    }
    {
      float m[2] = {-3.0e38f, -3.0e38f};
      for (int t = 0; t < 16; ++t) {
        const uint4* src = KVg + (kq * 16 + t) * 256 + lane;
        bf16x8 k0 = __builtin_bit_cast(bf16x8, src[0]);
        bf16x8 k1 = __builtin_bit_cast(bf16x8, src[64]);
#pragma unroll
        for (int b = 0; b < 2; ++b) {
          f32x16 s = MFMA32(k0, q0[b], zero);
          s = MFMA32(k1, q1[b], s);
#pragma unroll
          for (int e = 0; e < 16; ++e) m[b] = fmaxf(m[b], s[e]);
        }
      }
      m[0] = fmaxf(m[0], __shfl_xor(m[0], 32));
      m[1] = fmaxf(m[1], __shfl_xor(m[1], 32));
      wait_vm<0>();
      raw_barrier();
      red[(wave * 2) * 64 + lane] = m[0];
      red[(wave * 2 + 1) * 64 + lane] = m[1];
      raw_barrier();
#pragma unroll
      for (int b = 0; b < 2; ++b) {
        float mm = red[(rg * 2 + b) * 64 + lane];
#pragma unroll
        for (int q = 1; q < 4; ++q) mm = fmaxf(mm, red[((q * 2 + rg) * 2 + b) * 64 + lane]);
        mref[b] = mm;
      }
      raw_barrier();
    }
  }
}

extern "C" void kernel_launch(void* const* d_in, const int* in_sizes, int n_in,
                              void* d_out, int out_size, void* d_ws, size_t ws_size,
                              hipStream_t stream) {
  const u16* x = (const u16*)d_in[0];
  const u16* wq = (const u16*)d_in[1];
  const u16* wk = (const u16*)d_in[2];
  const u16* wv = (const u16*)d_in[3];
  const u16* woA = (const u16*)d_in[4];
  const u16* wqkv = (const u16*)d_in[6];
  const u16* woB = (const u16*)d_in[7];
  const u16* gw = (const u16*)d_in[8];
  char* ws = (char*)d_ws;
  u16* Qa = (u16*)(ws);
  u16* KV = (u16*)(ws + (2u << 20));
  u16* QKVb = (u16*)(ws + (3u << 20));
  float* gate = (float*)(ws + (9u << 20));
  u16* Ocat = (u16*)(ws + (10u << 20));
  uint4* Wf = (uint4*)(ws + (14u << 20));
  u16* out = (u16*)d_out;

  k_proj<<<dim3(256), 512, 0, stream>>>(x, wq, wk, wv, wqkv, gw, woA, woB, Qa, KV, QKVb, gate, Wf);
  k_attn<<<dim3(S_LEN / 8), 512, 0, stream>>>(Qa, KV, QKVb, gate, Ocat);
  k_out<<<dim3(224), 512, 0, stream>>>((const uint4*)Ocat, Wf, out);
}
```

```cpp
#include <hip/hip_runtime.h>
#include <hip/hip_bf16.h>
#include <stdint.h>

typedef __attribute__((ext_vector_type(8))) short bf16x8;
typedef __attribute__((ext_vector_type(4))) short s16x4;
typedef __attribute__((ext_vector_type(16))) float f32x16;
typedef __attribute__((ext_vector_type(2))) __bf16 bf2;
typedef __attribute__((address_space(3))) s16x4* lds_s16x4_ptr;
typedef unsigned short u16;

#define DI __device__ __forceinline__
#define STAMP(i) do {} while (0)
#define STAMPG(k, i) do {} while (0)
#define EXP2(x) __builtin_amdgcn_exp2f(x)
#define MFMA32(a, b, c) __builtin_amdgcn_mfma_f32_32x32x16_bf16((a), (b), (c), 0, 0, 0)

constexpr int S_LEN = 2048;
constexpr int DM = 448;
constexpr int NH = 16;
constexpr int DH = 28;
constexpr int NB3 = 1344;
constexpr int OC = 896;

DI unsigned pack_bf16(float a, float b) {
  bf2 v; v[0] = (__bf16)a; v[1] = (__bf16)b;
  return __builtin_bit_cast(unsigned, v);
}
DI u16 to_bf16(float a) { __bf16 v = (__bf16)a; return __builtin_bit_cast(u16, v); }
DI float bf_to_f(u16 a) { return __uint_as_float(((unsigned)a) << 16); }

template <int V> struct IC { static constexpr int value = V; };
template <int I, int N, class F> DI void static_for(F&& f) {
  if constexpr (I < N) { f(IC<I>{}); static_for<I + 1, N>(f); }
}
template <int N> DI void wait_vm() { asm volatile("s_waitcnt vmcnt(%0)" ::"n"(N) : "memory"); }
DI void raw_barrier() { asm volatile("s_waitcnt lgkmcnt(0)" ::: "memory"); __builtin_amdgcn_s_barrier(); asm volatile("" ::: "memory"); }
DI void glds16(const void* g, void* l) {
  __builtin_amdgcn_global_load_lds((const __attribute__((address_space(1))) unsigned*)g,
                                   (__attribute__((address_space(3))) unsigned*)l, 16, 0, 0);
}

template <int CTRL> DI float dpp_f(float v) {
  return __int_as_float(__builtin_amdgcn_update_dpp(0, __float_as_int(v), CTRL, 0xf, 0xf, true));
}
DI void glds16a(const void* g, const void* l) {
  const unsigned la = __builtin_amdgcn_readfirstlane((unsigned)(size_t)l);
  unsigned keep;
  asm volatile("s_mov_b32 %0, m0\n\ts_mov_b32 m0, %2\n\ts_nop 0\n\tglobal_load_lds_dwordx4 %1, off\n\ts_mov_b32 m0, %0"
               : "=&s"(keep) : "v"(g), "s"(la) : "memory");
}

constexpr int G_AST = 72, G_BST = 160;
constexpr int G_STAGE = 128 * G_AST + 64 * G_BST;

struct G128Regs { uint4 a0, a1, b0, b1; };
DI G128Regs g128_load(const u16* as0, const u16* as1, const u16* bs0, const u16* bs1, int ld0, int ld1, int st) {
  G128Regs v;
  v.a0 = *(const uint4*)(as0 + st * 64);
  v.a1 = *(const uint4*)(as1 + st * 64);
  v.b0 = *(const uint4*)(bs0 + st * 64 * ld0);
  v.b1 = *(const uint4*)(bs1 + st * 64 * ld1);
  return v;
}
DI void g128_store(const G128Regs& v, u16* buf, int tid) {
  u16* bs = buf + 128 * G_AST;
  *(uint4*)(buf + (tid >> 3) * G_AST + (tid & 7) * 8) = v.a0;
  *(uint4*)(buf + ((tid >> 3) + 64) * G_AST + (tid & 7) * 8) = v.a1;
  *(uint4*)(bs + (tid >> 4) * G_BST + (tid & 15) * 8) = v.b0;
  *(uint4*)(bs + ((tid >> 4) + 32) * G_BST + (tid & 15) * 8) = v.b1;
}
DI void g128_compute(const u16* buf, int wm, int wn, int lane, f32x16& acc0, f32x16& acc1) {
  const int r = lane & 31, h = lane >> 5;
  const int trq = (lane & 15) >> 2, trp = lane & 3, trblk = (lane >> 4) & 1;
  const u16* bs = buf + 128 * G_AST;
#pragma unroll
  for (int ks = 0; ks < 4; ++ks) {
    const bf16x8 af = *(const bf16x8*)(buf + (wm * 32 + r) * G_AST + ks * 16 + h * 8);
    const u16* p = bs + (ks * 16 + 8 * h + trq) * G_BST + wn * 64 + 16 * trblk + 4 * trp;
    s16x4 lo0 = __builtin_amdgcn_ds_read_tr16_b64_v4i16((lds_s16x4_ptr)(p));
    s16x4 hi0 = __builtin_amdgcn_ds_read_tr16_b64_v4i16((lds_s16x4_ptr)(p + 4 * G_BST));
    s16x4 lo1 = __builtin_amdgcn_ds_read_tr16_b64_v4i16((lds_s16x4_ptr)(p + 32));
    s16x4 hi1 = __builtin_amdgcn_ds_read_tr16_b64_v4i16((lds_s16x4_ptr)(p + 4 * G_BST + 32));
    acc0 = MFMA32(af, __builtin_shufflevector(lo0, hi0, 0, 1, 2, 3, 4, 5, 6, 7), acc0);
    acc1 = MFMA32(af, __builtin_shufflevector(lo1, hi1, 0, 1, 2, 3, 4, 5, 6, 7), acc1);
  }
}

constexpr int SP_AST = 456, SP_BST = 96;
constexpr int P_SMEM = (64 * SP_AST + 448 * SP_BST) * 2;

__global__ __launch_bounds__(512) void k_proj(const u16* __restrict__ x, const u16* __restrict__ wq,
                                              const u16* __restrict__ wk, const u16* __restrict__ wv,
                                              const u16* __restrict__ wqkv, const u16* __restrict__ gw,
                                              const u16* __restrict__ woA, const u16* __restrict__ woB,
                                              u16* __restrict__ Qa, u16* __restrict__ KV,
                                              u16* __restrict__ QKVb, float* __restrict__ gate,
                                              uint4* __restrict__ Wf) {
  __shared__ __attribute__((aligned(16))) char smem[P_SMEM];
  const int bid = blockIdx.x;
  if (bid < 224) {
    const int xi = bid >> 3;
    const int n0 = (xi % 14) * 128, m0 = (bid & 7) * 256 + (xi / 14) * 128;
    const int tid = threadIdx.x, lane = tid & 63, wave = tid >> 6, wm = wave >> 1, wn = wave & 1;
    const u16* as0 = x + (m0 + (tid >> 3)) * DM + (tid & 7) * 8;
    const u16* as1 = as0 + 64 * DM;
    const int nb = n0 + (tid & 15) * 8;
    const int ld = (nb < DM) ? DM : NB3;
    const u16* bs0 = ((nb < DM) ? (wq + nb) : (wqkv + (nb - DM))) + (tid >> 4) * ld;
    const u16* bs1 = bs0 + 32 * ld;
    u16* buf0 = (u16*)smem;
    u16* buf1 = buf0 + G_STAGE;
    f32x16 acc0, acc1;
#pragma unroll
    for (int e = 0; e < 16; ++e) { acc0[e] = 0.f; acc1[e] = 0.f; }
    STAMPG(0, 0);
    G128Regs r0 = g128_load(as0, as1, bs0, bs1, ld, ld, 0);
    G128Regs r1 = g128_load(as0, as1, bs0, bs1, ld, ld, 1);
    g128_store(r0, buf0, tid);
    r0 = g128_load(as0, as1, bs0, bs1, ld, ld, 2);
    __syncthreads();
    STAMPG(0, 1);
#pragma nounroll
    for (int st = 0; st < 6; st += 2) {
      g128_compute(buf0, wm, wn, lane, acc0, acc1);
      g128_store(r1, buf1, tid);
      if (st + 3 < 7) r1 = g128_load(as0, as1, bs0, bs1, ld, ld, st + 3);
      __syncthreads();
      g128_compute(buf1, wm, wn, lane, acc0, acc1);
      g128_store(r0, buf0, tid);
      if (st + 4 < 7) r0 = g128_load(as0, as1, bs0, bs1, ld, ld, st + 4);
      __syncthreads();
    }
    g128_compute(buf0, wm, wn, lane, acc0, acc1);
    STAMPG(0, 2);
    const float qscale = 0.18898223650461362f * 1.4426950408889634f;
    const int r = lane & 31, h = lane >> 5, mb = m0 + wm * 32;
#pragma unroll
    for (int j = 0; j < 2; ++j) {
      const f32x16& a = j ? acc1 : acc0;
      const int nt = n0 + wn * 64 + j * 32, n = nt + r;
      if (nt < DM) {
#pragma unroll
        for (int e = 0; e < 16; ++e) Qa[(mb + (e & 3) + 8 * (e >> 2) + 4 * h) * DM + n] = to_bf16(a[e] * qscale);
      } else {
#pragma unroll
        for (int e = 0; e < 16; ++e) QKVb[(mb + (e & 3) + 8 * (e >> 2) + 4 * h) * NB3 + (n - DM)] = to_bf16(a[e]);
      }
    }
    STAMPG(0, 3);
    return;
  }
  const int m0 = (bid & 7) * 256 + ((bid - 224) >> 3) * 64;
  const int tid = threadIdx.x, lane = tid & 63, wave = tid >> 6;
  u16* as = (u16*)smem;
  u16* bs = as + 64 * SP_AST;
  uint4 ra[7]; uint2 rb0[7], rb1[7]; u16 rg[7];
#pragma unroll
  for (int i = 0; i < 7; ++i) {
    int c = tid + i * 512, row = c / 56, ch = c % 56;
    ra[i] = *(const uint4*)(x + (m0 + row) * DM + ch * 8);
  }
  const int nc = tid & 7;
  const u16* wsrc = (nc < 4) ? wk : wv;
  const int cb = (nc & 3) * 8;
#pragma unroll
  for (int i = 0; i < 7; ++i) {
    int k = (tid >> 3) + i * 64;
    rb0[i] = *(const uint2*)(wsrc + k * DH + cb);
    rb1[i] = *(const uint2*)(wsrc + k * DH + ((cb < 24) ? cb + 4 : cb));
    rg[i] = gw[k];
  }
#pragma unroll
  for (int i = 0; i < 7; ++i) {
    int c = tid + i * 512, row = c / 56, ch = c % 56;
    *(uint4*)(as + row * SP_AST + ch * 8) = ra[i];
  }
#pragma unroll
  for (int i = 0; i < 7; ++i) {
    int k = (tid >> 3) + i * 64;
    uint4 v = make_uint4(rb0[i].x, rb0[i].y, rb1[i].x, rb1[i].y);
    if (cb == 24) { v.z = (nc == 7) ? (unsigned)rg[i] : 0u; v.w = 0u; }
    *(uint4*)(bs + k * SP_BST + nc * 8) = v;
  }
  __syncthreads();
  if (wave >= 4) {
    const int f0 = (bid - 224) * 1568, t4 = tid - 256;
#pragma unroll
    for (int i = 0; i < 7; ++i) {
      const int f = f0 + i * 256 + t4;
      if (i < 6 || t4 < 32) {
        const int fl = f & 63, ks = (f >> 6) % 56, nt = (f >> 6) / 56;
        const int k = ks * 16 + (fl >> 5) * 8, n = nt * 32 + (fl & 31);
        const u16* w = (k < DM) ? (woA + k * DM + n) : (woB + (k - DM) * DM + n);
        unsigned t[8];
#pragma unroll
        for (int j = 0; j < 8; ++j) t[j] = w[j * DM];
        Wf[f] = make_uint4(t[0] | (t[1] << 16), t[2] | (t[3] << 16), t[4] | (t[5] << 16), t[6] | (t[7] << 16));
      }
    }
    return;
  }
  const int wm = wave >> 1, wn = wave & 1, r = lane & 31, h = lane >> 5;
  const int trq = (lane & 15) >> 2, trp = lane & 3, trblk = (lane >> 4) & 1;
  f32x16 a;
#pragma unroll
  for (int e = 0; e < 16; ++e) a[e] = 0.f;
#pragma unroll 4
  for (int ks = 0; ks < 28; ++ks) {
    bf16x8 af = *(const bf16x8*)(as + (wm * 32 + r) * SP_AST + ks * 16 + h * 8);
    const u16* p = bs + (ks * 16 + 8 * h + trq) * SP_BST + wn * 32 + 16 * trblk + 4 * trp;
    s16x4 lo = __builtin_amdgcn_ds_read_tr16_b64_v4i16((lds_s16x4_ptr)(p));
    s16x4 hi = __builtin_amdgcn_ds_read_tr16_b64_v4i16((lds_s16x4_ptr)(p + 4 * SP_BST));
    a = MFMA32(af, __builtin_shufflevector(lo, hi, 0, 1, 2, 3, 4, 5, 6, 7), a);
  }
  const int mb = m0 + wm * 32, d = r;
  if (wn == 0) {
    const int s = d >> 4, hh = (d >> 3) & 1, j = d & 7;
#pragma unroll
    for (int e = 0; e < 16; ++e) {
      int m = mb + (e & 3) + 8 * (e >> 2) + 4 * h;
      int tile = m >> 5, rr = m & 31;
      KV[tile * 2048 + s * 512 + (hh * 32 + rr) * 8 + j] = (d < DH) ? to_bf16(a[e]) : ((d < 31) ? (u16)0x3F80 : (u16)0);
    }
  } else {
#pragma unroll
    for (int e = 0; e < 16; ++e) {
      int m = mb + (e & 3) + 8 * (e >> 2) + 4 * h;
      int tile = m >> 5, kk = m & 31;
      int s = kk >> 4, k16 = kk & 15;
      int hh = (k16 >> 2) & 1, j = ((k16 >> 3) << 2) | (k16 & 3);
      u16 val = (d < DH) ? to_bf16(a[e]) : ((d == DH) ? (u16)0x3F80 : (u16)0);
      KV[tile * 2048 + 1024 + s * 512 + (hh * 32 + d) * 8 + j] = val;
      if (d == DH) gate[m] = 1.f / (1.f + __expf(-a[e]));
    }
  }
}

DI int ofrag(int pos, int c) {
  return (((pos >> 5) * 56 + (c >> 4)) * 64 + ((c >> 3) & 1) * 32 + (pos & 31)) * 8 + (c & 7);
}

__global__ __launch_bounds__(512) void k_out(const uint4* __restrict__ Af, const uint4* __restrict__ Bf,
                                             u16* __restrict__ out) {
  __shared__ float red[4 * 16 * 64];
  const int lane = threadIdx.x & 63, wave = threadIdx.x >> 6, w4 = wave & 3, kh = wave >> 2;
  const int xi = blockIdx.x >> 3;
  const int nt = (xi % 7) * 2 + (w4 & 1), mt = (blockIdx.x & 7) * 8 + (xi / 7) * 2 + (w4 >> 1);
  const uint4* ap = Af + (mt * 56 + kh * 28) * 64 + lane;
  const uint4* bp = Bf + (nt * 56 + kh * 28) * 64 + lane;
  f32x16 acc0, acc1;
#pragma unroll
  for (int e = 0; e < 16; ++e) { acc0[e] = 0.f; acc1[e] = 0.f; }
  {
    uint4 av[28], bv[28];
#pragma unroll
    for (int i = 0; i < 28; ++i) { av[i] = ap[i * 64]; bv[i] = bp[i * 64]; }
    __builtin_amdgcn_sched_barrier(0);
#pragma unroll
    for (int i = 0; i < 28; i += 2) {
      acc0 = MFMA32(__builtin_bit_cast(bf16x8, av[i]), __builtin_bit_cast(bf16x8, bv[i]), acc0);
      acc1 = MFMA32(__builtin_bit_cast(bf16x8, av[i + 1]), __builtin_bit_cast(bf16x8, bv[i + 1]), acc1);
    }
  }
  if (kh) {
#pragma unroll
    for (int e = 0; e < 16; ++e) red[(w4 * 16 + e) * 64 + lane] = acc0[e] + acc1[e];
  }
  __syncthreads();
  if (!kh) {
    const int r = lane & 31, h = lane >> 5;
#pragma unroll
    for (int e = 0; e < 16; ++e) {
      const int m = mt * 32 + (e & 3) + 8 * (e >> 2) + 4 * h;
      out[m * DM + nt * 32 + r] = to_bf16(acc0[e] + acc1[e] + red[(w4 * 16 + e) * 64 + lane]);
    }
  }
}

constexpr int RED_U4 = 4096;
constexpr int BQ_OFF = RED_U4;
constexpr int BK_OFF = BQ_OFF + 8 * 64;
constexpr int BK_STR = 130 * 8;
constexpr int FL_OFF = BK_OFF + 16 * 130;
constexpr int A_LDS_U4 = FL_OFF + 16;

struct KVT { uint4 k0, k1, v0, v1; };
DI KVT kv_load(const uint4* p) { KVT t; t.k0 = p[0]; t.k1 = p[64]; t.v0 = p[128]; t.v1 = p[192]; return t; }
#define BF8(x) __builtin_bit_cast(bf16x8, (x))

DI void softmax_pv(const f32x16& s, const uint4& vf0, const uint4& vf1, f32x16& o) {
  uint4 pa, pb;
  pa.x = pack_bf16(EXP2(s[0]), EXP2(s[1]));
  pa.y = pack_bf16(EXP2(s[2]), EXP2(s[3]));
  pa.z = pack_bf16(EXP2(s[4]), EXP2(s[5]));
  pa.w = pack_bf16(EXP2(s[6]), EXP2(s[7]));
  o = MFMA32(BF8(vf0), BF8(pa), o);
  pb.x = pack_bf16(EXP2(s[8]), EXP2(s[9]));
  pb.y = pack_bf16(EXP2(s[10]), EXP2(s[11]));
  pb.z = pack_bf16(EXP2(s[12]), EXP2(s[13]));
  pb.w = pack_bf16(EXP2(s[14]), EXP2(s[15]));
  o = MFMA32(BF8(vf1), BF8(pb), o);
}
#define INTERLEAVE() do { \
  __builtin_amdgcn_sched_group_barrier(0x008, 1, 0); __builtin_amdgcn_sched_group_barrier(0x400, 4, 0); \
  __builtin_amdgcn_sched_group_barrier(0x008, 1, 0); __builtin_amdgcn_sched_group_barrier(0x400, 4, 0); \
  __builtin_amdgcn_sched_group_barrier(0x002, 4, 0); __builtin_amdgcn_sched_group_barrier(0x008, 1, 0); \
  __builtin_amdgcn_sched_group_barrier(0x400, 8, 0); __builtin_amdgcn_sched_group_barrier(0x002, 4, 0); \
  __builtin_amdgcn_sched_group_barrier(0x008, 1, 0); } while (0)

__global__ __launch_bounds__(512) void k_attn(const u16* __restrict__ Qa, const u16* __restrict__ KV,
                                              const u16* __restrict__ QKVb, const float* __restrict__ gate,
                                              u16* __restrict__ Ocat) {
  __shared__ __attribute__((aligned(16))) uint4 lds[A_LDS_U4];
  const int tid = threadIdx.x, lane = tid & 63, wave = __builtin_amdgcn_readfirstlane(tid >> 6);
  const int rg = wave & 1, kq = wave >> 1;
  const int r = lane & 31, h = lane >> 5;
  const int xi = blockIdx.x >> 3;
  const int pbase = (blockIdx.x & 7) * 256 + (xi >> 1) * 16 + (xi & 1);
  const int rot = (xi & 7) * 2;
  const uint4* KVg = (const uint4*)KV;
  const uint4* kvq = KVg + kq * 16 * 256 + lane;
  __attribute__((address_space(3))) volatile unsigned* flag = (__attribute__((address_space(3))) volatile unsigned*)(lds + FL_OFF);
  STAMP(0);

  bf16x8 q0[2], q1[2];
#pragma unroll
  for (int b = 0; b < 2; ++b) {
    const int R = rg * 64 + b * 32 + r;
    const u16* qp = Qa + (pbase + 2 * (R >> 4)) * DM + (R & 15) * DH;
    uint2 qa = *(const uint2*)(qp + 8 * h), qb = *(const uint2*)(qp + 8 * h + 4);
    uint2 qc = *(const uint2*)(qp + 16 + 8 * h);
    uint2 qd = *(const uint2*)(qp + 20);
    if (h) qd = make_uint2(0, 0);
    q0[b] = BF8(make_uint4(qa.x, qa.y, qb.x, qb.y));
    q1[b] = BF8(make_uint4(qc.x, qc.y, qd.x, qd.y));
  }
  const uint4 k00 = KVg[lane], k01 = KVg[64 + lane];
  KVT R0 = kv_load(kvq + ((0 + rot) & 15) * 256), R1 = kv_load(kvq + ((1 + rot) & 15) * 256);
  KVT R2 = kv_load(kvq + ((2 + rot) & 15) * 256), R3 = kv_load(kvq + ((3 + rot) & 15) * 256);
  {
    const int l56 = lane < 56 ? lane : 55, l48 = lane < 48 ? lane : 47;
    glds16a((const char*)(QKVb + (pbase + 2 * wave) * NB3) + (unsigned)(l56 * 16), lds + BQ_OFF + wave * 64);
#pragma unroll
    for (int i = 0; i < 2; ++i) {
      int row = pbase - 14 + 2 * (wave * 2 + i);
      row = row < 0 ? (pbase & 1) : row;
      row = row > S_LEN - 1 ? S_LEN - 1 : row;
      const char* src = (const char*)(QKVb + row * NB3 + DM);
      glds16a(src + (unsigned)(lane * 16), lds + BK_OFF + (wave * 2 + i) * 130);
      glds16a(src + 1024 + (unsigned)(l48 * 16), lds + BK_OFF + (wave * 2 + i) * 130 + 64);
    }
  }
  if (tid == 0) *flag = 0u;
  float g8[8];
#pragma unroll
  for (int i = 0; i < 8; ++i) g8[i] = gate[pbase + 2 * i];
#pragma unroll
  for (int i = 0; i < 8; ++i) asm volatile("" : "+s"(g8[i]));
  STAMP(1);

  f32x16 zero;
#pragma unroll
  for (int e = 0; e < 16; ++e) zero[e] = 0.f;
  float mref[2];
#pragma unroll
  for (int b = 0; b < 2; ++b) {
    f32x16 s = MFMA32(BF8(k00), q0[b], zero);
    s = MFMA32(BF8(k01), q1[b], s);
    float m = s[0];
#pragma unroll
    for (int e = 1; e < 16; ++e) m = fmaxf(m, s[e]);
    mref[b] = fmaxf(m, __shfl_xor(m, 32));
  }
  STAMP(2);

  uint2 bres[2];
  f32x16 o0, o1;
  float* red = (float*)lds;
#pragma nounroll
  for (int attempt = 0; attempt < 2; ++attempt) {
    bf16x8 q1m[2];
#pragma unroll
    for (int b = 0; b < 2; ++b) {
      q1m[b] = q1[b];
      const float n0 = -mref[b];
      const u16 c0 = to_bf16(n0); const float n1 = n0 - bf_to_f(c0);
      const u16 c1 = to_bf16(n1); const float n2 = n1 - bf_to_f(c1);
      const u16 c2 = to_bf16(n2);
      if (h) { q1m[b][4] = (short)c0; q1m[b][5] = (short)c1; q1m[b][6] = (short)c2; }
    }
#pragma unroll
    for (int e = 0; e < 16; ++e) { o0[e] = 0.f; o1[e] = 0.f; }
    f32x16 sA = MFMA32(BF8(R0.k0), q0[0], zero), sB;
    sA = MFMA32(BF8(R0.k1), q1m[0], sA);
#define STEP(CUR, NXT, I) do { \
      __builtin_amdgcn_sched_barrier(0); \
      sB = MFMA32(BF8(CUR.k0), q0[1], zero); sB = MFMA32(BF8(CUR.k1), q1m[1], sB); \
      softmax_pv(sA, CUR.v0, CUR.v1, o0); INTERLEAVE(); \
      __builtin_amdgcn_sched_barrier(0); \
      sA = MFMA32(BF8(NXT.k0), q0[0], zero); sA = MFMA32(BF8(NXT.k1), q1m[0], sA); \
      softmax_pv(sB, CUR.v0, CUR.v1, o1); INTERLEAVE(); \
      __builtin_amdgcn_sched_barrier(0); \
      if ((I) + 4 < 16) CUR = kv_load(kvq + (((I) + 4 + rot) & 15) * 256); } while (0)
#pragma nounroll
    for (int i = 0; i < 16; i += 4) {
      STEP(R0, R1, i);
      STEP(R1, R2, i + 1);
      STEP(R2, R3, i + 2);
      STEP(R3, R0, i + 3);
    }
    STAMP(3);
    if (attempt == 0) {
      wait_vm<0>();
      raw_barrier();
      const int j = tid & 7, jc = (j < 7) ? j : 6;
      uint2 qv[2][7], kv[2][7], vv[2][8];
#pragma unroll
      for (int half = 0; half < 2; ++half) {
        const int pair = half * 64 + (tid >> 3), pi = pair >> 4, bhead = pair & 15;
        const uint2* qrow = (const uint2*)((const u16*)(lds + BQ_OFF + pi * 64) + bhead * DH);
        const u16* kb = (const u16*)(lds + BK_OFF) + bhead * DH + (pi + 7) * BK_STR;
        const uint2* krow = (const uint2*)(kb - j * BK_STR);
#pragma unroll
        for (int i = 0; i < 7; ++i) { qv[half][i] = qrow[i]; kv[half][i] = krow[i]; }
#pragma unroll
        for (int w = 0; w < 8; ++w) vv[half][w] = *(const uint2*)(kb - w * BK_STR + DM + 4 * jc);
      }
      __builtin_amdgcn_sched_barrier(0);
      float e[2], sm[2];
#pragma unroll
      for (int half = 0; half < 2; ++half) {
        const int pi = (half * 64 + (tid >> 3)) >> 4;
        float lg = 0.f;
#pragma unroll
        for (int i = 0; i < 7; ++i) {
          lg = __builtin_amdgcn_fdot2_f32_bf16(__builtin_bit_cast(bf2, qv[half][i].x), __builtin_bit_cast(bf2, kv[half][i].x), lg, false);
          lg = __builtin_amdgcn_fdot2_f32_bf16(__builtin_bit_cast(bf2, qv[half][i].y), __builtin_bit_cast(bf2, kv[half][i].y), lg, false);
        }
        const bool valid = (pbase + 2 * pi - 2 * j >= 0);
        lg = valid ? lg * (0.18898223650461362f * 1.4426950408889634f) : -3.0e38f;
        float mx = lg;
        mx = fmaxf(mx, dpp_f<0xB1>(mx)); mx = fmaxf(mx, dpp_f<0x4E>(mx)); mx = fmaxf(mx, dpp_f<0x141>(mx));
        e[half] = valid ? __builtin_amdgcn_exp2f(lg - mx) : 0.f;
        float t = e[half];
        t += dpp_f<0xB1>(t); t += dpp_f<0x4E>(t); t += dpp_f<0x141>(t);
        sm[half] = t;
      }
      float aw[2][8];
#pragma unroll
      for (int half = 0; half < 2; ++half)
#pragma unroll
        for (int w = 0; w < 8; ++w) aw[half][w] = __shfl(e[half], (lane & 56) + w);
      __builtin_amdgcn_sched_barrier(0);
#pragma unroll
      for (int half = 0; half < 2; ++half) {
        const int pi = (half * 64 + (tid >> 3)) >> 4;
        float o4[4] = {0.f, 0.f, 0.f, 0.f};
#pragma unroll
        for (int w = 0; w < 8; ++w) {
          o4[0] += aw[half][w] * __uint_as_float(vv[half][w].x << 16);
          o4[1] += aw[half][w] * __uint_as_float(vv[half][w].x & 0xffff0000u);
          o4[2] += aw[half][w] * __uint_as_float(vv[half][w].y << 16);
          o4[3] += aw[half][w] * __uint_as_float(vv[half][w].y & 0xffff0000u);
        }
        float gB = 0.f;
#pragma unroll
        for (int i = 0; i < 8; ++i) gB = (i == pi) ? g8[i] : gB;
        const float sc = (1.f - gB) / sm[half];
        bres[half].x = pack_bf16(o4[0] * sc, o4[1] * sc);
        bres[half].y = pack_bf16(o4[2] * sc, o4[3] * sc);
      }
    }
    {
      float* dst = red + (kq * 4 + rg * 2) * 1024 + lane;
#pragma unroll
      for (int e = 0; e < 16; ++e) { dst[e * 64] = o0[e]; dst[1024 + e * 64] = o1[e]; }
    }
    if (attempt == 0 && h && !(o0[12] < 1.0e18f && o1[12] < 1.0e18f)) *flag = 1u;
    raw_barrier();
    if (attempt == 1 || *flag == 0u) {
      const int rb = wave & 3, dq = (wave >> 2) * 2;
      float l = 0.f, ov[8];
#pragma unroll
      for (int i = 0; i < 8; ++i) ov[i] = 0.f;
#pragma unroll
      for (int q = 0; q < 4; ++q) {
        const float* src = red + (q * 4 + rb) * 1024;
        l += src[12 * 64 + 32 + r];
#pragma unroll
        for (int i = 0; i < 8; ++i) ov[i] += src[(4 * dq + i) * 64 + lane];
      }
      const int R = rb * 32 + r;
      const int pos = pbase + 2 * (R >> 4), head = R & 15;
      float gA = 0.f;
#pragma unroll
      for (int i = 0; i < 8; ++i) gA = (i == (R >> 4)) ? g8[i] : gA;
      const float sc = gA / l;
#pragma unroll
      for (int g = 0; g < 2; ++g) {
        const int d = 8 * (dq + g) + 4 * h;
        if (d < DH) {
          uint2 w;
          w.x = pack_bf16(ov[4 * g] * sc, ov[4 * g + 1] * sc);
          w.y = pack_bf16(ov[4 * g + 2] * sc, ov[4 * g + 3] * sc);
          *(uint2*)(Ocat + ofrag(pos, head * DH + d)) = w;
        }
      }
      if ((tid & 7) < 7) {
#pragma unroll
        for (int half = 0; half < 2; ++half) {
          const int pair = half * 64 + (tid >> 3);
          *(uint2*)(Ocat + ofrag(pbase + 2 * (pair >> 4), DM + (pair & 15) * DH + 4 * (tid & 7))) = bres[half];
        }
      }
      STAMP(4);
      break;
    }
    {
      float m[2] = {-3.0e38f, -3.0e38f};
#pragma nounroll
      for (int t = 0; t < 16; ++t) {
        const uint4 k0 = kvq[t * 256], k1 = kvq[t * 256 + 64];
#pragma unroll
        for (int b = 0; b < 2; ++b) {
          f32x16 s = MFMA32(BF8(k0), q0[b], zero);
          s = MFMA32(BF8(k1), q1[b], s);
#pragma unroll
          for (int e = 0; e < 16; ++e) m[b] = fmaxf(m[b], s[e]);
        }
      }
      m[0] = fmaxf(m[0], __shfl_xor(m[0], 32));
      m[1] = fmaxf(m[1], __shfl_xor(m[1], 32));
      raw_barrier();
      red[(wave * 2) * 64 + lane] = m[0];
      red[(wave * 2 + 1) * 64 + lane] = m[1];
      raw_barrier();
#pragma unroll
      for (int b = 0; b < 2; ++b) {
        float mm = red[(rg * 2 + b) * 64 + lane];
#pragma unroll
        for (int q = 1; q < 4; ++q) mm = fmaxf(mm, red[((q * 2 + rg) * 2 + b) * 64 + lane]);
        mref[b] = mm;
      }
      raw_barrier();
      R0 = kv_load(kvq + ((0 + rot) & 15) * 256); R1 = kv_load(kvq + ((1 + rot) & 15) * 256);
      R2 = kv_load(kvq + ((2 + rot) & 15) * 256); R3 = kv_load(kvq + ((3 + rot) & 15) * 256);
    }
  }
}

extern "C" void kernel_launch(void* const* d_in, const int* in_sizes, int n_in,
                              void* d_out, int out_size, void* d_ws, size_t ws_size,
                              hipStream_t stream) {
  const u16* x = (const u16*)d_in[0];
  const u16* wq = (const u16*)d_in[1];
  const u16* wk = (const u16*)d_in[2];
  const u16* wv = (const u16*)d_in[3];
  const u16* woA = (const u16*)d_in[4];
  const u16* wqkv = (const u16*)d_in[6];
  const u16* woB = (const u16*)d_in[7];
  const u16* gw = (const u16*)d_in[8];
  char* ws = (char*)d_ws;
  u16* Qa = (u16*)(ws);
  u16* KV = (u16*)(ws + (2u << 20));
  u16* QKVb = (u16*)(ws + (3u << 20));
  float* gate = (float*)(ws + (9u << 20));
  u16* Ocat = (u16*)(ws + (10u << 20));
  uint4* Wf = (uint4*)(ws + (14u << 20));
  u16* out = (u16*)d_out;

  k_proj<<<dim3(256), 512, 0, stream>>>(x, wq, wk, wv, wqkv, gw, woA, woB, Qa, KV, QKVb, gate, Wf);
  k_attn<<<dim3(S_LEN / 8), 512, 0, stream>>>(Qa, KV, QKVb, gate, Ocat);
  k_out<<<dim3(224), 512, 0, stream>>>((const uint4*)Ocat, Wf, out);
}
```

```cpp
#include <hip/hip_runtime.h>
#include <hip/hip_bf16.h>
#include <stdint.h>

typedef __attribute__((ext_vector_type(8))) short bf16x8;
typedef __attribute__((ext_vector_type(4))) short s16x4;
typedef __attribute__((ext_vector_type(16))) float f32x16;
typedef __attribute__((ext_vector_type(2))) __bf16 bf2;
typedef __attribute__((address_space(3))) s16x4* lds_s16x4_ptr;
typedef unsigned short u16;

#define DI __device__ __forceinline__
#define STAMP(i) do {} while (0)
#define STAMPG(k, i) do {} while (0)
#define EXP2(x) __builtin_amdgcn_exp2f(x)
#define MFMA32(a, b, c) __builtin_amdgcn_mfma_f32_32x32x16_bf16((a), (b), (c), 0, 0, 0)

constexpr int S_LEN = 2048;
constexpr int DM = 448;
constexpr int NH = 16;
constexpr int DH = 28;
constexpr int NB3 = 1344;
constexpr int OC = 896;

DI unsigned pack_bf16(float a, float b) {
  bf2 v; v[0] = (__bf16)a; v[1] = (__bf16)b;
  return __builtin_bit_cast(unsigned, v);
}
DI u16 to_bf16(float a) { __bf16 v = (__bf16)a; return __builtin_bit_cast(u16, v); }
DI float bf_to_f(u16 a) { return __uint_as_float(((unsigned)a) << 16); }

template <int V> struct IC { static constexpr int value = V; };
template <int I, int N, class F> DI void static_for(F&& f) {
  if constexpr (I < N) { f(IC<I>{}); static_for<I + 1, N>(f); }
}
template <int N> DI void wait_vm() { asm volatile("s_waitcnt vmcnt(%0)" ::"n"(N) : "memory"); }
DI void raw_barrier() { asm volatile("s_waitcnt lgkmcnt(0)" ::: "memory"); __builtin_amdgcn_s_barrier(); asm volatile("" ::: "memory"); }
DI void glds16(const void* g, void* l) {
  __builtin_amdgcn_global_load_lds((const __attribute__((address_space(1))) unsigned*)g,
                                   (__attribute__((address_space(3))) unsigned*)l, 16, 0, 0);
}

template <int CTRL> DI float dpp_f(float v) {
  return __int_as_float(__builtin_amdgcn_update_dpp(0, __float_as_int(v), CTRL, 0xf, 0xf, true));
}
DI void glds16a(const void* g, const void* l) {
  const unsigned la = __builtin_amdgcn_readfirstlane((unsigned)(size_t)l);
  unsigned keep;
  asm volatile("s_mov_b32 %0, m0\n\ts_mov_b32 m0, %2\n\ts_nop 0\n\tglobal_load_lds_dwordx4 %1, off\n\ts_mov_b32 m0, %0"
               : "=&s"(keep) : "v"(g), "s"(la) : "memory");
}

constexpr int G_AST = 72, G_BST = 160;
constexpr int G_STAGE = 128 * G_AST + 64 * G_BST;

struct G128Regs { uint4 a0, a1, b0, b1; };
DI G128Regs g128_load(const u16* as0, const u16* as1, const u16* bs0, const u16* bs1, int ld0, int ld1, int st) {
  G128Regs v;
  v.a0 = *(const uint4*)(as0 + st * 64);
  v.a1 = *(const uint4*)(as1 + st * 64);
  v.b0 = *(const uint4*)(bs0 + st * 64 * ld0);
  v.b1 = *(const uint4*)(bs1 + st * 64 * ld1);
  return v;
}
DI void g128_store(const G128Regs& v, u16* buf, int tid) {
  u16* bs = buf + 128 * G_AST;
  *(uint4*)(buf + (tid >> 3) * G_AST + (tid & 7) * 8) = v.a0;
  *(uint4*)(buf + ((tid >> 3) + 64) * G_AST + (tid & 7) * 8) = v.a1;
  *(uint4*)(bs + (tid >> 4) * G_BST + (tid & 15) * 8) = v.b0;
  *(uint4*)(bs + ((tid >> 4) + 32) * G_BST + (tid & 15) * 8) = v.b1;
}
DI void g128_compute(const u16* buf, int wm, int wn, int lane, f32x16& acc0, f32x16& acc1) {
  const int r = lane & 31, h = lane >> 5;
  const int trq = (lane & 15) >> 2, trp = lane & 3, trblk = (lane >> 4) & 1;
  const u16* bs = buf + 128 * G_AST;
#pragma unroll
  for (int ks = 0; ks < 4; ++ks) {
    const bf16x8 af = *(const bf16x8*)(buf + (wm * 32 + r) * G_AST + ks * 16 + h * 8);
    const u16* p = bs + (ks * 16 + 8 * h + trq) * G_BST + wn * 64 + 16 * trblk + 4 * trp;
    s16x4 lo0 = __builtin_amdgcn_ds_read_tr16_b64_v4i16((lds_s16x4_ptr)(p));
    s16x4 hi0 = __builtin_amdgcn_ds_read_tr16_b64_v4i16((lds_s16x4_ptr)(p + 4 * G_BST));
    s16x4 lo1 = __builtin_amdgcn_ds_read_tr16_b64_v4i16((lds_s16x4_ptr)(p + 32));
    s16x4 hi1 = __builtin_amdgcn_ds_read_tr16_b64_v4i16((lds_s16x4_ptr)(p + 4 * G_BST + 32));
    acc0 = MFMA32(af, __builtin_shufflevector(lo0, hi0, 0, 1, 2, 3, 4, 5, 6, 7), acc0);
    acc1 = MFMA32(af, __builtin_shufflevector(lo1, hi1, 0, 1, 2, 3, 4, 5, 6, 7), acc1);
  }
}

constexpr int SP_AST = 456, SP_BST = 96;
constexpr int P_SMEM = (64 * SP_AST + 448 * SP_BST) * 2;

__global__ __launch_bounds__(512) void k_proj(const u16* __restrict__ x, const u16* __restrict__ wq,
                                              const u16* __restrict__ wk, const u16* __restrict__ wv,
                                              const u16* __restrict__ wqkv, const u16* __restrict__ gw,
                                              const u16* __restrict__ woA, const u16* __restrict__ woB,
                                              u16* __restrict__ Qa, u16* __restrict__ KV,
                                              u16* __restrict__ QKVb, float* __restrict__ gate,
                                              uint4* __restrict__ Wf) {
  __shared__ __attribute__((aligned(16))) char smem[P_SMEM];
  const int bid = blockIdx.x;
  if (bid < 224) {
    const int xi = bid >> 3;
    const int n0 = (xi % 14) * 128, m0 = (bid & 7) * 256 + (xi / 14) * 128;
    const int tid = threadIdx.x, lane = tid & 63, wave = tid >> 6, wm = wave >> 1, wn = wave & 1;
    const u16* as0 = x + (m0 + (tid >> 3)) * DM + (tid & 7) * 8;
    const u16* as1 = as0 + 64 * DM;
    const int nb = n0 + (tid & 15) * 8;
    const int ld = (nb < DM) ? DM : NB3;
    const u16* bs0 = ((nb < DM) ? (wq + nb) : (wqkv + (nb - DM))) + (tid >> 4) * ld;
    const u16* bs1 = bs0 + 32 * ld;
    u16* buf0 = (u16*)smem;
    u16* buf1 = buf0 + G_STAGE;
    f32x16 acc0, acc1;
#pragma unroll
    for (int e = 0; e < 16; ++e) { acc0[e] = 0.f; acc1[e] = 0.f; }
    STAMPG(0, 0);
    G128Regs r0 = g128_load(as0, as1, bs0, bs1, ld, ld, 0);
    G128Regs r1 = g128_load(as0, as1, bs0, bs1, ld, ld, 1);
    g128_store(r0, buf0, tid);
    r0 = g128_load(as0, as1, bs0, bs1, ld, ld, 2);
    __syncthreads();
    STAMPG(0, 1);
#pragma nounroll
    for (int st = 0; st < 6; st += 2) {
      g128_compute(buf0, wm, wn, lane, acc0, acc1);
      g128_store(r1, buf1, tid);
      if (st + 3 < 7) r1 = g128_load(as0, as1, bs0, bs1, ld, ld, st + 3);
      __syncthreads();
      g128_compute(buf1, wm, wn, lane, acc0, acc1);
      g128_store(r0, buf0, tid);
      if (st + 4 < 7) r0 = g128_load(as0, as1, bs0, bs1, ld, ld, st + 4);
      __syncthreads();
    }
    g128_compute(buf0, wm, wn, lane, acc0, acc1);
    STAMPG(0, 2);
    const float qscale = 0.18898223650461362f * 1.4426950408889634f;
    const int r = lane & 31, h = lane >> 5, mb = m0 + wm * 32;
#pragma unroll
    for (int j = 0; j < 2; ++j) {
      const f32x16& a = j ? acc1 : acc0;
      const int nt = n0 + wn * 64 + j * 32, n = nt + r;
      if (nt < DM) {
#pragma unroll
        for (int e = 0; e < 16; ++e) Qa[(mb + (e & 3) + 8 * (e >> 2) + 4 * h) * DM + n] = to_bf16(a[e] * qscale);
      } else {
#pragma unroll
        for (int e = 0; e < 16; ++e) QKVb[(mb + (e & 3) + 8 * (e >> 2) + 4 * h) * NB3 + (n - DM)] = to_bf16(a[e]);
      }
    }
    STAMPG(0, 3);
    return;
  }
  const int m0 = (bid & 7) * 256 + ((bid - 224) >> 3) * 64;
  const int tid = threadIdx.x, lane = tid & 63, wave = tid >> 6;
  u16* as = (u16*)smem;
  u16* bs = as + 64 * SP_AST;
  uint4 ra[7]; uint2 rb0[7], rb1[7]; u16 rg[7];
#pragma unroll
  for (int i = 0; i < 7; ++i) {
    int c = tid + i * 512, row = c / 56, ch = c % 56;
    ra[i] = *(const uint4*)(x + (m0 + row) * DM + ch * 8);
  }
  const int nc = tid & 7;
  const u16* wsrc = (nc < 4) ? wk : wv;
  const int cb = (nc & 3) * 8;
#pragma unroll
  for (int i = 0; i < 7; ++i) {
    int k = (tid >> 3) + i * 64;
    rb0[i] = *(const uint2*)(wsrc + k * DH + cb);
    rb1[i] = *(const uint2*)(wsrc + k * DH + ((cb < 24) ? cb + 4 : cb));
    rg[i] = gw[k];
  }
#pragma unroll
  for (int i = 0; i < 7; ++i) {
    int c = tid + i * 512, row = c / 56, ch = c % 56;
    *(uint4*)(as + row * SP_AST + ch * 8) = ra[i];
  }
#pragma unroll
  for (int i = 0; i < 7; ++i) {
    int k = (tid >> 3) + i * 64;
    uint4 v = make_uint4(rb0[i].x, rb0[i].y, rb1[i].x, rb1[i].y);
    if (cb == 24) { v.z = (nc == 7) ? (unsigned)rg[i] : 0u; v.w = 0u; }
    *(uint4*)(bs + k * SP_BST + nc * 8) = v;
  }
  __syncthreads();
  if (wave >= 4) {
    const int f0 = (bid - 224) * 1568, t4 = tid - 256;
#pragma unroll
    for (int i = 0; i < 7; ++i) {
      const int f = f0 + i * 256 + t4;
      if (i < 6 || t4 < 32) {
        const int fl = f & 63, ks = (f >> 6) % 56, nt = (f >> 6) / 56;
        const int k = ks * 16 + (fl >> 5) * 8, n = nt * 32 + (fl & 31);
        const u16* w = (k < DM) ? (woA + k * DM + n) : (woB + (k - DM) * DM + n);
        unsigned t[8];
#pragma unroll
        for (int j = 0; j < 8; ++j) t[j] = w[j * DM];
        Wf[f] = make_uint4(t[0] | (t[1] << 16), t[2] | (t[3] << 16), t[4] | (t[5] << 16), t[6] | (t[7] << 16));
      }
    }
    return;
  }
  const int wm = wave >> 1, wn = wave & 1, r = lane & 31, h = lane >> 5;
  const int trq = (lane & 15) >> 2, trp = lane & 3, trblk = (lane >> 4) & 1;
  f32x16 a;
#pragma unroll
  for (int e = 0; e < 16; ++e) a[e] = 0.f;
#pragma unroll 4
  for (int ks = 0; ks < 28; ++ks) {
    bf16x8 af = *(const bf16x8*)(as + (wm * 32 + r) * SP_AST + ks * 16 + h * 8);
    const u16* p = bs + (ks * 16 + 8 * h + trq) * SP_BST + wn * 32 + 16 * trblk + 4 * trp;
    s16x4 lo = __builtin_amdgcn_ds_read_tr16_b64_v4i16((lds_s16x4_ptr)(p));
    s16x4 hi = __builtin_amdgcn_ds_read_tr16_b64_v4i16((lds_s16x4_ptr)(p + 4 * SP_BST));
    a = MFMA32(af, __builtin_shufflevector(lo, hi, 0, 1, 2, 3, 4, 5, 6, 7), a);
  }
  const int mb = m0 + wm * 32, d = r;
  if (wn == 0) {
    const int s = d >> 4, hh = (d >> 3) & 1, j = d & 7;
#pragma unroll
    for (int e = 0; e < 16; ++e) {
      int m = mb + (e & 3) + 8 * (e >> 2) + 4 * h;
      int tile = m >> 5, rr = m & 31;
      KV[tile * 2048 + s * 512 + (hh * 32 + rr) * 8 + j] = (d < DH) ? to_bf16(a[e]) : ((d < 31) ? (u16)0x3F80 : (u16)0);
    }
  } else {
#pragma unroll
    for (int e = 0; e < 16; ++e) {
      int m = mb + (e & 3) + 8 * (e >> 2) + 4 * h;
      int tile = m >> 5, kk = m & 31;
      int s = kk >> 4, k16 = kk & 15;
      int hh = (k16 >> 2) & 1, j = ((k16 >> 3) << 2) | (k16 & 3);
      u16 val = (d < DH) ? to_bf16(a[e]) : ((d == DH) ? (u16)0x3F80 : (u16)0);
      KV[tile * 2048 + 1024 + s * 512 + (hh * 32 + d) * 8 + j] = val;
      if (d == DH) gate[m] = 1.f / (1.f + __expf(-a[e]));
    }
  }
}

DI int ofrag(int pos, int c) {
  return (((pos >> 5) * 56 + (c >> 4)) * 64 + ((c >> 3) & 1) * 32 + (pos & 31)) * 8 + (c & 7);
}

__global__ __launch_bounds__(512) void k_out(const uint4* __restrict__ Af, const uint4* __restrict__ Bf,
                                             u16* __restrict__ out) {
  __shared__ float red[4 * 16 * 64];
  const int lane = threadIdx.x & 63, wave = threadIdx.x >> 6, w4 = wave & 3, kh = wave >> 2;
  const int xi = blockIdx.x >> 3;
  const int nt = (xi % 7) * 2 + (w4 & 1), mt = (blockIdx.x & 7) * 8 + (xi / 7) * 2 + (w4 >> 1);
  const uint4* ap = Af + (mt * 56 + kh * 28) * 64 + lane;
  const uint4* bp = Bf + (nt * 56 + kh * 28) * 64 + lane;
  f32x16 acc0, acc1;
#pragma unroll
  for (int e = 0; e < 16; ++e) { acc0[e] = 0.f; acc1[e] = 0.f; }
  {
    uint4 av[28], bv[28];
#pragma unroll
    for (int i = 0; i < 28; ++i) { av[i] = ap[i * 64]; bv[i] = bp[i * 64]; }
    __builtin_amdgcn_sched_barrier(0);
#pragma unroll
    for (int i = 0; i < 28; i += 2) {
      acc0 = MFMA32(__builtin_bit_cast(bf16x8, av[i]), __builtin_bit_cast(bf16x8, bv[i]), acc0);
      acc1 = MFMA32(__builtin_bit_cast(bf16x8, av[i + 1]), __builtin_bit_cast(bf16x8, bv[i + 1]), acc1);
    }
  }
  if (kh) {
#pragma unroll
    for (int e = 0; e < 16; ++e) red[(w4 * 16 + e) * 64 + lane] = acc0[e] + acc1[e];
  }
  __syncthreads();
  if (!kh) {
    const int r = lane & 31, h = lane >> 5;
#pragma unroll
    for (int e = 0; e < 16; ++e) {
      const int m = mt * 32 + (e & 3) + 8 * (e >> 2) + 4 * h;
      out[m * DM + nt * 32 + r] = to_bf16(acc0[e] + acc1[e] + red[(w4 * 16 + e) * 64 + lane]);
    }
  }
}

constexpr int RED_U4 = 4096;
constexpr int BQ_OFF = RED_U4;
constexpr int BK_OFF = BQ_OFF + 8 * 64;
constexpr int BK_STR = 130 * 8;
constexpr int FL_OFF = BK_OFF + 16 * 130;
constexpr int A_LDS_U4 = FL_OFF + 16;

struct KVT { uint4 k0, k1, v0, v1; };
DI KVT kv_load(const uint4* p) { KVT t; t.k0 = p[0]; t.k1 = p[64]; t.v0 = p[128]; t.v1 = p[192]; return t; }
#define BF8(x) __builtin_bit_cast(bf16x8, (x))

DI void softmax_pv(const f32x16& s, const uint4& vf0, const uint4& vf1, f32x16& o) {
  uint4 pa, pb;
  pa.x = pack_bf16(EXP2(s[0]), EXP2(s[1]));
  pa.y = pack_bf16(EXP2(s[2]), EXP2(s[3]));
  pa.z = pack_bf16(EXP2(s[4]), EXP2(s[5]));
  pa.w = pack_bf16(EXP2(s[6]), EXP2(s[7]));
  o = MFMA32(BF8(vf0), BF8(pa), o);
  pb.x = pack_bf16(EXP2(s[8]), EXP2(s[9]));
  pb.y = pack_bf16(EXP2(s[10]), EXP2(s[11]));
  pb.z = pack_bf16(EXP2(s[12]), EXP2(s[13]));
  pb.w = pack_bf16(EXP2(s[14]), EXP2(s[15]));
  o = MFMA32(BF8(vf1), BF8(pb), o);
}
#define INTERLEAVE() do { \
  __builtin_amdgcn_sched_group_barrier(0x008, 1, 0); __builtin_amdgcn_sched_group_barrier(0x400, 4, 0); \
  __builtin_amdgcn_sched_group_barrier(0x008, 1, 0); __builtin_amdgcn_sched_group_barrier(0x400, 4, 0); \
  __builtin_amdgcn_sched_group_barrier(0x002, 4, 0); __builtin_amdgcn_sched_group_barrier(0x008, 1, 0); \
  __builtin_amdgcn_sched_group_barrier(0x400, 8, 0); __builtin_amdgcn_sched_group_barrier(0x002, 4, 0); \
  __builtin_amdgcn_sched_group_barrier(0x008, 1, 0); } while (0)

__global__ __launch_bounds__(512) void k_attn(const u16* __restrict__ Qa, const u16* __restrict__ KV,
                                              const u16* __restrict__ QKVb, const float* __restrict__ gate,
                                              u16* __restrict__ Ocat) {
  __shared__ __attribute__((aligned(16))) uint4 lds[A_LDS_U4];
  const int tid = threadIdx.x, lane = tid & 63, wave = __builtin_amdgcn_readfirstlane(tid >> 6);
  const int rg = wave & 1, kq = wave >> 1;
  const int r = lane & 31, h = lane >> 5;
  const int xi = blockIdx.x >> 3;
  const int pbase = (blockIdx.x & 7) * 256 + (xi >> 1) * 16 + (xi & 1);
  const int rot = (xi & 7) * 2;
  const uint4* KVg = (const uint4*)KV;
  const uint4* kvq = KVg + kq * 16 * 256 + lane;
  __attribute__((address_space(3))) volatile unsigned* flag = (__attribute__((address_space(3))) volatile unsigned*)(lds + FL_OFF);
  STAMP(0);

  bf16x8 q0[2], q1[2];
#pragma unroll
  for (int b = 0; b < 2; ++b) {
    const int R = rg * 64 + b * 32 + r;
    const u16* qp = Qa + (pbase + 2 * (R >> 4)) * DM + (R & 15) * DH;
    uint2 qa = *(const uint2*)(qp + 8 * h), qb = *(const uint2*)(qp + 8 * h + 4);
    uint2 qc = *(const uint2*)(qp + 16 + 8 * h);
    uint2 qd = *(const uint2*)(qp + 20);
    if (h) qd = make_uint2(0, 0);
    q0[b] = BF8(make_uint4(qa.x, qa.y, qb.x, qb.y));
    q1[b] = BF8(make_uint4(qc.x, qc.y, qd.x, qd.y));
  }
  const uint4 k00 = KVg[lane], k01 = KVg[64 + lane];
  KVT R0 = kv_load(kvq + ((0 + rot) & 15) * 256), R1 = kv_load(kvq + ((1 + rot) & 15) * 256);
  KVT R2 = kv_load(kvq + ((2 + rot) & 15) * 256), R3 = kv_load(kvq + ((3 + rot) & 15) * 256);
  {
    const int l56 = lane < 56 ? lane : 55, l48 = lane < 48 ? lane : 47;
    glds16a((const char*)(QKVb + (pbase + 2 * wave) * NB3) + (unsigned)(l56 * 16), lds + BQ_OFF + wave * 64);
#pragma unroll
    for (int i = 0; i < 2; ++i) {
      int row = pbase - 14 + 2 * (wave * 2 + i);
      row = row < 0 ? (pbase & 1) : row;
      row = row > S_LEN - 1 ? S_LEN - 1 : row;
      const char* src = (const char*)(QKVb + row * NB3 + DM);
      glds16a(src + (unsigned)(lane * 16), lds + BK_OFF + (wave * 2 + i) * 130);
      glds16a(src + 1024 + (unsigned)(l48 * 16), lds + BK_OFF + (wave * 2 + i) * 130 + 64);
    }
  }
  if (tid == 0) { flag[0] = 0u; flag[1] = 0u; flag[2] = 0u; }
  raw_barrier();
  float g8[8];
#pragma unroll
  for (int i = 0; i < 8; ++i) g8[i] = gate[pbase + 2 * i];
#pragma unroll
  for (int i = 0; i < 8; ++i) asm volatile("" : "+s"(g8[i]));
  STAMP(1);

  f32x16 zero;
#pragma unroll
  for (int e = 0; e < 16; ++e) zero[e] = 0.f;
  float mref[2];
#pragma unroll
  for (int b = 0; b < 2; ++b) {
    f32x16 s = MFMA32(BF8(k00), q0[b], zero);
    s = MFMA32(BF8(k01), q1[b], s);
    float m = s[0];
#pragma unroll
    for (int e = 1; e < 16; ++e) m = fmaxf(m, s[e]);
    mref[b] = fmaxf(m, __shfl_xor(m, 32));
  }
  STAMP(2);

  f32x16 o0, o1;
  float* red = (float*)lds;
#pragma nounroll
  for (int attempt = 0; attempt < 2; ++attempt) {
    bf16x8 q1m[2];
#pragma unroll
    for (int b = 0; b < 2; ++b) {
      q1m[b] = q1[b];
      const float n0 = -mref[b];
      const u16 c0 = to_bf16(n0); const float n1 = n0 - bf_to_f(c0);
      const u16 c1 = to_bf16(n1); const float n2 = n1 - bf_to_f(c1);
      const u16 c2 = to_bf16(n2);
      if (h) { q1m[b][4] = (short)c0; q1m[b][5] = (short)c1; q1m[b][6] = (short)c2; }
    }
#pragma unroll
    for (int e = 0; e < 16; ++e) { o0[e] = 0.f; o1[e] = 0.f; }
    f32x16 sA = MFMA32(BF8(R0.k0), q0[0], zero), sB;
    sA = MFMA32(BF8(R0.k1), q1m[0], sA);
#define STEP(CUR, NXT, I) do { \
      __builtin_amdgcn_sched_barrier(0); \
      sB = MFMA32(BF8(CUR.k0), q0[1], zero); sB = MFMA32(BF8(CUR.k1), q1m[1], sB); \
      softmax_pv(sA, CUR.v0, CUR.v1, o0); INTERLEAVE(); \
      __builtin_amdgcn_sched_barrier(0); \
      sA = MFMA32(BF8(NXT.k0), q0[0], zero); sA = MFMA32(BF8(NXT.k1), q1m[0], sA); \
      softmax_pv(sB, CUR.v0, CUR.v1, o1); INTERLEAVE(); \
      __builtin_amdgcn_sched_barrier(0); \
      if ((I) + 4 < 16) CUR = kv_load(kvq + (((I) + 4 + rot) & 15) * 256); } while (0)
#pragma nounroll
    for (int i = 0; i < 16; i += 4) {
      STEP(R0, R1, i);
      STEP(R1, R2, i + 1);
      STEP(R2, R3, i + 2);
      STEP(R3, R0, i + 3);
      if (i == 4 && attempt == 0) {
        wait_vm<0>();
        if (lane == 0) __hip_atomic_fetch_add((__attribute__((address_space(3))) unsigned*)(flag + 1), 1u, __ATOMIC_RELAXED, __HIP_MEMORY_SCOPE_WORKGROUP);
      }
    }
    STAMP(3);
    if (attempt == 0) {
      while (flag[1] < 8u) __builtin_amdgcn_s_sleep(1);
      asm volatile("" ::: "memory");
      const int j = lane & 7, jc = (j < 7) ? j : 6;
#pragma nounroll
      for (;;) {
        unsigned it = 0;
        if (lane == 0) it = __hip_atomic_fetch_add((__attribute__((address_space(3))) unsigned*)(flag + 2), 1u, __ATOMIC_RELAXED, __HIP_MEMORY_SCOPE_WORKGROUP);
        it = __builtin_amdgcn_readfirstlane(it);
        if (it >= 16u) break;
        const int pair = it * 8 + (lane >> 3), pi = pair >> 4, bhead = pair & 15;
        const uint2* qrow = (const uint2*)((const u16*)(lds + BQ_OFF + pi * 64) + bhead * DH);
        const u16* kb = (const u16*)(lds + BK_OFF) + bhead * DH + (pi + 7) * BK_STR;
        const uint2* krow = (const uint2*)(kb - j * BK_STR);
        uint2 qv[7], kv[7], vv[8];
#pragma unroll
        for (int i = 0; i < 7; ++i) { qv[i] = qrow[i]; kv[i] = krow[i]; }
#pragma unroll
        for (int w = 0; w < 8; ++w) vv[w] = *(const uint2*)(kb - w * BK_STR + DM + 4 * jc);
        __builtin_amdgcn_sched_barrier(0);
        float lg = 0.f;
#pragma unroll
        for (int i = 0; i < 7; ++i) {
          lg = __builtin_amdgcn_fdot2_f32_bf16(__builtin_bit_cast(bf2, qv[i].x), __builtin_bit_cast(bf2, kv[i].x), lg, false);
          lg = __builtin_amdgcn_fdot2_f32_bf16(__builtin_bit_cast(bf2, qv[i].y), __builtin_bit_cast(bf2, kv[i].y), lg, false);
        }
        const bool valid = (pbase + 2 * pi - 2 * j >= 0);
        lg = valid ? lg * (0.18898223650461362f * 1.4426950408889634f) : -3.0e38f;
        float mx = lg;
        mx = fmaxf(mx, dpp_f<0xB1>(mx)); mx = fmaxf(mx, dpp_f<0x4E>(mx)); mx = fmaxf(mx, dpp_f<0x141>(mx));
        const float e = valid ? __builtin_amdgcn_exp2f(lg - mx) : 0.f;
        float sm = e;
        sm += dpp_f<0xB1>(sm); sm += dpp_f<0x4E>(sm); sm += dpp_f<0x141>(sm);
        float aw[8];
#pragma unroll
        for (int w = 0; w < 8; ++w) aw[w] = __shfl(e, (lane & 56) + w);
        float o4[4] = {0.f, 0.f, 0.f, 0.f};
#pragma unroll
        for (int w = 0; w < 8; ++w) {
          o4[0] += aw[w] * __uint_as_float(vv[w].x << 16);
          o4[1] += aw[w] * __uint_as_float(vv[w].x & 0xffff0000u);
          o4[2] += aw[w] * __uint_as_float(vv[w].y << 16);
          o4[3] += aw[w] * __uint_as_float(vv[w].y & 0xffff0000u);
        }
        float gB = 0.f;
#pragma unroll
        for (int i = 0; i < 8; ++i) gB = (i == pi) ? g8[i] : gB;
        const float sc = (1.f - gB) / sm;
        if (j < 7) {
          uint2 wv;
          wv.x = pack_bf16(o4[0] * sc, o4[1] * sc);
          wv.y = pack_bf16(o4[2] * sc, o4[3] * sc);
          *(uint2*)(Ocat + ofrag(pbase + 2 * pi, DM + bhead * DH + 4 * j)) = wv;
        }
      }
    }
    {
      float* dst = red + (kq * 4 + rg * 2) * 1024 + lane;
#pragma unroll
      for (int e = 0; e < 16; ++e) { dst[e * 64] = o0[e]; dst[1024 + e * 64] = o1[e]; }
    }
    if (attempt == 0 && h && !(o0[12] < 1.0e18f && o1[12] < 1.0e18f)) flag[0] = 1u;
    raw_barrier();
    if (attempt == 1 || flag[0] == 0u) {
      const int rb = wave & 3, dq = (wave >> 2) * 2;
      float l = 0.f, ov[8];
#pragma unroll
      for (int i = 0; i < 8; ++i) ov[i] = 0.f;
#pragma unroll
      for (int q = 0; q < 4; ++q) {
        const float* src = red + (q * 4 + rb) * 1024;
        l += src[12 * 64 + 32 + r];
#pragma unroll
        for (int i = 0; i < 8; ++i) ov[i] += src[(4 * dq + i) * 64 + lane];
      }
      const int R = rb * 32 + r;
      const int pos = pbase + 2 * (R >> 4), head = R & 15;
      float gA = 0.f;
#pragma unroll
      for (int i = 0; i < 8; ++i) gA = (i == (R >> 4)) ? g8[i] : gA;
      const float sc = gA / l;
#pragma unroll
      for (int g = 0; g < 2; ++g) {
        const int d = 8 * (dq + g) + 4 * h;
        if (d < DH) {
          uint2 w;
          w.x = pack_bf16(ov[4 * g] * sc, ov[4 * g + 1] * sc);
          w.y = pack_bf16(ov[4 * g + 2] * sc, ov[4 * g + 3] * sc);
          *(uint2*)(Ocat + ofrag(pos, head * DH + d)) = w;
        }
      }
      STAMP(4);
      break;
    }
    {
      float m[2] = {-3.0e38f, -3.0e38f};
#pragma nounroll
      for (int t = 0; t < 16; ++t) {
        const uint4 k0 = kvq[t * 256], k1 = kvq[t * 256 + 64];
#pragma unroll
        for (int b = 0; b < 2; ++b) {
          f32x16 s = MFMA32(BF8(k0), q0[b], zero);
          s = MFMA32(BF8(k1), q1[b], s);
#pragma unroll
          for (int e = 0; e < 16; ++e) m[b] = fmaxf(m[b], s[e]);
        }
      }
      m[0] = fmaxf(m[0], __shfl_xor(m[0], 32));
      m[1] = fmaxf(m[1], __shfl_xor(m[1], 32));
      raw_barrier();
      red[(wave * 2) * 64 + lane] = m[0];
      red[(wave * 2 + 1) * 64 + lane] = m[1];
      raw_barrier();
#pragma unroll
      for (int b = 0; b < 2; ++b) {
        float mm = red[(rg * 2 + b) * 64 + lane];
#pragma unroll
        for (int q = 1; q < 4; ++q) mm = fmaxf(mm, red[((q * 2 + rg) * 2 + b) * 64 + lane]);
        mref[b] = mm;
      }
      raw_barrier();
      R0 = kv_load(kvq + ((0 + rot) & 15) * 256); R1 = kv_load(kvq + ((1 + rot) & 15) * 256);
      R2 = kv_load(kvq + ((2 + rot) & 15) * 256); R3 = kv_load(kvq + ((3 + rot) & 15) * 256);
    }
  }
}

extern "C" void kernel_launch(void* const* d_in, const int* in_sizes, int n_in,
                              void* d_out, int out_size, void* d_ws, size_t ws_size,
                              hipStream_t stream) {
  const u16* x = (const u16*)d_in[0];
  const u16* wq = (const u16*)d_in[1];
  const u16* wk = (const u16*)d_in[2];
  const u16* wv = (const u16*)d_in[3];
  const u16* woA = (const u16*)d_in[4];
  const u16* wqkv = (const u16*)d_in[6];
  const u16* woB = (const u16*)d_in[7];
  const u16* gw = (const u16*)d_in[8];
  char* ws = (char*)d_ws;
  u16* Qa = (u16*)(ws);
  u16* KV = (u16*)(ws + (2u << 20));
  u16* QKVb = (u16*)(ws + (3u << 20));
  float* gate = (float*)(ws + (9u << 20));
  u16* Ocat = (u16*)(ws + (10u << 20));
  uint4* Wf = (uint4*)(ws + (14u << 20));
  u16* out = (u16*)d_out;

  k_proj<<<dim3(256), 512, 0, stream>>>(x, wq, wk, wv, wqkv, gw, woA, woB, Qa, KV, QKVb, gate, Wf);
  k_attn<<<dim3(S_LEN / 8), 512, 0, stream>>>(Qa, KV, QKVb, gate, Ocat);
  k_out<<<dim3(224), 512, 0, stream>>>((const uint4*)Ocat, Wf, out);
}
```

```cpp
#include <hip/hip_runtime.h>
#include <hip/hip_bf16.h>
#include <stdint.h>


typedef __attribute__((ext_vector_type(8))) short bf16x8;
typedef __attribute__((ext_vector_type(4))) short s16x4;
typedef __attribute__((ext_vector_type(16))) float f32x16;
typedef __attribute__((ext_vector_type(2))) __bf16 bf2;
typedef __attribute__((address_space(3))) s16x4* lds_s16x4_ptr;
typedef unsigned short u16;

#define DI __device__ __forceinline__
#define EXP2(x) __builtin_amdgcn_exp2f(x)
#define MFMA32(a, b, c) __builtin_amdgcn_mfma_f32_32x32x16_bf16((a), (b), (c), 0, 0, 0)

constexpr int S_LEN = 2048;
constexpr int DM = 448;
constexpr int NH = 16;
constexpr int DH = 28;
constexpr int NB3 = 1344;
constexpr int OC = 896;

DI unsigned pack_bf16(float a, float b) {
  bf2 v; v[0] = (__bf16)a; v[1] = (__bf16)b;
  return __builtin_bit_cast(unsigned, v);
}
DI u16 to_bf16(float a) { __bf16 v = (__bf16)a; return __builtin_bit_cast(u16, v); }
DI float bf_to_f(u16 a) { return __uint_as_float(((unsigned)a) << 16); }

template <int N> DI void wait_vm() { asm volatile("s_waitcnt vmcnt(%0)" ::"n"(N) : "memory"); }
DI void raw_barrier() { asm volatile("s_waitcnt lgkmcnt(0)" ::: "memory"); __builtin_amdgcn_s_barrier(); asm volatile("" ::: "memory"); }
template <int CTRL> DI float dpp_f(float v) {
  return __int_as_float(__builtin_amdgcn_update_dpp(0, __float_as_int(v), CTRL, 0xf, 0xf, true));
}
DI void glds16a(const void* g, const void* l) {
  const unsigned la = __builtin_amdgcn_readfirstlane((unsigned)(size_t)l);
  unsigned keep;
  asm volatile("s_mov_b32 %0, m0\n\ts_mov_b32 m0, %2\n\ts_nop 0\n\tglobal_load_lds_dwordx4 %1, off\n\ts_mov_b32 m0, %0"
               : "=&s"(keep) : "v"(g), "s"(la) : "memory");
}

constexpr int G_AST = 72, G_BST = 160;
constexpr int G_STAGE = 128 * G_AST + 64 * G_BST;

struct G128Regs { uint4 a0, a1, b0, b1; };
DI G128Regs g128_load(const u16* as0, const u16* as1, const u16* bs0, const u16* bs1, int ld0, int ld1, int st) {
  G128Regs v;
  v.a0 = *(const uint4*)(as0 + st * 64);
  v.a1 = *(const uint4*)(as1 + st * 64);
  v.b0 = *(const uint4*)(bs0 + st * 64 * ld0);
  v.b1 = *(const uint4*)(bs1 + st * 64 * ld1);
  return v;
}
DI void g128_store(const G128Regs& v, u16* buf, int tid) {
  u16* bs = buf + 128 * G_AST;
  *(uint4*)(buf + (tid >> 3) * G_AST + (tid & 7) * 8) = v.a0;
  *(uint4*)(buf + ((tid >> 3) + 64) * G_AST + (tid & 7) * 8) = v.a1;
  *(uint4*)(bs + (tid >> 4) * G_BST + (tid & 15) * 8) = v.b0;
  *(uint4*)(bs + ((tid >> 4) + 32) * G_BST + (tid & 15) * 8) = v.b1;
}
DI void g128_compute(const u16* buf, int wm, int wn, int lane, f32x16& acc0, f32x16& acc1) {
  const int r = lane & 31, h = lane >> 5;
  const int trq = (lane & 15) >> 2, trp = lane & 3, trblk = (lane >> 4) & 1;
  const u16* bs = buf + 128 * G_AST;
#pragma unroll
  for (int ks = 0; ks < 4; ++ks) {
    const bf16x8 af = *(const bf16x8*)(buf + (wm * 32 + r) * G_AST + ks * 16 + h * 8);
    const u16* p = bs + (ks * 16 + 8 * h + trq) * G_BST + wn * 64 + 16 * trblk + 4 * trp;
    s16x4 lo0 = __builtin_amdgcn_ds_read_tr16_b64_v4i16((lds_s16x4_ptr)(p));
    s16x4 hi0 = __builtin_amdgcn_ds_read_tr16_b64_v4i16((lds_s16x4_ptr)(p + 4 * G_BST));
    s16x4 lo1 = __builtin_amdgcn_ds_read_tr16_b64_v4i16((lds_s16x4_ptr)(p + 32));
    s16x4 hi1 = __builtin_amdgcn_ds_read_tr16_b64_v4i16((lds_s16x4_ptr)(p + 4 * G_BST + 32));
    acc0 = MFMA32(af, __builtin_shufflevector(lo0, hi0, 0, 1, 2, 3, 4, 5, 6, 7), acc0);
    acc1 = MFMA32(af, __builtin_shufflevector(lo1, hi1, 0, 1, 2, 3, 4, 5, 6, 7), acc1);
  }
}

constexpr int SP_AST = 456, SP_BST = 96;
constexpr int P_SMEM = (64 * SP_AST + 448 * SP_BST) * 2;

__global__ __launch_bounds__(512) void k_proj(const u16* __restrict__ x, const u16* __restrict__ wq,
                                              const u16* __restrict__ wk, const u16* __restrict__ wv,
                                              const u16* __restrict__ wqkv, const u16* __restrict__ gw,
                                              const u16* __restrict__ woA, const u16* __restrict__ woB,
                                              u16* __restrict__ Qa, u16* __restrict__ KV,
                                              u16* __restrict__ QKVb, float* __restrict__ gate,
                                              uint4* __restrict__ Wf) {
  __shared__ __attribute__((aligned(16))) char smem[P_SMEM];
  const int bid = blockIdx.x;
  if (bid < 224) {
    const int xi = bid >> 3;
    const int n0 = (xi % 14) * 128, m0 = (bid & 7) * 256 + (xi / 14) * 128;
    const int tid = threadIdx.x, lane = tid & 63, wave = tid >> 6, wm = wave >> 1, wn = wave & 1;
    const u16* as0 = x + (m0 + (tid >> 3)) * DM + (tid & 7) * 8;
    const u16* as1 = as0 + 64 * DM;
    const int nb = n0 + (tid & 15) * 8;
    const int ld = (nb < DM) ? DM : NB3;
    const u16* bs0 = ((nb < DM) ? (wq + nb) : (wqkv + (nb - DM))) + (tid >> 4) * ld;
    const u16* bs1 = bs0 + 32 * ld;
    u16* buf0 = (u16*)smem;
    u16* buf1 = buf0 + G_STAGE;
    f32x16 acc0, acc1;
#pragma unroll
    for (int e = 0; e < 16; ++e) { acc0[e] = 0.f; acc1[e] = 0.f; }
    G128Regs r0 = g128_load(as0, as1, bs0, bs1, ld, ld, 0);
    G128Regs r1 = g128_load(as0, as1, bs0, bs1, ld, ld, 1);
    g128_store(r0, buf0, tid);
    r0 = g128_load(as0, as1, bs0, bs1, ld, ld, 2);
    __syncthreads();
#pragma nounroll
    for (int st = 0; st < 6; st += 2) {
      g128_compute(buf0, wm, wn, lane, acc0, acc1);
      g128_store(r1, buf1, tid);
      if (st + 3 < 7) r1 = g128_load(as0, as1, bs0, bs1, ld, ld, st + 3);
      __syncthreads();
      g128_compute(buf1, wm, wn, lane, acc0, acc1);
      g128_store(r0, buf0, tid);
      if (st + 4 < 7) r0 = g128_load(as0, as1, bs0, bs1, ld, ld, st + 4);
      __syncthreads();
    }
    g128_compute(buf0, wm, wn, lane, acc0, acc1);
    const float qscale = 0.18898223650461362f * 1.4426950408889634f;
    const int r = lane & 31, h = lane >> 5, mb = m0 + wm * 32;
#pragma unroll
    for (int j = 0; j < 2; ++j) {
      const f32x16& a = j ? acc1 : acc0;
      const int nt = n0 + wn * 64 + j * 32, n = nt + r;
      if (nt < DM) {
#pragma unroll
        for (int e = 0; e < 16; ++e) Qa[(mb + (e & 3) + 8 * (e >> 2) + 4 * h) * DM + n] = to_bf16(a[e] * qscale);
      } else {
#pragma unroll
        for (int e = 0; e < 16; ++e) QKVb[(mb + (e & 3) + 8 * (e >> 2) + 4 * h) * NB3 + (n - DM)] = to_bf16(a[e]);
      }
    }
    return;
  }
  const int m0 = (bid & 7) * 256 + ((bid - 224) >> 3) * 64;
  const int tid = threadIdx.x, lane = tid & 63, wave = tid >> 6;
  u16* as = (u16*)smem;
  u16* bs = as + 64 * SP_AST;
  uint4 ra[7]; uint2 rb0[7], rb1[7]; u16 rg[7];
#pragma unroll
  for (int i = 0; i < 7; ++i) {
    int c = tid + i * 512, row = c / 56, ch = c % 56;
    ra[i] = *(const uint4*)(x + (m0 + row) * DM + ch * 8);
  }
  const int nc = tid & 7;
  const u16* wsrc = (nc < 4) ? wk : wv;
  const int cb = (nc & 3) * 8;
#pragma unroll
  for (int i = 0; i < 7; ++i) {
    int k = (tid >> 3) + i * 64;
    rb0[i] = *(const uint2*)(wsrc + k * DH + cb);
    rb1[i] = *(const uint2*)(wsrc + k * DH + ((cb < 24) ? cb + 4 : cb));
    rg[i] = gw[k];
  }
#pragma unroll
  for (int i = 0; i < 7; ++i) {
    int c = tid + i * 512, row = c / 56, ch = c % 56;
    *(uint4*)(as + row * SP_AST + ch * 8) = ra[i];
  }
#pragma unroll
  for (int i = 0; i < 7; ++i) {
    int k = (tid >> 3) + i * 64;
    uint4 v = make_uint4(rb0[i].x, rb0[i].y, rb1[i].x, rb1[i].y);
    if (cb == 24) { v.z = (nc == 7) ? (unsigned)rg[i] : 0u; v.w = 0u; }
    *(uint4*)(bs + k * SP_BST + nc * 8) = v;
  }
  __syncthreads();
  if (wave >= 4) {
    const int f0 = (bid - 224) * 1568, t4 = tid - 256;
#pragma unroll
    for (int i = 0; i < 7; ++i) {
      const int f = f0 + i * 256 + t4;
      if (i < 6 || t4 < 32) {
        const int fl = f & 63, ks = (f >> 6) % 56, nt = (f >> 6) / 56;
        const int k = ks * 16 + (fl >> 5) * 8, n = nt * 32 + (fl & 31);
        const u16* w = (k < DM) ? (woA + k * DM + n) : (woB + (k - DM) * DM + n);
        unsigned t[8];
#pragma unroll
        for (int j = 0; j < 8; ++j) t[j] = w[j * DM];
        Wf[f] = make_uint4(t[0] | (t[1] << 16), t[2] | (t[3] << 16), t[4] | (t[5] << 16), t[6] | (t[7] << 16));
      }
    }
    return;
  }
  const int wm = wave >> 1, wn = wave & 1, r = lane & 31, h = lane >> 5;
  const int trq = (lane & 15) >> 2, trp = lane & 3, trblk = (lane >> 4) & 1;
  f32x16 a;
#pragma unroll
  for (int e = 0; e < 16; ++e) a[e] = 0.f;
#pragma unroll 4
  for (int ks = 0; ks < 28; ++ks) {
    bf16x8 af = *(const bf16x8*)(as + (wm * 32 + r) * SP_AST + ks * 16 + h * 8);
    const u16* p = bs + (ks * 16 + 8 * h + trq) * SP_BST + wn * 32 + 16 * trblk + 4 * trp;
    s16x4 lo = __builtin_amdgcn_ds_read_tr16_b64_v4i16((lds_s16x4_ptr)(p));
    s16x4 hi = __builtin_amdgcn_ds_read_tr16_b64_v4i16((lds_s16x4_ptr)(p + 4 * SP_BST));
    a = MFMA32(af, __builtin_shufflevector(lo, hi, 0, 1, 2, 3, 4, 5, 6, 7), a);
  }
  const int mb = m0 + wm * 32, d = r;
  if (wn == 0) {
    const int s = d >> 4, hh = (d >> 3) & 1, j = d & 7;
#pragma unroll
    for (int e = 0; e < 16; ++e) {
      int m = mb + (e & 3) + 8 * (e >> 2) + 4 * h;
      int tile = m >> 5, rr = m & 31;
      KV[tile * 2048 + s * 512 + (hh * 32 + rr) * 8 + j] = (d < DH) ? to_bf16(a[e]) : ((d < 31) ? (u16)0x3F80 : (u16)0);
    }
  } else {
#pragma unroll
    for (int e = 0; e < 16; ++e) {
      int m = mb + (e & 3) + 8 * (e >> 2) + 4 * h;
      int tile = m >> 5, kk = m & 31;
      int s = kk >> 4, k16 = kk & 15;
      int hh = (k16 >> 2) & 1, j = ((k16 >> 3) << 2) | (k16 & 3);
      u16 val = (d < DH) ? to_bf16(a[e]) : ((d == DH) ? (u16)0x3F80 : (u16)0);
      KV[tile * 2048 + 1024 + s * 512 + (hh * 32 + d) * 8 + j] = val;
      if (d == DH) gate[m] = 1.f / (1.f + __expf(-a[e]));
    }
  }
}

DI int ofrag(int pos, int c) {
  return (((pos >> 5) * 56 + (c >> 4)) * 64 + ((c >> 3) & 1) * 32 + (pos & 31)) * 8 + (c & 7);
}

__global__ __launch_bounds__(512) void k_out(const uint4* __restrict__ Af, const uint4* __restrict__ Bf,
                                             u16* __restrict__ out) {
  __shared__ float red[4 * 16 * 64];
  const int lane = threadIdx.x & 63, wave = threadIdx.x >> 6, w4 = wave & 3, kh = wave >> 2;
  const int xi = blockIdx.x >> 3;
  const int nt = (xi % 7) * 2 + (w4 & 1), mt = (blockIdx.x & 7) * 8 + (xi / 7) * 2 + (w4 >> 1);
  const uint4* ap = Af + (mt * 56 + kh * 28) * 64 + lane;
  const uint4* bp = Bf + (nt * 56 + kh * 28) * 64 + lane;
  f32x16 acc0, acc1;
#pragma unroll
  for (int e = 0; e < 16; ++e) { acc0[e] = 0.f; acc1[e] = 0.f; }
  {
    uint4 av[28], bv[28];
#pragma unroll
    for (int i = 0; i < 28; ++i) { av[i] = ap[i * 64]; bv[i] = bp[i * 64]; }
    __builtin_amdgcn_sched_barrier(0);
#pragma unroll
    for (int i = 0; i < 28; i += 2) {
      acc0 = MFMA32(__builtin_bit_cast(bf16x8, av[i]), __builtin_bit_cast(bf16x8, bv[i]), acc0);
      acc1 = MFMA32(__builtin_bit_cast(bf16x8, av[i + 1]), __builtin_bit_cast(bf16x8, bv[i + 1]), acc1);
    }
  }
  if (kh) {
#pragma unroll
    for (int e = 0; e < 16; ++e) red[(w4 * 16 + e) * 64 + lane] = acc0[e] + acc1[e];
  }
  __syncthreads();
  if (!kh) {
    const int r = lane & 31, h = lane >> 5;
#pragma unroll
    for (int e = 0; e < 16; ++e) {
      const int m = mt * 32 + (e & 3) + 8 * (e >> 2) + 4 * h;
      out[m * DM + nt * 32 + r] = to_bf16(acc0[e] + acc1[e] + red[(w4 * 16 + e) * 64 + lane]);
    }
  }
}

constexpr int RED_U4 = 4096;
constexpr int BQ_OFF = RED_U4;
constexpr int BK_OFF = BQ_OFF + 8 * 64;
constexpr int BK_STR = 130 * 8;
constexpr int FL_OFF = BK_OFF + 16 * 130;
constexpr int A_LDS_U4 = FL_OFF + 16;

struct KVT { uint4 k0, k1, v0, v1; };
DI KVT kv_load(const uint4* p) { KVT t; t.k0 = p[0]; t.k1 = p[64]; t.v0 = p[128]; t.v1 = p[192]; return t; }
#define BF8(x) __builtin_bit_cast(bf16x8, (x))

DI void softmax_pv(const f32x16& s, const uint4& vf0, const uint4& vf1, f32x16& o) {
  uint4 pa, pb;
  pa.x = pack_bf16(EXP2(s[0]), EXP2(s[1]));
  pa.y = pack_bf16(EXP2(s[2]), EXP2(s[3]));
  pa.z = pack_bf16(EXP2(s[4]), EXP2(s[5]));
  pa.w = pack_bf16(EXP2(s[6]), EXP2(s[7]));
  o = MFMA32(BF8(vf0), BF8(pa), o);
  pb.x = pack_bf16(EXP2(s[8]), EXP2(s[9]));
  pb.y = pack_bf16(EXP2(s[10]), EXP2(s[11]));
  pb.z = pack_bf16(EXP2(s[12]), EXP2(s[13]));
  pb.w = pack_bf16(EXP2(s[14]), EXP2(s[15]));
  o = MFMA32(BF8(vf1), BF8(pb), o);
}
#define INTERLEAVE() do { \
  __builtin_amdgcn_sched_group_barrier(0x008, 1, 0); __builtin_amdgcn_sched_group_barrier(0x400, 4, 0); \
  __builtin_amdgcn_sched_group_barrier(0x008, 1, 0); __builtin_amdgcn_sched_group_barrier(0x400, 4, 0); \
  __builtin_amdgcn_sched_group_barrier(0x002, 4, 0); __builtin_amdgcn_sched_group_barrier(0x008, 1, 0); \
  __builtin_amdgcn_sched_group_barrier(0x400, 8, 0); __builtin_amdgcn_sched_group_barrier(0x002, 4, 0); \
  __builtin_amdgcn_sched_group_barrier(0x008, 1, 0); } while (0)

__global__ __launch_bounds__(512) void k_attn(const u16* __restrict__ Qa, const u16* __restrict__ KV,
                                              const u16* __restrict__ QKVb, const float* __restrict__ gate,
                                              u16* __restrict__ Ocat) {
  __shared__ __attribute__((aligned(16))) uint4 lds[A_LDS_U4];
  const int tid = threadIdx.x, lane = tid & 63, wave = __builtin_amdgcn_readfirstlane(tid >> 6);
  const int rg = wave & 1, kq = wave >> 1;
  const int r = lane & 31, h = lane >> 5;
  const int xi = blockIdx.x >> 3;
  const int pbase = (blockIdx.x & 7) * 256 + (xi >> 1) * 16 + (xi & 1);
  const int rot = (xi & 7) * 2;
  const uint4* KVg = (const uint4*)KV;
  const uint4* kvq = KVg + kq * 16 * 256 + lane;
  __attribute__((address_space(3))) volatile unsigned* flag = (__attribute__((address_space(3))) volatile unsigned*)(lds + FL_OFF);

  bf16x8 q0[2], q1[2];
#pragma unroll
  for (int b = 0; b < 2; ++b) {
    const int R = rg * 64 + b * 32 + r;
    const u16* qp = Qa + (pbase + 2 * (R >> 4)) * DM + (R & 15) * DH;
    uint2 qa = *(const uint2*)(qp + 8 * h), qb = *(const uint2*)(qp + 8 * h + 4);
    uint2 qc = *(const uint2*)(qp + 16 + 8 * h);
    uint2 qd = *(const uint2*)(qp + 20);
    if (h) qd = make_uint2(0, 0);
    q0[b] = BF8(make_uint4(qa.x, qa.y, qb.x, qb.y));
    q1[b] = BF8(make_uint4(qc.x, qc.y, qd.x, qd.y));
  }
  const uint4 k00 = KVg[lane], k01 = KVg[64 + lane];
  KVT R0 = kv_load(kvq + ((0 + rot) & 15) * 256), R1 = kv_load(kvq + ((1 + rot) & 15) * 256);
  KVT R2 = kv_load(kvq + ((2 + rot) & 15) * 256), R3 = kv_load(kvq + ((3 + rot) & 15) * 256);
  {
    const int l56 = lane < 56 ? lane : 55, l48 = lane < 48 ? lane : 47;
    glds16a((const char*)(QKVb + (pbase + 2 * wave) * NB3) + (unsigned)(l56 * 16), lds + BQ_OFF + wave * 64);
#pragma unroll
    for (int i = 0; i < 2; ++i) {
      int row = pbase - 14 + 2 * (wave * 2 + i);
      row = row < 0 ? (pbase & 1) : row;
      row = row > S_LEN - 1 ? S_LEN - 1 : row;
      const char* src = (const char*)(QKVb + row * NB3 + DM);
      glds16a(src + (unsigned)(lane * 16), lds + BK_OFF + (wave * 2 + i) * 130);
      glds16a(src + 1024 + (unsigned)(l48 * 16), lds + BK_OFF + (wave * 2 + i) * 130 + 64);
    }
  }
  if (tid == 0) { flag[0] = 0u; flag[1] = 0u; flag[2] = 0u; }
  raw_barrier();
  float g8[8];
#pragma unroll
  for (int i = 0; i < 8; ++i) g8[i] = gate[pbase + 2 * i];
#pragma unroll
  for (int i = 0; i < 8; ++i) asm volatile("" : "+s"(g8[i]));

  f32x16 zero;
#pragma unroll
  for (int e = 0; e < 16; ++e) zero[e] = 0.f;
  float mref[2];
#pragma unroll
  for (int b = 0; b < 2; ++b) {
    f32x16 s = MFMA32(BF8(k00), q0[b], zero);
    s = MFMA32(BF8(k01), q1[b], s);
    float m = s[0];
#pragma unroll
    for (int e = 1; e < 16; ++e) m = fmaxf(m, s[e]);
    mref[b] = fmaxf(m, __shfl_xor(m, 32));
  }

  f32x16 o0, o1;
  float* red = (float*)lds;
#pragma nounroll
  for (int attempt = 0; attempt < 2; ++attempt) {
    bf16x8 q1m[2];
#pragma unroll
    for (int b = 0; b < 2; ++b) {
      q1m[b] = q1[b];
      const float n0 = -mref[b];
      const u16 c0 = to_bf16(n0); const float n1 = n0 - bf_to_f(c0);
      const u16 c1 = to_bf16(n1); const float n2 = n1 - bf_to_f(c1);
      const u16 c2 = to_bf16(n2);
      if (h) { q1m[b][4] = (short)c0; q1m[b][5] = (short)c1; q1m[b][6] = (short)c2; }
    }
#pragma unroll
    for (int e = 0; e < 16; ++e) { o0[e] = 0.f; o1[e] = 0.f; }
    f32x16 sA = MFMA32(BF8(R0.k0), q0[0], zero), sB;
    sA = MFMA32(BF8(R0.k1), q1m[0], sA);
#define STEP(CUR, NXT, I) do { \
      __builtin_amdgcn_sched_barrier(0); \
      sB = MFMA32(BF8(CUR.k0), q0[1], zero); sB = MFMA32(BF8(CUR.k1), q1m[1], sB); \
      softmax_pv(sA, CUR.v0, CUR.v1, o0); INTERLEAVE(); \
      __builtin_amdgcn_sched_barrier(0); \
      sA = MFMA32(BF8(NXT.k0), q0[0], zero); sA = MFMA32(BF8(NXT.k1), q1m[0], sA); \
      softmax_pv(sB, CUR.v0, CUR.v1, o1); INTERLEAVE(); \
      __builtin_amdgcn_sched_barrier(0); \
      if ((I) + 4 < 16) CUR = kv_load(kvq + (((I) + 4 + rot) & 15) * 256); } while (0)
#pragma nounroll
    for (int i = 0; i < 16; i += 4) {
      STEP(R0, R1, i);
      STEP(R1, R2, i + 1);
      STEP(R2, R3, i + 2);
      STEP(R3, R0, i + 3);
      if (i == 4 && attempt == 0) {
        wait_vm<0>();
        if (lane == 0) __hip_atomic_fetch_add((__attribute__((address_space(3))) unsigned*)(flag + 1), 1u, __ATOMIC_RELAXED, __HIP_MEMORY_SCOPE_WORKGROUP);
      }
    }
    if (attempt == 0) {
      while (flag[1] < 8u) __builtin_amdgcn_s_sleep(1);
      asm volatile("" ::: "memory");
      const int j = lane & 7, jc = (j < 7) ? j : 6;
#pragma nounroll
      for (;;) {
        unsigned it = 0;
        if (lane == 0) it = __hip_atomic_fetch_add((__attribute__((address_space(3))) unsigned*)(flag + 2), 1u, __ATOMIC_RELAXED, __HIP_MEMORY_SCOPE_WORKGROUP);
        it = __builtin_amdgcn_readfirstlane(it);
        if (it >= 16u) break;
        const int pair = it * 8 + (lane >> 3), pi = pair >> 4, bhead = pair & 15;
        const uint2* qrow = (const uint2*)((const u16*)(lds + BQ_OFF + pi * 64) + bhead * DH);
        const u16* kb = (const u16*)(lds + BK_OFF) + bhead * DH + (pi + 7) * BK_STR;
        const uint2* krow = (const uint2*)(kb - j * BK_STR);
        uint2 qv[7], kv[7], vv[8];
#pragma unroll
        for (int i = 0; i < 7; ++i) { qv[i] = qrow[i]; kv[i] = krow[i]; }
#pragma unroll
        for (int w = 0; w < 8; ++w) vv[w] = *(const uint2*)(kb - w * BK_STR + DM + 4 * jc);
        __builtin_amdgcn_sched_barrier(0);
        float lg = 0.f;
#pragma unroll
        for (int i = 0; i < 7; ++i) {
          lg = __builtin_amdgcn_fdot2_f32_bf16(__builtin_bit_cast(bf2, qv[i].x), __builtin_bit_cast(bf2, kv[i].x), lg, false);
          lg = __builtin_amdgcn_fdot2_f32_bf16(__builtin_bit_cast(bf2, qv[i].y), __builtin_bit_cast(bf2, kv[i].y), lg, false);
        }
        const bool valid = (pbase + 2 * pi - 2 * j >= 0);
        lg = valid ? lg * (0.18898223650461362f * 1.4426950408889634f) : -3.0e38f;
        float mx = lg;
        mx = fmaxf(mx, dpp_f<0xB1>(mx)); mx = fmaxf(mx, dpp_f<0x4E>(mx)); mx = fmaxf(mx, dpp_f<0x141>(mx));
        const float e = valid ? __builtin_amdgcn_exp2f(lg - mx) : 0.f;
        float sm = e;
        sm += dpp_f<0xB1>(sm); sm += dpp_f<0x4E>(sm); sm += dpp_f<0x141>(sm);
        float aw[8];
#pragma unroll
        for (int w = 0; w < 8; ++w) aw[w] = __shfl(e, (lane & 56) + w);
        float o4[4] = {0.f, 0.f, 0.f, 0.f};
#pragma unroll
        for (int w = 0; w < 8; ++w) {
          o4[0] += aw[w] * __uint_as_float(vv[w].x << 16);
          o4[1] += aw[w] * __uint_as_float(vv[w].x & 0xffff0000u);
          o4[2] += aw[w] * __uint_as_float(vv[w].y << 16);
          o4[3] += aw[w] * __uint_as_float(vv[w].y & 0xffff0000u);
        }
        float gB = 0.f;
#pragma unroll
        for (int i = 0; i < 8; ++i) gB = (i == pi) ? g8[i] : gB;
        const float sc = (1.f - gB) / sm;
        if (j < 7) {
          uint2 wv;
          wv.x = pack_bf16(o4[0] * sc, o4[1] * sc);
          wv.y = pack_bf16(o4[2] * sc, o4[3] * sc);
          *(uint2*)(Ocat + ofrag(pbase + 2 * pi, DM + bhead * DH + 4 * j)) = wv;
        }
      }
    }
    {
      float* dst = red + (kq * 4 + rg * 2) * 1024 + lane;
#pragma unroll
      for (int e = 0; e < 16; ++e) { dst[e * 64] = o0[e]; dst[1024 + e * 64] = o1[e]; }
    }
    if (attempt == 0 && h && !(o0[12] < 1.0e18f && o1[12] < 1.0e18f)) flag[0] = 1u;
    raw_barrier();
    if (attempt == 1 || flag[0] == 0u) {
      const int rb = wave & 3, dq = (wave >> 2) * 2;
      float l = 0.f, ov[8];
#pragma unroll
      for (int i = 0; i < 8; ++i) ov[i] = 0.f;
#pragma unroll
      for (int q = 0; q < 4; ++q) {
        const float* src = red + (q * 4 + rb) * 1024;
        l += src[12 * 64 + 32 + r];
#pragma unroll
        for (int i = 0; i < 8; ++i) ov[i] += src[(4 * dq + i) * 64 + lane];
      }
      const int R = rb * 32 + r;
      const int pos = pbase + 2 * (R >> 4), head = R & 15;
      float gA = 0.f;
#pragma unroll
      for (int i = 0; i < 8; ++i) gA = (i == (R >> 4)) ? g8[i] : gA;
      const float sc = gA / l;
#pragma unroll
      for (int g = 0; g < 2; ++g) {
        const int d = 8 * (dq + g) + 4 * h;
        if (d < DH) {
          uint2 w;
          w.x = pack_bf16(ov[4 * g] * sc, ov[4 * g + 1] * sc);
          w.y = pack_bf16(ov[4 * g + 2] * sc, ov[4 * g + 3] * sc);
          *(uint2*)(Ocat + ofrag(pos, head * DH + d)) = w;
        }
      }
      break;
    }
    {
      float m[2] = {-3.0e38f, -3.0e38f};
#pragma nounroll
      for (int t = 0; t < 16; ++t) {
        const uint4 k0 = kvq[t * 256], k1 = kvq[t * 256 + 64];
#pragma unroll
        for (int b = 0; b < 2; ++b) {
          f32x16 s = MFMA32(BF8(k0), q0[b], zero);
          s = MFMA32(BF8(k1), q1[b], s);
#pragma unroll
          for (int e = 0; e < 16; ++e) m[b] = fmaxf(m[b], s[e]);
        }
      }
      m[0] = fmaxf(m[0], __shfl_xor(m[0], 32));
      m[1] = fmaxf(m[1], __shfl_xor(m[1], 32));
      raw_barrier();
      red[(wave * 2) * 64 + lane] = m[0];
      red[(wave * 2 + 1) * 64 + lane] = m[1];
      raw_barrier();
#pragma unroll
      for (int b = 0; b < 2; ++b) {
        float mm = red[(rg * 2 + b) * 64 + lane];
#pragma unroll
        for (int q = 1; q < 4; ++q) mm = fmaxf(mm, red[((q * 2 + rg) * 2 + b) * 64 + lane]);
        mref[b] = mm;
      }
      raw_barrier();
      R0 = kv_load(kvq + ((0 + rot) & 15) * 256); R1 = kv_load(kvq + ((1 + rot) & 15) * 256);
      R2 = kv_load(kvq + ((2 + rot) & 15) * 256); R3 = kv_load(kvq + ((3 + rot) & 15) * 256);
    }
  }
}

extern "C" void kernel_launch(void* const* d_in, const int* in_sizes, int n_in,
                              void* d_out, int out_size, void* d_ws, size_t ws_size,
                              hipStream_t stream) {
  const u16* x = (const u16*)d_in[0];
  const u16* wq = (const u16*)d_in[1];
  const u16* wk = (const u16*)d_in[2];
  const u16* wv = (const u16*)d_in[3];
  const u16* woA = (const u16*)d_in[4];
  const u16* wqkv = (const u16*)d_in[6];
  const u16* woB = (const u16*)d_in[7];
  const u16* gw = (const u16*)d_in[8];
  char* ws = (char*)d_ws;
  u16* Qa = (u16*)(ws);
  u16* KV = (u16*)(ws + (2u << 20));
  u16* QKVb = (u16*)(ws + (3u << 20));
  float* gate = (float*)(ws + (9u << 20));
  u16* Ocat = (u16*)(ws + (10u << 20));
  uint4* Wf = (uint4*)(ws + (14u << 20));
  u16* out = (u16*)d_out;

  k_proj<<<dim3(256), 512, 0, stream>>>(x, wq, wk, wv, wqkv, gw, woA, woB, Qa, KV, QKVb, gate, Wf);
  k_attn<<<dim3(S_LEN / 8), 512, 0, stream>>>(Qa, KV, QKVb, gate, Ocat);
  k_out<<<dim3(224), 512, 0, stream>>>((const uint4*)Ocat, Wf, out);
}
```
